```python
import torch

def _softmax(x, axis=-1):
    return torch.softmax(x, dim=axis)
def _gelu(x):
    return torch.nn.functional.gelu(x, approximate="tanh")
def _silu(x):
    return torch.nn.functional.silu(x)
def _sigmoid(x):
    return torch.sigmoid(x)
def _layernorm(x, axis=-1, eps=1e-5):
    m = x.mean(dim=axis, keepdim=True)
    v = x.var(dim=axis, keepdim=True, unbiased=False)
    return (x - m) / torch.sqrt(v + eps)
def _scatter_add(tgt, idx, src, axis=0):
    return tgt.clone().index_add_(axis, idx, src)
def _segment_sum(src, seg, n):
    out = torch.zeros((n,) + src.shape[1:], dtype=src.dtype)
    return out.index_add_(0, seg, src)
def _pad(x, pw):
    flat = tuple(v for pair in reversed(pw) for v in pair)
    return torch.nn.functional.pad(x, flat)

kernel_name = 'parallel_sum__sliding_block__glu_gated_reglu__prompt'

def setup_inputs():

    _w = (lambda a: (lambda t: t.to(torch.bfloat16) if t.is_floating_point() else t)(torch.as_tensor(a)))
    def _A_setup():
        B, S, D = 1, 2048, 512
        return {
            "x": _w((lambda *s: torch.randn(*s, dtype=torch.bfloat16) * 0.05)(B, S, D)),
            "wqkv": _w((lambda *s: torch.randn(*s, dtype=torch.bfloat16) * 0.05)(D, 3 * D)),
            "wo": _w((lambda *s: torch.randn(*s, dtype=torch.bfloat16) * 0.05)(D, D)),
        }
    _A_in = _A_setup()

    def _B_setup():
        B, S, D, F = 1, 2048, 512, 1376
        return {
            "x": _w((lambda *s: torch.randn(*s, dtype=torch.bfloat16) * 0.05)(B, S, D)),
            "W_gate": _w((lambda *s: torch.randn(*s, dtype=torch.bfloat16) * 0.8)(D, F) * (2.0 / D) ** 0.5),
            "W_up": _w((lambda *s: torch.randn(*s, dtype=torch.bfloat16) * 0.8)(D, F) * (2.0 / D) ** 0.5),
            "W_down": _w((lambda *s: torch.randn(*s, dtype=torch.bfloat16) * 0.8)(F, D) * (1.0 / F) ** 0.5),
        }
    _B_in = _B_setup()

    return {
        "A_x": _A_in["x"],
        "A_wqkv": _A_in["wqkv"],
        "A_wo": _A_in["wo"],
        "B_x": _B_in["x"],
        "B_W_gate": _B_in["W_gate"],
        "B_W_up": _B_in["W_up"],
        "B_W_down": _B_in["W_down"],
    }

def reference(A_x, A_wqkv, A_wo, B_x, B_W_gate, B_W_up, B_W_down):
    def _A_ref(x, wqkv, wo):
        B, S, D = x.shape
        H = 32; Dh = D // H; W = 256
        qkv = (lambda x, *d: x.permute(*d))((x @ wqkv).reshape(B, S, 3, H, Dh), 2, 0, 3, 1, 4)
        q, k, v = qkv[0], qkv[1], qkv[2]
        scores = torch.einsum("bhqd,bhkd->bhqk", q, k) * (Dh ** -0.5)
        idx = torch.arange(S)
        mask = (idx[:, None] - idx[None, :] >= 0) & (idx[:, None] - idx[None, :] < W)
        scores = torch.where(mask[None, None], scores, -1e9)
        p = _softmax(scores, axis=-1)
        o = torch.einsum("bhqk,bhkd->bhqd", p, v)
        return (lambda x, *d: x.permute(*d))(o, 0, 2, 1, 3).reshape(B, S, D) @ wo

    def _B_ref(x, W_gate, W_up, W_down):
        gate = torch.einsum("bsd,df->bsf", x, W_gate)
        up = torch.einsum("bsd,df->bsf", x, W_up)
        gate = gate * (lambda x, dt: x.to(dt))(gate > 0, gate.dtype)
        h = gate * up
        return torch.einsum("bsf,fd->bsd", h, W_down)

    x = A_x
    xn = _layernorm(x, axis=-1)
    return x + _A_ref(xn, A_wqkv, A_wo) + _B_ref(xn, B_W_gate, B_W_up, B_W_down)
```

```cpp
#include <hip/hip_runtime.h>
#include <hip/hip_bf16.h>
typedef unsigned short u16;
__device__ __forceinline__ float b2f(u16 v){ return __uint_as_float(((unsigned)v)<<16); }
__device__ __forceinline__ u16 f2b(float f){ unsigned u=__float_as_uint(f); u += 0x7fffu + ((u>>16)&1u); return (u16)(u>>16); }
constexpr int S=2048, D=512, F=1376, H=32, DH=16, W=256;

__global__ void ln_k(const u16* x, u16* xn){
  int row=blockIdx.x; int t=threadIdx.x;
  float v[8]; float s=0;
  for(int i=0;i<8;i++){ v[i]=b2f(x[row*D+t*8+i]); s+=v[i]; }
  for(int m=32;m>=1;m>>=1) s+=__shfl_xor(s,m);
  float mean=s/D; float q=0;
  for(int i=0;i<8;i++){ float d=v[i]-mean; q+=d*d; }
  for(int m=32;m>=1;m>>=1) q+=__shfl_xor(q,m);
  float r=rsqrtf(q/D+1e-5f);
  for(int i=0;i<8;i++) xn[row*D+t*8+i]=f2b((v[i]-mean)*r);
}
__global__ void gemm_k(const u16* A, const u16* B, u16* C, int M,int N,int K,int ldc){
  int n=blockIdx.x*64+threadIdx.x; int m=blockIdx.y*4+threadIdx.y;
  if(n>=N) return;
  float acc=0;
  for(int k=0;k<K;k++) acc+=b2f(A[m*K+k])*b2f(B[(size_t)k*N+n]);
  C[(size_t)m*ldc+n]=f2b(acc);
}
__global__ void glu_k(const u16* g,const u16* u,u16* h,int n){
  int i=blockIdx.x*256+threadIdx.x; if(i>=n) return;
  float gv=b2f(g[i]); gv=gv>0?gv:0; h[i]=f2b(gv*b2f(u[i]));
}
__global__ void attn_k(const u16* qkv,u16* o){
  int q=blockIdx.x*64+threadIdx.x; int h=blockIdx.y;
  float qv[DH]; for(int d=0;d<DH;d++) qv[d]=b2f(qkv[q*1536+h*16+d]);
  float m=-1e30f,l=0,acc[DH]; for(int d=0;d<DH;d++) acc[d]=0;
  int lo=q-W+1; if(lo<0) lo=0;
  for(int j=lo;j<=q;j++){
    float s=0; for(int d=0;d<DH;d++) s+=qv[d]*b2f(qkv[j*1536+512+h*16+d]);
    s*=0.25f; float mn=fmaxf(m,s); float a=__expf(m-mn); float p=__expf(s-mn);
    l=l*a+p; for(int d=0;d<DH;d++) acc[d]=acc[d]*a+p*b2f(qkv[j*1536+1024+h*16+d]); m=mn;
  }
  for(int d=0;d<DH;d++) o[q*512+h*16+d]=f2b(acc[d]/l);
}
__global__ void final_k(const u16* x,const u16* o,const u16* wo,const u16* h,const u16* wd,u16* out){
  int n=blockIdx.x*64+threadIdx.x; int m=blockIdx.y*4+threadIdx.y;
  float a=0,b=0;
  for(int k=0;k<D;k++) a+=b2f(o[m*D+k])*b2f(wo[k*D+n]);
  for(int k=0;k<F;k++) b+=b2f(h[m*F+k])*b2f(wd[k*D+n]);
  out[m*D+n]=f2b(b2f(x[m*D+n])+a+b);
}
extern "C" void kernel_launch(void* const* d_in, const int* in_sizes, int n_in, void* d_out, int out_size, void* d_ws, size_t ws_size, hipStream_t stream){
  const u16* x=(const u16*)d_in[0]; const u16* wqkv=(const u16*)d_in[1]; const u16* wo=(const u16*)d_in[2];
  const u16* wg=(const u16*)d_in[4]; const u16* wu=(const u16*)d_in[5]; const u16* wd=(const u16*)d_in[6];
  u16* ws=(u16*)d_ws; u16* xn=ws; u16* qkv=xn+S*D; u16* g=qkv+S*1536; u16* u=g+S*F; u16* h=u+S*F; u16* o=h+S*F;
  ln_k<<<S,64,0,stream>>>(x,xn);
  gemm_k<<<dim3(1536/64,S/4),dim3(64,4),0,stream>>>(xn,wqkv,qkv,S,1536,D,1536);
  gemm_k<<<dim3((F+63)/64,S/4),dim3(64,4),0,stream>>>(xn,wg,g,S,F,D,F);
  gemm_k<<<dim3((F+63)/64,S/4),dim3(64,4),0,stream>>>(xn,wu,u,S,F,D,F);
  glu_k<<<(S*F+255)/256,256,0,stream>>>(g,u,h,S*F);
  attn_k<<<dim3(S/64,H),64,0,stream>>>(qkv,o);
  final_k<<<dim3(D/64,S/4),dim3(64,4),0,stream>>>(x,o,wo,h,wd,(u16*)d_out);
}
```

```cpp
#include <hip/hip_runtime.h>
#include <stdint.h>

#define FI __device__ __forceinline__
#define LAS __attribute__((address_space(3)))
#define GAS __attribute__((address_space(1)))
typedef unsigned short u16;
typedef unsigned long long u64;
typedef short bf16x8 __attribute__((ext_vector_type(8)));
typedef short s16x4 __attribute__((ext_vector_type(4)));
typedef float f32x16 __attribute__((ext_vector_type(16)));
typedef float f32x4 __attribute__((ext_vector_type(4)));
typedef float f32x2 __attribute__((ext_vector_type(2)));
typedef unsigned u32x4 __attribute__((ext_vector_type(4)));
typedef __bf16 bf16x2_t __attribute__((ext_vector_type(2)));
typedef LAS const char* lcp;
typedef LAS char* lp;

constexpr int SLOT = 16384, HALFS = 8192;
constexpr int L_KVR = 131072;
constexpr int L_QR = 147456;
constexpr int L_MISC = 155648;
constexpr int L_CS = L_MISC;
constexpr int L_CSW = L_MISC + 2048;
constexpr int L_ZERO = L_MISC + 4096;
constexpr int L_ONES = L_MISC + 6144;
constexpr int L_CNT = L_ONES + 64;
constexpr int L_FINE = L_MISC + 6400;
constexpr int LDS_BYTES = 163840;

constexpr size_t WS_EPOCH = 0, WS_TMO = 64;
constexpr size_t WS_KVF = 4096;
constexpr size_t WS_HF = 8192;
constexpr size_t WS_OF = 12288;
constexpr size_t WS_ST = 65536;
constexpr size_t WS_CS = 131072;
constexpr size_t WS_KV = 1u << 20;
constexpr size_t WS_C = 8u << 20;
constexpr size_t WS_DBG = 32u << 20;
constexpr int NKS = 118;

FI f32x16 mfma(bf16x8 a, bf16x8 b, f32x16 c) { return __builtin_amdgcn_mfma_f32_32x32x16_bf16(a, b, c, 0, 0, 0); }
FI unsigned pk2(float lo, float hi) { f32x2 v = {lo, hi}; bf16x2_t r = __builtin_convertvector(v, bf16x2_t); return __builtin_bit_cast(unsigned, r); }
FI u32x4 pack8(const f32x16& y, int b) { u32x4 r; r[0] = pk2(y[b], y[b + 1]); r[1] = pk2(y[b + 2], y[b + 3]); r[2] = pk2(y[b + 4], y[b + 5]); r[3] = pk2(y[b + 6], y[b + 7]); return r; }
typedef short v4i16_t __attribute__((ext_vector_type(4)));
FI s16x4 trd(lcp p) { return __builtin_bit_cast(s16x4, __builtin_amdgcn_ds_read_tr16_b64_v4i16((LAS v4i16_t*)p)); }
template <int D2> FI bf16x8 trfrag(lcp p) { s16x4 lo = trd(p), hi = trd(p + D2); return __builtin_shufflevector(lo, hi, 0, 1, 2, 3, 4, 5, 6, 7); }
FI bf16x8 ldsf(lcp p) { return *(const LAS bf16x8*)p; }
FI f32x4 ldsf4(lcp p) { return *(const LAS f32x4*)p; }

FI void dma16(unsigned voff, const void* sbase, unsigned dst) {
    asm volatile("s_mov_b32 m0, %2\n\ts_nop 0\n\tglobal_load_lds_dwordx4 %0, %1" :: "v"(voff), "s"(sbase), "s"(dst) : "memory", "m0");
}
FI void dma16_sc1(unsigned voff, const void* sbase, unsigned dst) {
    asm volatile("s_mov_b32 m0, %2\n\ts_nop 0\n\tglobal_load_lds_dwordx4 %0, %1 sc1" :: "v"(voff), "s"(sbase), "s"(dst) : "memory", "m0");
}
FI void aload16(u32x4& v, const void* base, unsigned voff) { asm volatile("global_load_dwordx4 %0, %1, %2" : "=v"(v) : "v"(voff), "s"(base) : "memory"); }
FI void aload16_sc1(u32x4& v, const void* base, unsigned voff) { asm volatile("global_load_dwordx4 %0, %1, %2 sc1" : "=v"(v) : "v"(voff), "s"(base) : "memory"); }
FI void aload_flag(u64& v, const char* base, unsigned voff) { asm volatile("global_load_dwordx2 %0, %1, %2 sc1" : "=v"(v) : "v"(voff), "s"(base) : "memory"); }

#define SB() __builtin_amdgcn_sched_barrier(0)
#define PIN(x) asm volatile("" : "+v"(x))
#define FST() do {} while (0)
#define SYNC(N) do { SB(); FST(); asm volatile("s_waitcnt vmcnt(" #N ") lgkmcnt(0)\n\ts_barrier" ::: "memory"); FST(); SB(); } while (0)
#define SYNCV(N) do { SB(); FST(); asm volatile("s_waitcnt vmcnt(" #N ")\n\ts_barrier" ::: "memory"); FST(); SB(); } while (0)
#define WAITV(N) do { SB(); asm volatile("s_waitcnt vmcnt(" #N ")" ::: "memory"); SB(); } while (0)

#define STAMP(k) do {} while (0)

struct Params {
    const u16* x; const u16* wqkv; const u16* wo; const u16* wg; const u16* wu; const u16* wd; u16* out; char* ws;
};

constexpr int wslot(int s) { return s == 0 ? SLOT : s == 1 ? 3 * SLOT : s == 2 ? 5 * SLOT : s == 3 ? 7 * SLOT : s == 4 ? 0 : s == 5 ? 2 * SLOT : s == 6 ? 4 * SLOT : s == 7 ? 6 * SLOT : ((s - 1) & 7) * SLOT; }
constexpr int xslot(int c) { return c == 0 ? 0 : c == 1 ? 2 * SLOT : c == 2 ? 4 * SLOT : c == 3 ? 6 * SLOT : c == 4 ? L_KVR : c == 5 ? SLOT : c == 6 ? 3 * SLOT : 5 * SLOT; }
constexpr int dloc(int u) {
    return u < 4 ? ((7 + u) & 7) * SLOT + HALFS
         : u < 12 ? ((3 + (u - 4)) & 7) * SLOT
         : u < 16 ? (3 + (u - 12)) * SLOT + HALFS
         : u < 18 ? L_KVR + (u - 16) * HALFS
         : u == 18 ? L_QR
         : dloc(u - 19);
}
constexpr int kvloc(int j) { return (3 + j) * SLOT + HALFS; }

struct Ctx {
    const Params* p;
    unsigned lds0;
    lp lds;
    int lane, tm, g, rb, cg, tb, nb, ft0; bool has3;
    unsigned vq, vf, vx, vlin, vd;
    const char *px, *pq, *pkv, *pwd, *pwo; unsigned offA, offB, ldst16, ldst32;
};

FI void issue_x(const Ctx& c, int chunk, int w) {
    const char* base = c.px + chunk * 128;
    const unsigned dst = c.lds0 + xslot(chunk) + 16 * w * 128;
    dma16(c.vx, base, dst); dma16(c.vx ^ 64u, base + 8 * 1024, dst + 8 * 128);
}
template <int G, int S> FI void issue_w(const Ctx& c) {
    constexpr int so = wslot(S) + G * HALFS;
    if constexpr (G == 0 && S >= 16) {
        const char* base = c.pq + (size_t)(128 * (S - 16)) * 3072;
        const unsigned dst = c.ldst32 + so;
        dma16(c.vq, base, dst); dma16(c.vq, base + 16 * 3072, dst + 1024);
    } else if constexpr (G == 0 && S < 8) {
        const char* base = c.pkv + (size_t)(64 * S) * 3072;
        const unsigned dst = c.ldst16 + so;
        dma16(c.vq, base, dst); dma16(c.vq, base + 1024, dst + 4096);
    } else {
        constexpr int k0 = 64 * (S & 7);
        const unsigned off = (G == 0 ? c.offA : (S < 8 ? c.offB : c.offB + 64)) + k0 * 2752;
        const unsigned dst = c.ldst16 + so;
        dma16(c.vf + off, c.p->wg, dst); dma16(c.vf + off, c.p->wu, dst + 4096);
    }
}
FI void issue_d(const Ctx& c, int u, int loc) {
    const char* base;
    if (u < 21) base = c.pwd + (size_t)u * 65536;
    else if (u == 21) base = c.pwd + (size_t)21 * 65536 - (c.tm >= 2 ? 32768 : 0);
    else base = c.pwo + (size_t)(u - 22) * 65536;
    const unsigned dst = c.lds0 + loc + c.tm * 2048;
    dma16(c.vd, base, dst); dma16(c.vd, base + 8 * 1024, dst + 1024);
}

FI float blo(unsigned d) { return __uint_as_float(d << 16); }
FI float bhi(unsigned d) { return __uint_as_float(d & 0xffff0000u); }
template <int CTRL> FI float dppadd(float x) { return x + __builtin_bit_cast(float, __builtin_amdgcn_update_dpp(0, __builtin_bit_cast(int, x), CTRL, 0xf, 0xf, true)); }
FI void swap32add(float& u, float v) { auto rr = __builtin_amdgcn_permlane32_swap(__float_as_uint(u), __float_as_uint(v), false, false); u = __uint_as_float(rr[0]) + __uint_as_float(rr[1]); }
FI void swap16add(float& u, float v) { auto rr = __builtin_amdgcn_permlane16_swap(__float_as_uint(u), __float_as_uint(v), false, false); u = __uint_as_float(rr[0]) + __uint_as_float(rr[1]); }

struct G1State {
    bf16x8 xf[32];
    bf16x8 wq[4];
    f32x16 a0, a1;
    unsigned wb[4];
    unsigned xb[4];
};
template <bool PAIR> FI bf16x8 rdfrag(const G1State& st, int so, int f) {
    if (PAIR) return trfrag<256>((lcp)(uintptr_t)(st.wb[f >> 1] + so + (f & 1) * 4096));
    else return trfrag<256>((lcp)(uintptr_t)(st.wb[f & 3] + so + (f >> 2) * 4096));
}
template <int C> FI void rdx(G1State& st) {
#pragma unroll
    for (int e = 0; e < 4; ++e) st.xf[4 * C + e] = ldsf((lcp)(uintptr_t)(st.xb[e] + xslot(C)));
}

template <int G, int S, int MODE, int ST, int XC, bool XFRESH, bool XNEXT, bool FRESH, int NEXT, class F>
FI void stage(G1State& st, F&& hook) {
    constexpr int so = wslot(S) + G * HALFS, sn = wslot(S + 1) + G * HALFS;
    constexpr bool PAIR = MODE >= 2;
    if constexpr (FRESH) {
#pragma unroll
        for (int f = 0; f < 4; ++f) st.wq[f] = rdfrag<PAIR>(st, so, f);
    }
    if constexpr (XFRESH) rdx<XC>(st);
    if constexpr (FRESH) hook(-1);
    SB();
#pragma unroll
    for (int f = 0; f < 8; ++f) {
        const int pos = PAIR ? 4 * ST + (f >> 1) : 8 * ST + f;
        const bf16x8 w = st.wq[f & 3];
        if (MODE == 0) st.a0 = mfma(w, st.xf[pos], st.a0);
        else if ((f & 1) == 0) st.a0 = mfma(w, st.xf[pos], st.a0);
        else if (MODE == 2) st.a1 = mfma(w, st.xf[pos], st.a1);
        else st.a1 = mfma(st.xf[pos], w, st.a1);
        SB();
        if (f < 4) st.wq[f & 3] = rdfrag<PAIR>(st, so, f + 4);
        else if (NEXT >= 0) st.wq[f & 3] = rdfrag<(NEXT == 1)>(st, sn, f - 4);
        if constexpr (XNEXT) { if (f == 4) rdx<(XC + 1) & 7>(st); }
        hook(f);
        SB();
    }
}

FI u64 mix64(u64 z) { z += 0x9E3779B97F4A7C15ull; z = (z ^ (z >> 30)) * 0xBF58476D1CE4E5B9ull; z = (z ^ (z >> 27)) * 0x94D049BB133111EBull; return z ^ (z >> 31); }

constexpr unsigned SPIN_CAP = 1u << 18;
FI void set_timeout(char* ws, int lane) { if (lane == 0) __hip_atomic_store((GAS unsigned*)(ws + WS_TMO), 1u, __ATOMIC_RELAXED, __HIP_MEMORY_SCOPE_AGENT); }
FI void poll_flags(const u64* flags, int n, u64 tag, int lane, char* ws) {
    const GAS u64* pp = (const GAS u64*)flags + (lane < n ? lane : 0);
    for (unsigned spins = 0;; ++spins) {
        const u64 v = __hip_atomic_load(pp, __ATOMIC_RELAXED, __HIP_MEMORY_SCOPE_AGENT);
        if (__all(v == tag)) return;
        if (spins > SPIN_CAP) { set_timeout(ws, lane); return; }
        __builtin_amdgcn_s_sleep(2);
    }
}
FI void store_flag(char* ws, size_t off, u64 tag) { __hip_atomic_store((GAS u64*)(ws + off), tag, __ATOMIC_RELAXED, __HIP_MEMORY_SCOPE_AGENT); }

__global__ __launch_bounds__(512, 2) void fused_kernel(Params P) {
    extern __shared__ __attribute__((aligned(16))) char smem[];
    Ctx c; c.p = &P;
    const int tid = threadIdx.x; c.lane = tid & 63;
    const int w = __builtin_amdgcn_readfirstlane(tid >> 6);
    c.tm = w & 3; c.g = w >> 2;
    c.lds = (lp)smem; c.lds0 = (unsigned)(uintptr_t)c.lds;
    const int xcd = blockIdx.x & 7, idx = blockIdx.x >> 3;
    c.rb = 4 * (xcd >> 1) + (idx >> 3); c.cg = 8 * (xcd & 1) + (idx & 7);
    c.tb = 4 * xcd + (idx >> 3); c.nb = idx & 7;
    c.has3 = c.cg < 11; c.ft0 = c.has3 ? 3 * c.cg : 33 + 2 * (c.cg - 11);
    const int ftA = c.has3 ? c.ft0 + 2 : c.ft0;
    const int lane = c.lane, h = lane >> 5, r32 = lane & 31;
    c.vq = (lane >> 2) * 3072 + (lane & 3) * 16; c.vf = (lane >> 2) * 2752 + (lane & 3) * 16;
    c.vx = (lane >> 3) * 1024 + (((lane & 7) ^ (lane >> 4)) * 16); c.vlin = lane * 16;
    char* ws = P.ws;
    STAMP(0);
    u64 ep; aload_flag(ep, ws + WS_EPOCH, 0u);
    u32x4 ax, aw0, aw1, aw2, aw3;
    aload16(ax, (const char*)P.x + (size_t)(128 * c.rb + 8 * c.cg + w) * 1024, c.vlin);
    int ptile = 2 * w + (c.rb & 1); if (ptile > 8) ptile = c.rb & 1;
    {
        const char* tb; unsigned pitch, vo;
        if (ptile < 3) { tb = (const char*)P.wqkv + (ptile == 0 ? 512 : ptile == 1 ? 1024 : 0) * 2 + 64 * c.cg; pitch = 3072; vo = c.vq; }
        else { const int ft = ptile < 5 ? c.ft0 : ptile < 7 ? c.ft0 + 1 : ftA; tb = (const char*)((ptile & 1) ? P.wg : P.wu) + 64 * ft; pitch = 2752; vo = c.vf; }
        tb += (size_t)(64 * (c.rb >> 1)) * pitch;
        aload16(aw0, tb, vo); aload16(aw1, tb + 16 * pitch, vo); aload16(aw2, tb + 32 * pitch, vo); aload16(aw3, tb + 48 * pitch, vo);
    }
    c.vd = (lane >> 3) * 1024 + (((lane & 7) ^ (((lane >> 4) & 1) << 2)) * 16);
    c.px = (const char*)P.x + (size_t)(128 * c.rb + 16 * w) * 1024;
    c.pq = (const char*)P.wqkv + ((size_t)(32 * c.tm) * 1536 + 32 * c.cg) * 2;
    c.pkv = (const char*)P.wqkv + ((size_t)(16 * c.tm) * 1536 + 512 + 32 * c.cg) * 2;
    c.pwd = (const char*)P.wd + (size_t)(16 * c.tm) * 1024 + c.nb * 128;
    c.pwo = (const char*)P.wo + (size_t)(16 * c.tm) * 1024 + c.nb * 128;
    c.offB = (unsigned)((16 * c.tm * 1376 + 32 * c.ft0) * 2);
    c.offA = (unsigned)((16 * c.tm * 1376 + 32 * ftA) * 2);
    c.ldst16 = c.lds0 + 16 * c.tm * 64; c.ldst32 = c.lds0 + 32 * c.tm * 64;
    issue_x(c, 0, w); if (c.g == 0) issue_w<0, 0>(c); else issue_w<1, 0>(c);
    issue_x(c, 1, w); if (c.g == 0) issue_w<0, 1>(c); else issue_w<1, 1>(c);
    issue_x(c, 2, w); if (c.g == 0) issue_w<0, 2>(c); else issue_w<1, 2>(c);
    issue_x(c, 3, w); if (c.g == 0) issue_w<0, 3>(c); else issue_w<1, 3>(c);
    issue_x(c, 4, w);
    const __amdgpu_buffer_rsrc_t wsr = __builtin_amdgcn_make_buffer_rsrc(ws, 0, 32u << 20, 0x00020000);
    *(LAS unsigned*)(c.lds + L_ZERO + tid * 4) = 0u;
    if (tid < 4) { *(LAS unsigned*)(c.lds + L_ONES + tid * 4) = 0x3f803f80u; *(LAS unsigned*)(c.lds + L_ONES + 1024 + tid * 4) = 0x3f803f80u; }
    if (tid >= 64 && tid < 72) *(LAS unsigned*)(c.lds + L_CNT + (tid - 64) * 4) = 0u;

    G1State st;
    const int q4 = (lane & 15) >> 2, p4 = lane & 3, b16 = (lane >> 4) & 1;
    { const unsigned lb = c.lds0 + (8 * h + q4) * 64 + 32 * b16 + 8 * p4;
#pragma unroll
      for (int j = 0; j < 4; ++j) st.wb[j] = lb + ((j ^ c.tm) * 1024); }
    { const int row = 32 * c.tm + r32;
#pragma unroll
      for (int j = 0; j < 4; ++j) st.xb[j] = c.lds0 + row * 128 + (((2 * (j ^ c.tm) + h) ^ ((row >> 1) & 7)) * 16); }
    st.a0 = f32x16{}; st.a1 = f32x16{};

    WAITV(18);
    asm volatile("" : "+v"(ep), "+v"(ax), "+v"(aw0), "+v"(aw1), "+v"(aw2), "+v"(aw3));
    const u64 tag = mix64(((u64)(unsigned)__builtin_amdgcn_readfirstlane((int)(unsigned)(ep >> 32)) << 32) | (unsigned)__builtin_amdgcn_readfirstlane((int)(unsigned)ep));
    const unsigned tagLo = (unsigned)tag, tagHi = (unsigned)(tag >> 32);
    {
        float s = 0.f, q = 0.f;
#pragma unroll
        for (int i = 0; i < 4; ++i) { const float a = blo(ax[i]), b = bhi(ax[i]); s += a + b; q = __builtin_fmaf(a, a, q); q = __builtin_fmaf(b, b, q); }
        s = dppadd<0x121>(s); q = dppadd<0x121>(q); s = dppadd<0x122>(s); q = dppadd<0x122>(q); s = dppadd<0x124>(s); q = dppadd<0x124>(q); s = dppadd<0x128>(s); q = dppadd<0x128>(q);
        swap16add(s, s); swap16add(q, q); swap32add(s, s); swap32add(q, q);
        const float mean = s * (1.f / 512.f), var = q * (1.f / 512.f) - mean * mean, rstd = __builtin_amdgcn_rsqf(fmaxf(var, 0.f) + 1e-5f);
        if (lane == 0) __builtin_amdgcn_raw_buffer_store_b128((u32x4){__float_as_uint(mean), tagLo, __float_as_uint(rstd), tagHi}, wsr, (unsigned)(WS_ST + (128 * c.rb + 8 * c.cg + w) * 16), 0, 16);
        float v[8];
#pragma unroll
        for (int i = 0; i < 4; ++i) { v[2 * i] = (blo(aw0[i]) + blo(aw1[i])) + (blo(aw2[i]) + blo(aw3[i])); v[2 * i + 1] = (bhi(aw0[i]) + bhi(aw1[i])) + (bhi(aw2[i]) + bhi(aw3[i])); }
        swap32add(v[0], v[4]); swap32add(v[1], v[5]); swap32add(v[2], v[6]); swap32add(v[3], v[7]);
        swap16add(v[0], v[2]); swap16add(v[1], v[3]);
        v[0] = dppadd<0x124>(v[0]); v[0] = dppadd<0x128>(v[0]); v[1] = dppadd<0x124>(v[1]); v[1] = dppadd<0x128>(v[1]);
        const int rho = lane >> 4, col = 8 * (lane & 3) + 4 * (rho >> 1) + 2 * (rho & 1);
        if ((lane & 12) == 0) __builtin_amdgcn_raw_buffer_store_b128((u32x4){__float_as_uint(v[0]), tagLo, __float_as_uint(v[1]), tagLo}, wsr,
                                                                      (unsigned)(WS_CS + (((c.cg * 9 + ptile) * 8 + (c.rb >> 1)) * 32 + col) * 8), 0, 16);
    }
    SYNC(16);
    STAMP(1);
    const int T = 4 * c.rb + c.tm;
    float ln_a = 0.f, ln_m = 0.f;

    u32x4 gs, g0a, g0b, g1a, g1b;
    const int gt0 = w == 0 ? 0 : w == 1 ? 1 : w == 2 ? 2 : w == 3 ? 7 : w - 1, gt1 = w == 3 ? 8 : gt0;
    const unsigned gso = (unsigned)((128 * c.rb + 32 * c.tm + r32) * 16);
    const unsigned gco = (unsigned)((lane >> 3) * 256 + (lane & 7) * 32);
    const char* gb = ws + WS_CS + (size_t)c.cg * (9 * 2048); const unsigned go0 = gco + gt0 * 2048, go1 = gco + gt1 * 2048;
#define GATHER_ISSUE() do { aload16_sc1(gs, ws + WS_ST, gso); aload16_sc1(g0a, gb, go0); aload16_sc1(g0b, gb + 16, go0); aload16_sc1(g1a, gb, go1); aload16_sc1(g1b, gb + 16, go1); } while (0)
#define GATHER_OK() (gs[1] == tagLo && gs[3] == tagHi && g0a[1] == tagLo && g0a[3] == tagLo && g0b[1] == tagLo && g0b[3] == tagLo && g1a[1] == tagLo && g1a[3] == tagLo && g1b[1] == tagLo && g1b[3] == tagLo)
#define GATHER_RED(A, B, tile) do { float v0 = __uint_as_float(A[0]), v1 = __uint_as_float(A[2]), v2 = __uint_as_float(B[0]), v3 = __uint_as_float(B[2]); \
        swap32add(v0, v2); swap32add(v1, v3); swap16add(v0, v1); v0 = dppadd<0x128>(v0); \
        if ((lane & 8) == 0) *(LAS float*)(c.lds + L_CS + (tile) * 128 + (4 * (lane & 7) + 2 * (lane >> 5) + ((lane >> 4) & 1)) * 4) = v0; } while (0)
#define GATHER_FINISH() do { WAITV(10); asm volatile("" : "+v"(gs), "+v"(g0a), "+v"(g0b), "+v"(g1a), "+v"(g1b)); \
        if (!__all(GATHER_OK())) { const unsigned o0_ = (unsigned)(WS_CS + (c.cg * 9 + gt0) * 2048) + gco, o1_ = (unsigned)(WS_CS + (c.cg * 9 + gt1) * 2048) + gco; \
            for (unsigned spins = 0;; ++spins) { __builtin_amdgcn_s_sleep(2); \
                gs = __builtin_amdgcn_raw_buffer_load_b128(wsr, (unsigned)WS_ST + gso, 0, 16); \
                g0a = __builtin_amdgcn_raw_buffer_load_b128(wsr, o0_, 0, 16); g0b = __builtin_amdgcn_raw_buffer_load_b128(wsr, o0_ + 16, 0, 16); \
                g1a = __builtin_amdgcn_raw_buffer_load_b128(wsr, o1_, 0, 16); g1b = __builtin_amdgcn_raw_buffer_load_b128(wsr, o1_ + 16, 0, 16); \
                if (__all(GATHER_OK())) break; if (spins > SPIN_CAP) { set_timeout(ws, lane); break; } } } \
        ln_m = __uint_as_float(gs[0]); ln_a = __uint_as_float(gs[2]); GATHER_RED(g0a, g0b, gt0); GATHER_RED(g1a, g1b, gt1); } while (0)

#define RD_CS(dst, tile) do { _Pragma("unroll") for (int q_ = 0; q_ < 4; ++q_) { const f32x4 t_ = ldsf4((lcp)c.lds + L_CS + (tile) * 128 + (8 * q_ + 4 * h) * 4); \
            dst[4 * q_] = t_[0]; dst[4 * q_ + 1] = t_[1]; dst[4 * q_ + 2] = t_[2]; dst[4 * q_ + 3] = t_[3]; } } while (0)
#define SPREAD(dst, v, row) do { if (h == 0) *(LAS float*)(c.lds + L_CSW + c.tm * 256 + (row) * 128 + r32 * 4) = (v); \
        _Pragma("unroll") for (int q_ = 0; q_ < 4; ++q_) { const f32x4 t_ = ldsf4((lcp)c.lds + L_CSW + c.tm * 256 + (row) * 128 + (8 * q_ + 4 * h) * 4); \
            dst[4 * q_] = t_[0]; dst[4 * q_ + 1] = t_[1]; dst[4 * q_ + 2] = t_[2]; dst[4 * q_ + 3] = t_[3]; } } while (0)
#define EPI_PAIR(ft, tg, valid) do { f32x16 cg_, cu_; RD_CS(cg_, tg); RD_CS(cu_, (tg) + 1); \
        f32x16 y; const float a2 = ln_a * ln_a; \
        _Pragma("unroll") for (int r_ = 0; r_ < 16; ++r_) { const float g_ = fmaxf(__builtin_fmaf(-ln_m, cg_[r_], st.a0[r_]), 0.f), u_ = __builtin_fmaf(-ln_m, cu_[r_], st.a1[r_]); y[r_] = (g_ * a2) * u_; } \
        if (valid) { const unsigned o_ = (unsigned)(WS_C + ((size_t)T * NKS + 2 * (ft)) * 1024) + lane * 16; \
            __builtin_amdgcn_raw_buffer_store_b128(pack8(y, 0), wsr, o_, 0, 16); __builtin_amdgcn_raw_buffer_store_b128(pack8(y, 8), wsr, o_ + 1024, 0, 16); } \
        st.a0 = f32x16{}; st.a1 = f32x16{}; } while (0)
#define H1(...) [&](int f_) { if (f_ == 1) { __VA_ARGS__ } }
#define HF(...) [&](int f_) { if (f_ == -1) { __VA_ARGS__ } }

    const int th = w & 1, kq = w >> 1;
    const int T2 = 2 * c.tb + th, rb2 = c.tb >> 1;
    if (c.g == 0) {
        stage<0, 0, 3, 0, 0, true, false, true, -1>(st, HF(;)); SYNCV(12);
        stage<0, 1, 3, 1, 1, true, false, true, -1>(st, HF(issue_w<0, 4>(c); issue_x(c, 5, w);)); SYNCV(12);
        stage<0, 2, 3, 2, 2, true, false, true, -1>(st, HF(issue_w<0, 5>(c); issue_x(c, 6, w);)); SYNCV(12);
        stage<0, 3, 3, 3, 3, true, false, true, -1>(st, HF(issue_w<0, 6>(c); issue_x(c, 7, w);)); SYNCV(6);
        stage<0, 4, 3, 4, 4, true, true, true, 1>(st, [&](int f_) { if (f_ == -1) { issue_w<0, 7>(c); issue_w<0, 8>(c); } if (f_ == 3) GATHER_ISSUE(); }); SYNCV(11);
        stage<0, 5, 3, 5, 5, false, true, false, 1>(st, H1(issue_w<0, 9>(c);)); SYNCV(9);
        stage<0, 6, 3, 6, 6, false, true, false, 1>(st, H1(issue_w<0, 10>(c); issue_w<0, 11>(c);)); SYNCV(11);
        stage<0, 7, 3, 7, 7, false, false, false, 1>(st, [&](int f_) { if (f_ == 1) { issue_w<0, 12>(c); issue_w<0, 13>(c); } if (f_ == 4) GATHER_FINISH(); }); SYNC(8);
        STAMP(2);
        { f32x16 ck; RD_CS(ck, 0);
          f32x16 y;
#pragma unroll
          for (int r = 0; r < 16; ++r) y[r] = ln_a * __builtin_fmaf(-ln_m, ck[r], st.a0[r]);
          const u32x4 k0 = pack8(y, 0), k1 = pack8(y, 8);
          *(LAS u32x4*)(c.lds + L_KVR + c.tm * 4096 + lane * 16) = k0; *(LAS u32x4*)(c.lds + L_KVR + c.tm * 4096 + 1024 + lane * 16) = k1;
          const unsigned o = (unsigned)(WS_KV + ((size_t)c.cg * 64 + T) * 4096) + lane * 16;
          __builtin_amdgcn_raw_buffer_store_b128(k0, wsr, o, 0, 16); __builtin_amdgcn_raw_buffer_store_b128(k1, wsr, o + 1024, 0, 16);
          const float csv = *(const LAS float*)(c.lds + L_CS + 128 + r32 * 4);
          f32x16 aa, mm; SPREAD(aa, ln_a, 0); SPREAD(mm, ln_m, 1);
#pragma unroll
          for (int r = 0; r < 16; ++r) y[r] = aa[r] * __builtin_fmaf(-mm[r], csv, st.a1[r]);
          const u32x4 v0 = pack8(y, 0), v1 = pack8(y, 8);
          *(LAS u32x4*)(c.lds + L_KVR + c.tm * 4096 + 2048 + lane * 16) = v0; *(LAS u32x4*)(c.lds + L_KVR + c.tm * 4096 + 3072 + lane * 16) = v1;
          __builtin_amdgcn_raw_buffer_store_b128(v0, wsr, o + 2048, 0, 16); __builtin_amdgcn_raw_buffer_store_b128(v1, wsr, o + 3072, 0, 16);
          st.a0 = f32x16{}; st.a1 = f32x16{}; }
        stage<0, 8, 2, 0, -1, false, false, false, 1>(st, H1(issue_w<0, 14>(c); issue_w<0, 15>(c);)); SYNCV(14);
        stage<0, 9, 2, 1, -1, false, false, false, 1>(st, H1(issue_w<0, 16>(c);)); SYNCV(14);
        stage<0, 10, 2, 2, -1, false, false, false, 1>(st, H1(issue_w<0, 17>(c);)); SYNCV(8);
        if (w == 0 && lane == 0) store_flag(ws, WS_KVF + ((size_t)c.cg * 16 + c.rb) * 8, tag);
        stage<0, 11, 2, 3, -1, false, false, false, 1>(st, H1(issue_w<0, 18>(c);)); SYNCV(10);
        STAMP(3);
        stage<0, 12, 2, 4, -1, false, false, false, 1>(st, H1(issue_w<0, 19>(c);)); SYNCV(10);
        stage<0, 13, 2, 5, -1, false, false, false, 1>(st, H1(issue_d(c, 4, dloc(4));)); SYNCV(10);
        stage<0, 14, 2, 6, -1, false, false, false, 1>(st, H1(issue_d(c, 5, dloc(5));)); SYNCV(10);
        stage<0, 15, 2, 7, -1, false, false, false, 0>(st, H1(issue_d(c, 6, dloc(6));)); SYNCV(10);
        STAMP(4);
        EPI_PAIR(ftA, 7, c.has3);
        stage<0, 16, 0, 0, -1, false, false, false, 0>(st, H1(issue_d(c, 7, dloc(7));)); SYNCV(10);
        stage<0, 17, 0, 1, -1, false, false, false, 0>(st, H1(issue_d(c, 8, dloc(8));)); SYNCV(4);
        if (w == 0 && lane == 0) store_flag(ws, WS_HF + ((size_t)c.rb * 16 + c.cg) * 8, tag);
        stage<0, 18, 0, 2, -1, false, false, false, 0>(st, H1(issue_d(c, 9, dloc(9));)); SYNCV(10);
        stage<0, 19, 0, 3, -1, false, false, false, -1>(st, H1(issue_d(c, 10, dloc(10));));
        { f32x16 cq; RD_CS(cq, 2);
          f32x16 y; const float sc = ln_a * (0.25f * 1.4426950408889634f);
#pragma unroll
          for (int r = 0; r < 16; ++r) y[r] = sc * __builtin_fmaf(-ln_m, cq[r], st.a0[r]);
          *(LAS u32x4*)(c.lds + L_QR + c.tm * 2048 + lane * 16) = pack8(y, 0); *(LAS u32x4*)(c.lds + L_QR + c.tm * 2048 + 1024 + lane * 16) = pack8(y, 8); }
        SYNC(10);
        issue_d(c, 11, dloc(11));
        STAMP(5);
    } else {
        stage<1, 0, 2, 0, 0, true, false, true, -1>(st, HF(;)); SYNCV(12);
        stage<1, 1, 2, 1, 1, true, false, true, -1>(st, HF(issue_w<1, 4>(c); issue_x(c, 5, w);)); SYNCV(12);
        stage<1, 2, 2, 2, 2, true, false, true, -1>(st, HF(issue_w<1, 5>(c); issue_x(c, 6, w);)); SYNCV(12);
        stage<1, 3, 2, 3, 3, true, false, true, -1>(st, HF(issue_w<1, 6>(c); issue_x(c, 7, w);)); SYNCV(6);
        stage<1, 4, 2, 4, 4, true, true, true, 1>(st, [&](int f_) { if (f_ == -1) { issue_w<1, 7>(c); issue_w<1, 8>(c); } if (f_ == 3) GATHER_ISSUE(); }); SYNCV(11);
        stage<1, 5, 2, 5, 5, false, true, false, 1>(st, H1(issue_w<1, 9>(c);)); SYNCV(9);
        stage<1, 6, 2, 6, 6, false, true, false, 1>(st, H1(issue_w<1, 10>(c); issue_w<1, 11>(c);)); SYNCV(11);
        stage<1, 7, 2, 7, 7, false, false, false, 1>(st, [&](int f_) { if (f_ == 1) { issue_w<1, 12>(c); issue_w<1, 13>(c); } if (f_ == 4) GATHER_FINISH(); }); SYNC(8);
        EPI_PAIR(c.ft0, 3, true);
        stage<1, 8, 2, 0, -1, false, false, false, 1>(st, H1(issue_w<1, 14>(c); issue_w<1, 15>(c);)); SYNCV(12);
        stage<1, 9, 2, 1, -1, false, false, false, 1>(st, H1(issue_d(c, 0, dloc(0));)); SYNCV(12);
        stage<1, 10, 2, 2, -1, false, false, false, 1>(st, H1(issue_d(c, 1, dloc(1));)); SYNCV(12);
        stage<1, 11, 2, 3, -1, false, false, false, 1>(st, H1(issue_d(c, 2, dloc(2));)); SYNCV(12);
        stage<1, 12, 2, 4, -1, false, false, false, 1>(st, H1(issue_d(c, 3, dloc(3));)); SYNCV(10);
        stage<1, 13, 2, 5, -1, false, false, false, 1>(st, H1(;)); SYNCV(8);
        u64 kf_ = 0;
        aload_flag(kf_, ws + WS_KVF + (size_t)c.cg * 128, (unsigned)(((c.rb >= 2 ? c.rb - 2 : 0) + (lane & 1)) * 8));
        stage<1, 14, 2, 6, -1, false, false, false, 1>(st, H1(;)); SYNCV(8);
        stage<1, 15, 2, 7, -1, false, false, false, -1>(st, H1(;)); SYNCV(0);
        EPI_PAIR(c.ft0 + 1, 5, true);
        if (c.rb >= 1) {
            asm volatile("" : "+v"(kf_));
            const bool need = c.rb >= 2 || (lane & 1) == 0;
            if (!__all(!need || kf_ == tag)) poll_flags((const u64*)(ws + WS_KVF) + c.cg * 16 + (c.rb >= 2 ? c.rb - 2 : 0), c.rb >= 2 ? 2 : 1, tag, lane, ws);
#pragma unroll
            for (int j = 0; j < 4; ++j) {
                if (c.rb >= 2 || j >= 2) {
                    const char* base = ws + WS_KV + ((size_t)c.cg * 64 + (4 * c.rb - 8 + 2 * j)) * 4096 + c.tm * 2048;
                    const unsigned dst = c.lds0 + kvloc(j) + c.tm * 2048;
                    dma16_sc1(c.vlin, base, dst); dma16_sc1(c.vlin, base + 1024, dst + 1024);
                }
            }
        }
        SYNCV(63);
        if (c.rb >= 2) { SYNCV(8); } else { SYNCV(0); }
        SYNCV(63); SYNC(0);
    }
    bf16x8 hf[22];
    const unsigned cbase = (unsigned)(WS_C + ((size_t)T2 * NKS + kq) * 1024) + lane * 16;
    {
        u64 hfl; aload_flag(hfl, ws + WS_HF + (size_t)rb2 * 128, (unsigned)((lane & 15) * 8));
        const bf16x8 qf = ldsf((lcp)c.lds + L_QR + c.tm * 2048 + c.g * 1024 + lane * 16);
        const int i0 = T >= 8 ? 0 : 8 - T;
        auto kvaddr = [&](int i) -> unsigned { const int ti = c.tm + i; return ti < 8 ? (unsigned)((3 + (ti >> 1)) * SLOT + HALFS + (ti & 1) * 4096) : (unsigned)(L_KVR + (ti - 8) * 4096); };
        f32x16 mk8, mk0;
#pragma unroll
        for (int r = 0; r < 16; ++r) { const int kl = (r & 3) + 8 * (r >> 2) + 4 * h; mk8[r] = kl <= r32 ? 0.f : -1e30f; mk0[r] = kl > r32 ? 0.f : -1e30f; }
        const f32x16 zero = f32x16{};
        const unsigned klane = c.lds0 + c.g * 1024 + lane * 16;
        const unsigned lanebit = r32 < 16 ? 1u : 0u;
        const unsigned vb = c.lds0 + (r32 < 16 ? (unsigned)(((r32 & 15) + 16 * c.g + 32 * h) * 16) : (unsigned)L_ONES);
        const int fb = T == 0 ? 8 : 7;
        unsigned ka[9];
#pragma unroll
        for (int i = 0; i < 9; ++i) ka[i] = kvaddr(i >= i0 ? i : fb);
        float m0 = -1e30f, m1 = -1e30f;
        if (T == 0) {
            const f32x16 s = mfma(ldsf((lcp)(uintptr_t)(klane + ka[8])), qf, mk8);
#pragma unroll
            for (int r = 0; r < 16; r += 2) m0 = fmaxf(fmaxf(m0, s[r]), s[r + 1]);
        } else {
#pragma unroll
            for (int i = 0; i < 9; ++i) {
                const f32x16 s = mfma(ldsf((lcp)(uintptr_t)(klane + ka[i])), qf, i == 8 ? mk8 : (i == 0 ? mk0 : zero));
#pragma unroll
                for (int r = 0; r < 16; r += 4) { m0 = fmaxf(fmaxf(m0, s[r]), s[r + 1]); m1 = fmaxf(fmaxf(m1, s[r + 2]), s[r + 3]); }
            }
        }
        float m = fmaxf(m0, m1);
        { auto rr = __builtin_amdgcn_permlane32_swap(__float_as_uint(m), __float_as_uint(m), false, false); m = fmaxf(__uint_as_float(rr[0]), __uint_as_float(rr[1])); }
        STAMP(6);
        WAITV(0); asm volatile("" : "+v"(hfl));
        if (!__all(hfl == tag)) poll_flags((const u64*)(ws + WS_HF) + rb2 * 16, 16, tag, lane, ws);
#pragma unroll
        for (int i = 0; i < 22; ++i) { const bool ok = (i < 21) || (kq < 2);
            hf[i] = __builtin_bit_cast(bf16x8, __builtin_amdgcn_raw_buffer_load_b128(wsr, ok ? cbase + i * 4096 : cbase, 0, 16)); }
        STAMP(7);
        f32x16 cm, c8, c0;
#pragma unroll
        for (int r = 0; r < 16; ++r) { cm[r] = -m; c8[r] = mk8[r] - m; c0[r] = mk0[r] - m; }
        f32x16 o = f32x16{};
        if (T == 0) {
            const unsigned va = lanebit * (ka[8] + 2048) + vb;
            f32x16 s = mfma(ldsf((lcp)(uintptr_t)(klane + ka[8])), qf, c8);
#pragma unroll
            for (int r = 0; r < 16; ++r) s[r] = __builtin_amdgcn_exp2f(s[r]);
            o = mfma(ldsf((lcp)(uintptr_t)va), __builtin_bit_cast(bf16x8, pack8(s, 0)), o);
            o = mfma(ldsf((lcp)(uintptr_t)(va + 1024)), __builtin_bit_cast(bf16x8, pack8(s, 8)), o);
        } else {
#pragma unroll
            for (int i = 0; i < 9; ++i) {
                const bool valid = i >= i0;
                const unsigned d = valid ? 0u : (unsigned)(L_ZERO - L_ONES), kav = valid ? ka[i] + 2048 : (unsigned)L_ZERO;
                const unsigned va = lanebit * (kav - d) + (vb + d);
                const bf16x8 kf = ldsf((lcp)(uintptr_t)(klane + ka[i]));
                const bf16x8 v0 = ldsf((lcp)(uintptr_t)va), v1 = ldsf((lcp)(uintptr_t)(va + 1024));
                f32x16 s = mfma(kf, qf, i == 8 ? c8 : (i == 0 ? c0 : cm));
#pragma unroll
                for (int r = 0; r < 16; ++r) s[r] = __builtin_amdgcn_exp2f(s[r]);
                o = mfma(v0, __builtin_bit_cast(bf16x8, pack8(s, 0)), o);
                o = mfma(v1, __builtin_bit_cast(bf16x8, pack8(s, 8)), o);
            }
        }
        const float inv = __builtin_amdgcn_rcpf(o[8]);
        f32x16 y;
#pragma unroll
        for (int r = 0; r < 8; ++r) y[r] = o[r] * inv;
        const unsigned oo = (unsigned)(WS_C + ((size_t)T * NKS + 86 + 2 * c.cg + c.g) * 1024) + lane * 16;
        __builtin_amdgcn_raw_buffer_store_b128(pack8(y, 0), wsr, oo, 0, 16);
    }
    STAMP(8);
    {
        const int tok = 64 * c.tb + 32 * th + r32;
        f32x16 acc0 = f32x16{}, acc1 = f32x16{};
        const unsigned wl = c.lds0 + kq * 2048 + (4 * h + q4) * 128 + 32 * b16 + 8 * p4;
        const unsigned swz = ((q4 >> 1) & 1) << 6;
        const unsigned wl0 = wl + (0u ^ swz), wl1 = wl + (64u ^ swz);
        bf16x8 wa[3], wb_[3];
#define RDW(u) do { wa[(u) % 3] = trfrag<1024>((lcp)(uintptr_t)(wl0 + dloc(u))); wb_[(u) % 3] = trfrag<1024>((lcp)(uintptr_t)(wl1 + dloc(u))); } while (0)
#define G2STEP(u, frag, nxt) do { acc0 = mfma(wa[(u) % 3], frag, acc0); acc1 = mfma(wb_[(u) % 3], frag, acc1); SB(); if ((nxt) >= 0) RDW((nxt) < 0 ? 0 : (nxt)); SB(); } while (0)
        RDW(0); RDW(1); RDW(2);
        SYNC(63);
#pragma unroll
        for (int u = 12; u < 19; ++u) if (((u & 1) == c.g)) issue_d(c, u, dloc(u));
        STAMP(9);
        G2STEP(0, hf[0], 3); G2STEP(1, hf[1], 4); G2STEP(2, hf[2], 5); G2STEP(3, hf[3], 6);
        if (c.g == 0) { WAITV(8); } else { WAITV(6); }
        { unsigned old = 0; if (lane == 0) old = __hip_atomic_fetch_add((LAS unsigned*)(c.lds + L_CNT + 4), 1u, __ATOMIC_RELAXED, __HIP_MEMORY_SCOPE_WORKGROUP);
          if (lane == 0 && old == 7) store_flag(ws, WS_OF + ((size_t)c.rb * 16 + c.cg) * 8, tag); }
        SB();
        G2STEP(4, hf[4], 7); G2STEP(5, hf[5], 8); G2STEP(6, hf[6], 9); G2STEP(7, hf[7], 10); G2STEP(8, hf[8], 11); G2STEP(9, hf[9], -1); G2STEP(10, hf[10], -1);
        u64 ofl; aload_flag(ofl, ws + WS_OF + (size_t)rb2 * 128, (unsigned)((lane & 15) * 8));
        STAMP(10);
        SYNC(0);
        RDW(12); RDW(13);
        asm volatile("" : "+v"(ofl));
        const bool ordy = __all(ofl == tag);
        bf16x8 of[8];
        uint2 xa, xb2;
        const int nt = (w >> 1) & 1, rh = w >> 2;
        const int n0 = 64 * c.nb + 32 * nt + 16 * rh + 4 * h;
#define LOAD_O() do { _Pragma("unroll") for (int i = 0; i < 8; ++i) of[i] = __builtin_bit_cast(bf16x8, __builtin_amdgcn_raw_buffer_load_b128(wsr, cbase + (86 + 4 * i) * 1024, 0, 16)); } while (0)
        if (ordy) LOAD_O();
        { const u16* xr = P.x + (size_t)tok * 512 + n0; xa = *(const uint2*)xr; xb2 = *(const uint2*)(xr + 8); }
#pragma unroll
        for (int u = 19; u < 30; ++u) if (((u & 1) == c.g)) issue_d(c, u, dloc(u));
        STAMP(11);
        G2STEP(11, hf[11], 14); G2STEP(12, hf[12], 15); G2STEP(13, hf[13], 16); G2STEP(14, hf[14], 17); G2STEP(15, hf[15], 18); G2STEP(16, hf[16], -1); G2STEP(17, hf[17], -1); G2STEP(18, hf[18], -1);
        if (!ordy) { poll_flags((const u64*)(ws + WS_OF) + rb2 * 16, 16, tag, lane, ws); LOAD_O(); }
        STAMP(12);
        if (ordy) { WAITV(0); }
        SYNC(63);
        RDW(19); RDW(20); RDW(21);
        G2STEP(19, hf[19], 22); G2STEP(20, hf[20], 23);
        if (kq < 2) { acc0 = mfma(wa[21 % 3], hf[21], acc0); acc1 = mfma(wb_[21 % 3], hf[21], acc1); }
        SB(); RDW(24); SB();
        G2STEP(22, of[0], 25); G2STEP(23, of[1], 26); G2STEP(24, of[2], 27); G2STEP(25, of[3], 28); G2STEP(26, of[4], 29); G2STEP(27, of[5], -1); G2STEP(28, of[6], -1); G2STEP(29, of[7], -1);
        STAMP(13);
        SYNC(63);
        { lp rbp = c.lds + (kq * 2 + th) * 8192 + lane * 16;
#pragma unroll
          for (int q = 0; q < 4; ++q) { *(LAS f32x4*)(rbp + q * 1024) = (f32x4){acc0[4 * q], acc0[4 * q + 1], acc0[4 * q + 2], acc0[4 * q + 3]};
                                        *(LAS f32x4*)(rbp + 4096 + q * 1024) = (f32x4){acc1[4 * q], acc1[4 * q + 1], acc1[4 * q + 2], acc1[4 * q + 3]}; } }
        SYNC(63);
        { f32x4 s0 = {0.f, 0.f, 0.f, 0.f}, s1 = s0;
#pragma unroll
          for (int k = 0; k < 4; ++k) { const lcp bp = (lcp)c.lds + (k * 2 + th) * 8192 + nt * 4096 + 2 * rh * 1024 + lane * 16; s0 += ldsf4(bp); s1 += ldsf4(bp + 1024); }
          uint2 oa, ob;
          oa.x = pk2(s0[0] + blo(xa.x), s0[1] + bhi(xa.x)); oa.y = pk2(s0[2] + blo(xa.y), s0[3] + bhi(xa.y));
          ob.x = pk2(s1[0] + blo(xb2.x), s1[1] + bhi(xb2.x)); ob.y = pk2(s1[2] + blo(xb2.y), s1[3] + bhi(xb2.y));
          u16* orow = P.out + (size_t)tok * 512 + n0;
          *(uint2*)orow = oa; *(uint2*)(orow + 8) = ob; }
    }
    STAMP(14);
    if (blockIdx.x == 0 && w == 0) {
#pragma unroll
        for (int j = 0; j < 4; ++j) poll_flags((const u64*)(ws + WS_KVF) + 64 * j, 64, tag, lane, ws);
        if (lane == 0) store_flag(ws, WS_EPOCH, tag);
    }
}

extern "C" void kernel_launch(void* const* d_in, const int* in_sizes, int n_in, void* d_out, int out_size, void* d_ws, size_t ws_size, hipStream_t stream) {
    Params p;
    p.x = (const u16*)d_in[0]; p.wqkv = (const u16*)d_in[1]; p.wo = (const u16*)d_in[2];
    p.wg = (const u16*)d_in[4]; p.wu = (const u16*)d_in[5]; p.wd = (const u16*)d_in[6];
    p.out = (u16*)d_out; p.ws = (char*)d_ws;
    (void)hipFuncSetAttribute((const void*)fused_kernel, hipFuncAttributeMaxDynamicSharedMemorySize, LDS_BYTES);
    fused_kernel<<<256, 512, LDS_BYTES, stream>>>(p);
}
```

```cpp
#include <hip/hip_runtime.h>
#include <stdint.h>
#include <type_traits>

#define FI __device__ __forceinline__
#define LAS __attribute__((address_space(3)))
#define GAS __attribute__((address_space(1)))
typedef unsigned short u16;
typedef unsigned long long u64;
typedef short bf16x8 __attribute__((ext_vector_type(8)));
typedef short s16x4 __attribute__((ext_vector_type(4)));
typedef float f32x16 __attribute__((ext_vector_type(16)));
typedef float f32x4 __attribute__((ext_vector_type(4)));
typedef float f32x2 __attribute__((ext_vector_type(2)));
typedef unsigned u32x4 __attribute__((ext_vector_type(4)));
typedef __bf16 bf16x2_t __attribute__((ext_vector_type(2)));
typedef LAS const char* lcp;
typedef LAS char* lp;

constexpr int GR = 8192;
constexpr int L_KV = 13 * GR;
constexpr int L_KVR = 17 * GR;
constexpr int L_MISC = 19 * GR;
constexpr int L_CS = L_MISC;
constexpr int L_CSW = L_MISC + 2048;
constexpr int L_ZERO = L_MISC + 4096;
constexpr int L_ONES = L_MISC + 6144;
constexpr int L_CNT = L_ONES + 64;
constexpr int L_FINE = L_MISC + 6400;
constexpr int LDS_BYTES = 163840;

constexpr size_t WS_EPOCH = 0, WS_TMO = 64;
constexpr size_t WS_KVF = 4096;
constexpr size_t WS_HF = 8192;
constexpr size_t WS_OF = 12288;
constexpr size_t WS_ST = 65536;
constexpr size_t WS_CS = 131072;
constexpr size_t WS_SCR = 1u << 19;
constexpr size_t WS_KV = 1u << 20;
constexpr size_t WS_C = 8u << 20;
constexpr size_t WS_DBG = 32u << 20;
constexpr int NKS = 118;

FI f32x16 mfma(bf16x8 a, bf16x8 b, f32x16 c) { return __builtin_amdgcn_mfma_f32_32x32x16_bf16(a, b, c, 0, 0, 0); }
FI unsigned pk2(float lo, float hi) { f32x2 v = {lo, hi}; bf16x2_t r = __builtin_convertvector(v, bf16x2_t); return __builtin_bit_cast(unsigned, r); }
FI u32x4 pack8(const f32x16& y, int b) { u32x4 r; r[0] = pk2(y[b], y[b + 1]); r[1] = pk2(y[b + 2], y[b + 3]); r[2] = pk2(y[b + 4], y[b + 5]); r[3] = pk2(y[b + 6], y[b + 7]); return r; }
typedef short v4i16_t __attribute__((ext_vector_type(4)));
FI s16x4 trd(lcp p) { return __builtin_bit_cast(s16x4, __builtin_amdgcn_ds_read_tr16_b64_v4i16((LAS v4i16_t*)p)); }
template <int D2> FI bf16x8 trfrag(lcp p) { s16x4 lo = trd(p), hi = trd(p + D2); return __builtin_shufflevector(lo, hi, 0, 1, 2, 3, 4, 5, 6, 7); }
FI bf16x8 ldsf(lcp p) { return *(const LAS bf16x8*)p; }
FI f32x4 ldsf4(lcp p) { return *(const LAS f32x4*)p; }

FI void dma16(unsigned voff, const void* sbase, unsigned dst) {
    asm volatile("s_mov_b32 m0, %2\n\ts_nop 0\n\tglobal_load_lds_dwordx4 %0, %1" :: "v"(voff), "s"(sbase), "s"(__builtin_amdgcn_readfirstlane(dst)) : "memory", "m0");
}
FI void dma16_sc1(unsigned voff, const void* sbase, unsigned dst) {
    asm volatile("s_mov_b32 m0, %2\n\ts_nop 0\n\tglobal_load_lds_dwordx4 %0, %1 sc1" :: "v"(voff), "s"(sbase), "s"(__builtin_amdgcn_readfirstlane(dst)) : "memory", "m0");
}
FI void aload16(u32x4& v, const void* base, unsigned voff) { asm volatile("global_load_dwordx4 %0, %1, %2" : "=v"(v) : "v"(voff), "s"(base) : "memory"); }
FI void aload16_sc1(u32x4& v, const void* base, unsigned voff) { asm volatile("global_load_dwordx4 %0, %1, %2 sc1" : "=v"(v) : "v"(voff), "s"(base) : "memory"); }
FI void aload_flag(u64& v, const char* base, unsigned voff) { asm volatile("global_load_dwordx2 %0, %1, %2 sc1" : "=v"(v) : "v"(voff), "s"(base) : "memory"); }

#define SB() __builtin_amdgcn_sched_barrier(0)
#define PIN(x) asm volatile("" : "+v"(x))
FI int opaque(int v) { asm volatile("" : "+v"(v)); return v; }
#define FST() do {} while (0)
#define SYNC(N) do { SB(); FST(); asm volatile("s_waitcnt vmcnt(" #N ") lgkmcnt(0)\n\ts_barrier" ::: "memory"); FST(); SB(); } while (0)
#define SYNCV(N) do { SB(); FST(); asm volatile("s_waitcnt vmcnt(" #N ")\n\ts_barrier" ::: "memory"); FST(); SB(); } while (0)
#define WAITV(N) do { SB(); asm volatile("s_waitcnt vmcnt(" #N ")" ::: "memory"); SB(); } while (0)

#define STAMP(k) do {} while (0)

struct Params {
    const u16* x; const u16* wqkv; const u16* wo; const u16* wg; const u16* wu; const u16* wd; u16* out; char* ws;
};

constexpr int wslot(int s) { return (s == 0 ? 4 : s == 1 ? 9 : s == 2 ? 14 : s == 3 ? 0 : s == 4 ? 3 : s == 5 ? 6 : s == 6 ? 11 : s == 7 ? 0 : s == 8 ? 3 : s == 9 ? 6 : s == 10 ? 10 : 0) * GR; }
constexpr int xslot(int c) { return (c == 0 ? 0 : c == 1 ? 2 : c == 2 ? 7 : c == 3 ? 12 : c == 4 ? 17 : c == 5 ? 9 : c == 6 ? 14 : 17) * GR; }
FI constexpr int dloc0(int u) { return (u == 0 ? 9 : u < 4 ? 3 + (u - 1) : u < 7 ? 6 + (u - 4) : u < 10 ? 10 + (u - 7) : u - 10) * GR; }
FI constexpr int dloc(int u) { return u < 13 ? dloc0(u) : u < 26 ? dloc0(u - 13) : L_KV + (u - 26) * GR; }

struct Ctx {
    const Params* p;
    unsigned lds0;
    lp lds;
    int lane, w, tm, g, rb, cg, tb, nb;
    unsigned vq, vf0, vf1, vf2, vx, vlin, vd;
    const char *px, *pq, *pf, *pwd, *pwo;
};

FI void issue_x(const Ctx& c, int chunk) {
    const char* base = c.px + chunk * 128;
    const unsigned dst = c.lds0 + xslot(chunk) + 16 * c.w * 128;
    dma16(c.vx, base, dst); dma16(c.vx ^ 64u, base + 8 * 1024, dst + 8 * 128);
}
template <int S> FI void issue_w(const Ctx& c) {
    if constexpr (S < 8) {
        const char* base = c.pf + (size_t)(64 * S) * 2752;
        const unsigned dst = c.lds0 + wslot(S) + c.g * 12288 + c.tm * 3072;
        dma16(c.vf0, base, dst); dma16(c.vf1, base, dst + 1024); dma16(c.vf2, base, dst + 2048);
    } else {
        const char* base = c.pq + (size_t)(128 * (S - 8)) * 3072;
        const unsigned dst = c.lds0 + wslot(S) + c.w * 1024;
        dma16(c.vq, base + 1024, dst); dma16(c.vq, base + 2048, dst + 8192); dma16(c.vq, base, dst + 16384);
    }
}
FI void issue_d(const Ctx& c, int u) {
    const char* base;
    if (u < 21) base = c.pwd + (size_t)u * 65536;
    else if (u == 21) base = c.pwd + (size_t)21 * 65536 - (c.w >= 4 ? 32768 : 0);
    else base = c.pwo + (size_t)(u - 22) * 65536;
    dma16(c.vd, base, c.lds0 + dloc(u) + c.w * 1024);
}

FI float blo(unsigned d) { return __uint_as_float(d << 16); }
FI float bhi(unsigned d) { return __uint_as_float(d & 0xffff0000u); }
template <int CTRL> FI float dppadd(float x) { return x + __builtin_bit_cast(float, __builtin_amdgcn_update_dpp(0, __builtin_bit_cast(int, x), CTRL, 0xf, 0xf, true)); }
FI void swap32add(float& u, float v) { auto rr = __builtin_amdgcn_permlane32_swap(__float_as_uint(u), __float_as_uint(v), false, false); u = __uint_as_float(rr[0]) + __uint_as_float(rr[1]); }
FI void swap16add(float& u, float v) { auto rr = __builtin_amdgcn_permlane16_swap(__float_as_uint(u), __float_as_uint(v), false, false); u = __uint_as_float(rr[0]) + __uint_as_float(rr[1]); }

struct G1State {
    bf16x8 xf[32];
    bf16x8 wq[4];
    f32x16 a0, a1, b0, b1;
    unsigned wb[4];
    unsigned xb[4];
};
constexpr int fkind(int G, int f) { return G == 0 ? (f < 8 ? (f & 3) : (f & 1)) : (f < 4 ? (f & 1) : f < 6 ? (f & 1) : f < 8 ? 2 + (f & 1) : f < 10 ? (f & 1) : 2 + (f & 1)); }
constexpr int fpos(int G, int f) { return G == 0 ? (f < 8 ? (f >> 2) : 2 + ((f - 8) >> 1)) : (f < 4 ? (f >> 1) : f < 8 ? 2 : 3); }
constexpr int qkind(int G, int f) { return G == 0 ? ((f == 1 || f == 3 || f == 7 || f == 9) ? 1 : 0) : ((f == 3 || f == 5 || f == 9 || f == 11) ? 1 : 0); }
constexpr int qpos(int G, int f) {
    return G == 0 ? (f == 0 ? 0 : f == 1 ? 0 : f == 2 ? 1 : f == 3 ? 1 : f == 4 ? 2 : f == 5 ? 3 : f == 6 ? 4 : f == 7 ? 4 : f == 8 ? 5 : f == 9 ? 5 : f == 10 ? 6 : 7)
                  : (f == 0 ? 0 : f == 1 ? 1 : f == 2 ? 2 : f == 3 ? 2 : f == 4 ? 3 : f == 5 ? 3 : f == 6 ? 4 : f == 7 ? 5 : f == 8 ? 6 : f == 9 ? 6 : f == 10 ? 7 : 7);
}
template <int G, int S> FI bf16x8 rdfrag(const G1State& st, int f) {
    if constexpr (S < 8) { const int k = fkind(G, f); return trfrag<768>((lcp)(uintptr_t)(st.wb[fpos(G, f)] + (k & 1) * 12288 + (k >= 2 ? 64 : (G == 0 ? 0 : 128)))); }
    else { const int e = qpos(G, f); return trfrag<256>((lcp)(uintptr_t)(st.wb[e & 3] + (e >> 2) * 4096 + (qkind(G, f) ? 16384 : G * 8192))); }
}
template <int C> FI void rdx(G1State& st) {
#pragma unroll
    for (int e = 0; e < 4; ++e) { st.xf[4 * C + e] = ldsf((lcp)(uintptr_t)st.xb[e]); if (C < 7) st.xb[e] = (unsigned)opaque((int)(st.xb[e] + (xslot(C + 1) - xslot(C)))); }
}
template <int G, int S, bool FRESH, bool PF, class F>
FI void stage(G1State& st, F&& hook) {
    if constexpr (FRESH) {
#pragma unroll
        for (int f = 0; f < 4; ++f) st.wq[f] = rdfrag<G, S>(st, f);
        if constexpr (S < 8) rdx<(S < 8 ? S : 0)>(st);
        SB();
    }
#pragma unroll
    for (int f = 0; f < 12; ++f) {
        const bf16x8 w = st.wq[f & 3];
        if constexpr (S < 8) {
            const int k = fkind(G, f); const bf16x8 x = st.xf[4 * S + fpos(G, f)];
            if (k == 0) st.a0 = mfma(w, x, st.a0); else if (k == 1) st.a1 = mfma(w, x, st.a1); else if (k == 2) st.b0 = mfma(w, x, st.b0); else st.b1 = mfma(w, x, st.b1);
        } else {
            const bf16x8 x = st.xf[8 * (S - 8) + qpos(G, f)];
            if (qkind(G, f)) st.b0 = mfma(w, x, st.b0); else if (G == 0) st.a0 = mfma(w, x, st.a0); else st.a0 = mfma(x, w, st.a0);
        }
        SB();
        if (f < 8) st.wq[f & 3] = rdfrag<G, S>(st, f + 4);
        else if (PF) { if (f == 8) { _Pragma("unroll") for (int j = 0; j < 4; ++j) st.wb[j] = (unsigned)opaque((int)(st.wb[j] + (wslot(S + 1) - wslot(S)))); }
            st.wq[f & 3] = rdfrag<G, (PF ? S + 1 : S)>(st, f - 8); }
        if constexpr (S < 7) { if (f == 6) rdx<(S < 7 ? S + 1 : 0)>(st); }
        hook(f);
        SB();
    }
}

FI u64 mix64(u64 z) { z += 0x9E3779B97F4A7C15ull; z = (z ^ (z >> 30)) * 0xBF58476D1CE4E5B9ull; z = (z ^ (z >> 27)) * 0x94D049BB133111EBull; return z ^ (z >> 31); }

constexpr unsigned SPIN_CAP = 1u << 18;
FI void set_timeout(char* ws, int lane) { if (lane == 0) __hip_atomic_store((GAS unsigned*)(ws + WS_TMO), 1u, __ATOMIC_RELAXED, __HIP_MEMORY_SCOPE_AGENT); }
FI void poll_flags(const u64* flags, int n, u64 tag, int lane, char* ws) {
    const GAS u64* pp = (const GAS u64*)flags + (lane < n ? lane : 0);
    for (unsigned spins = 0;; ++spins) {
        const u64 v = __hip_atomic_load(pp, __ATOMIC_RELAXED, __HIP_MEMORY_SCOPE_AGENT);
        if (__all(v == tag)) return;
        if (spins > SPIN_CAP) { set_timeout(ws, lane); return; }
        __builtin_amdgcn_s_sleep(2);
    }
}
FI void store_flag(char* ws, size_t off, u64 tag) { __hip_atomic_store((GAS u64*)(ws + off), tag, __ATOMIC_RELAXED, __HIP_MEMORY_SCOPE_AGENT); }

__global__ __launch_bounds__(512, 2) void fused_kernel(Params P) {
    extern __shared__ __attribute__((aligned(16))) char smem[];
    Ctx c; c.p = &P;
    const int tid = threadIdx.x; c.lane = tid & 63;
    const int w = __builtin_amdgcn_readfirstlane(tid >> 6);
    c.w = w; c.tm = w & 3; c.g = w >> 2;
    c.lds = (lp)smem; c.lds0 = (unsigned)(uintptr_t)c.lds;
    const int xcd = blockIdx.x & 7, idx = blockIdx.x >> 3;
    c.rb = 4 * (xcd >> 1) + (idx >> 3); c.cg = 8 * (xcd & 1) + (idx & 7);
    c.tb = 4 * xcd + (idx >> 3); c.nb = idx & 7;
    const int fs = c.cg < 11 ? 3 * c.cg : c.cg < 15 ? 33 + 2 * (c.cg - 11) : 40;
    const bool v0 = c.cg != 15, v2 = c.cg < 11 || c.cg == 15;
    const int lane = c.lane, h = lane >> 5, r32 = lane & 31;
    char* ws = P.ws;
    STAMP(0);
    c.px = (const char*)P.x + (size_t)(128 * c.rb + 16 * w) * 1024;
    c.pf = (const char*)(c.g ? P.wu : P.wg) + (size_t)(16 * c.tm) * 2752 + fs * 64;
    u64 tag = 0; unsigned tagLo = 0, tagHi = 0; float ln_a = 0.f, ln_m = 0.f;
    const int T = 4 * c.rb + c.tm;
    const __amdgpu_buffer_rsrc_t wsr = __builtin_amdgcn_make_buffer_rsrc(ws, 0, 32u << 20, 0x00020000);
    c.pq = (const char*)P.wqkv + ((size_t)(16 * w) * 1536 + 32 * c.cg) * 2;
    c.pwd = (const char*)P.wd + (size_t)(8 * w) * 1024 + c.nb * 128;
    c.pwo = (const char*)P.wo + (size_t)(8 * w) * 1024 + c.nb * 128;
    u32x4 gs, g0a, g0b;
    const int gt0 = w < 6 ? 3 + w : w - 6;
#define GATHER_OFFS(tile) const int gl_ = opaque(lane); const unsigned gso = (unsigned)(WS_ST + (128 * c.rb + 32 * c.tm + (gl_ & 31)) * 16), go0 = (unsigned)(WS_CS + (c.cg * 9 + (tile)) * 2048 + (gl_ >> 3) * 256 + (gl_ & 7) * 32)
#define GATHER_ISSUE(tile, ST) do { GATHER_OFFS(tile); if (ST) aload16_sc1(gs, P.ws, gso); aload16_sc1(g0a, P.ws, go0); aload16_sc1(g0b, P.ws + 16, go0); } while (0)
#define GATHER_OK(ST) ((!(ST) || (gs[1] == tagLo && gs[3] == tagHi)) && g0a[1] == tagLo && g0a[3] == tagLo && g0b[1] == tagLo && g0b[3] == tagLo)
#define GATHER_RED(tile) do { const int gl_ = opaque(lane); float v0_ = __uint_as_float(g0a[0]), v1_ = __uint_as_float(g0a[2]), v2_ = __uint_as_float(g0b[0]), v3_ = __uint_as_float(g0b[2]); \
        swap32add(v0_, v2_); swap32add(v1_, v3_); swap16add(v0_, v1_); v0_ = dppadd<0x128>(v0_); \
        if ((gl_ & 8) == 0) *(LAS float*)(c.lds + L_CS + (tile) * 128 + (4 * (gl_ & 7) + 2 * (gl_ >> 5) + ((gl_ >> 4) & 1)) * 4) = v0_; } while (0)
#define GATHER_CHECK(tile, ST) do { if (ST) { asm volatile("" : "+v"(gs), "+v"(g0a), "+v"(g0b)); } else { asm volatile("" : "+v"(g0a), "+v"(g0b)); } \
        if (!__all(GATHER_OK(ST))) { GATHER_OFFS(tile); \
            for (unsigned spins = 0;; ++spins) { __builtin_amdgcn_s_sleep(2); \
                if (ST) gs = __builtin_amdgcn_raw_buffer_load_b128(wsr, gso, 0, 16); \
                g0a = __builtin_amdgcn_raw_buffer_load_b128(wsr, go0, 0, 16); g0b = __builtin_amdgcn_raw_buffer_load_b128(wsr, go0 + 16, 0, 16); __builtin_amdgcn_s_waitcnt(0x0F70); \
                if (__all(GATHER_OK(ST))) break; if (spins > SPIN_CAP) { set_timeout(ws, lane); break; } } } \
        if (ST) { ln_m = __uint_as_float(gs[0]); ln_a = __uint_as_float(gs[2]); PIN(ln_m); PIN(ln_a); } } while (0)
#define RD_CS(dst, tile) do { _Pragma("unroll") for (int q_ = 0; q_ < 4; ++q_) { const f32x4 t_ = ldsf4((lcp)c.lds + L_CS + (tile) * 128 + (8 * q_ + 4 * h) * 4); \
            dst[4 * q_] = t_[0]; dst[4 * q_ + 1] = t_[1]; dst[4 * q_ + 2] = t_[2]; dst[4 * q_ + 3] = t_[3]; } } while (0)
#define SPREAD(dst, v, row) do { if (h == 0) *(LAS float*)(c.lds + L_CSW + c.tm * 256 + (row) * 128 + r32 * 4) = (v); \
        _Pragma("unroll") for (int q_ = 0; q_ < 4; ++q_) { const f32x4 t_ = ldsf4((lcp)c.lds + L_CSW + c.tm * 256 + (row) * 128 + (8 * q_ + 4 * h) * 4); \
            dst[4 * q_] = t_[0]; dst[4 * q_ + 1] = t_[1]; dst[4 * q_ + 2] = t_[2]; dst[4 * q_ + 3] = t_[3]; } } while (0)
#define H1(...) [&](int f_) { if (f_ == 1) { __VA_ARGS__ } }
#define F4(v, b) (f32x4){v[b], v[(b) + 1], v[(b) + 2], v[(b) + 3]}

    const int th = w & 1, kq = w >> 1;
    const int T2 = 2 * c.tb + th, rb2 = c.tb >> 1;
    u64 hfl;
    bf16x8 qf;
    auto gemm1 = [&](auto Gc) {
        constexpr int G = decltype(Gc)::value;
        const int lane = opaque(c.lane), h = lane >> 5, r32 = lane & 31, tid = w * 64 + lane;
        const unsigned vq0 = (lane >> 2) * 3072 + (lane & 3) * 16;
        c.vf0 = (lane / 12) * 2752 + (lane % 12) * 16; c.vf1 = ((lane + 64) / 12) * 2752 + ((lane + 64) % 12) * 16; c.vf2 = ((lane + 128) / 12) * 2752 + ((lane + 128) % 12) * 16;
        c.vx = (lane >> 3) * 1024 + (((lane & 7) ^ (lane >> 4)) * 16); c.vlin = lane * 16;
        G1State st;
    issue_x(c, 0); issue_w<0>(c);
    u64 ep; aload_flag(ep, ws + WS_EPOCH, 0u);
    u32x4 ax, aw0, aw1, aw2, aw3;
    aload16(ax, (const char*)P.x + (size_t)(128 * c.rb + 8 * c.cg + w) * 1024, c.vlin);
    const int ptile = 2 * w + (c.rb & 1); const bool pvalid = ptile <= 8;
    aw0 = aw1 = aw2 = aw3 = (u32x4){0u, 0u, 0u, 0u};
    if (pvalid) {
        const char* tb; unsigned pitch, vo;
        if (ptile < 3) { tb = (const char*)P.wqkv + (ptile == 0 ? 512 : ptile == 1 ? 1024 : 0) * 2 + 64 * c.cg; pitch = 3072; vo = vq0; }
        else { tb = (const char*)((ptile & 1) ? P.wg : P.wu) + 64 * (fs + ((ptile - 3) >> 1)); pitch = 2752; vo = (lane >> 2) * 2752 + (lane & 3) * 16; }
        tb += (size_t)(64 * (c.rb >> 1)) * pitch;
        aload16(aw0, tb, vo); aload16(aw1, tb + 16 * pitch, vo); aload16(aw2, tb + 32 * pitch, vo); aload16(aw3, tb + 48 * pitch, vo);
    }
    issue_x(c, 1); issue_w<1>(c); issue_x(c, 2); issue_w<2>(c); issue_x(c, 3);
    *(LAS unsigned*)(c.lds + L_ZERO + tid * 4) = 0u;
    if (tid < 4) { *(LAS unsigned*)(c.lds + L_ONES + tid * 4) = 0x3f803f80u; *(LAS unsigned*)(c.lds + L_ONES + 1024 + tid * 4) = 0x3f803f80u; }
    if (tid >= 64 && tid < 72) *(LAS unsigned*)(c.lds + L_CNT + (tid - 64) * 4) = 0u;

    const int q4 = (lane & 15) >> 2, p4 = lane & 3, b16 = (lane >> 4) & 1;
    { const unsigned lb = c.lds0 + (8 * h + q4) * 192 + 32 * b16 + 8 * p4;
#pragma unroll
      for (int j = 0; j < 4; ++j) st.wb[j] = lb + ((j ^ c.tm) * 3072) + wslot(0); }
    { const int row = 32 * c.tm + r32;
#pragma unroll
      for (int j = 0; j < 4; ++j) st.xb[j] = c.lds0 + xslot(0) + row * 128 + (((2 * (j ^ c.tm) + h) ^ ((row >> 1) & 7)) * 16); }
    st.a0 = f32x16{}; st.a1 = f32x16{}; st.b0 = f32x16{}; st.b1 = f32x16{};

    SYNC(7);
    asm volatile("" : "+v"(ep), "+v"(ax), "+v"(aw0), "+v"(aw1), "+v"(aw2), "+v"(aw3));
    tag = mix64(((u64)(unsigned)__builtin_amdgcn_readfirstlane((int)(unsigned)(ep >> 32)) << 32) | (unsigned)__builtin_amdgcn_readfirstlane((int)(unsigned)ep));
    tagLo = (unsigned)tag; tagHi = (unsigned)(tag >> 32);
    STAMP(1);

    float as_ = 0.f, aq_ = 0.f, av[8];
    auto aux0 = [&](int f) {
        if (f < 4) { const float a = blo(ax[f]), b = bhi(ax[f]); as_ += a + b; aq_ = __builtin_fmaf(a, a, aq_); aq_ = __builtin_fmaf(b, b, aq_); PIN(as_); PIN(aq_); }
        if (f >= 4 && f < 8) { const int i = f - 4; av[2 * i] = (blo(aw0[i]) + blo(aw1[i])) + (blo(aw2[i]) + blo(aw3[i])); av[2 * i + 1] = (bhi(aw0[i]) + bhi(aw1[i])) + (bhi(aw2[i]) + bhi(aw3[i])); PIN(av[2 * i]); PIN(av[2 * i + 1]); }
        if (f == 8) { as_ = dppadd<0x121>(as_); aq_ = dppadd<0x121>(aq_); as_ = dppadd<0x122>(as_); aq_ = dppadd<0x122>(aq_); as_ = dppadd<0x124>(as_); aq_ = dppadd<0x124>(aq_); as_ = dppadd<0x128>(as_); aq_ = dppadd<0x128>(aq_); PIN(as_); PIN(aq_); }
        if (f == 9) { swap16add(as_, as_); swap16add(aq_, aq_); swap32add(as_, as_); swap32add(aq_, aq_); PIN(as_); PIN(aq_); }
        if (f == 10) { swap32add(av[0], av[4]); swap32add(av[1], av[5]); swap32add(av[2], av[6]); swap32add(av[3], av[7]); PIN(av[0]); PIN(av[1]); PIN(av[2]); PIN(av[3]); }
        if (f == 11) { swap16add(av[0], av[2]); swap16add(av[1], av[3]); av[0] = dppadd<0x124>(av[0]); av[1] = dppadd<0x124>(av[1]); av[0] = dppadd<0x128>(av[0]); av[1] = dppadd<0x128>(av[1]); PIN(av[0]); PIN(av[1]); }
    };
    auto aux1 = [&](int f) {
        if (f == 0) { const float mean = as_ * (1.f / 512.f), var = aq_ * (1.f / 512.f) - mean * mean, rstd = __builtin_amdgcn_rsqf(fmaxf(var, 0.f) + 1e-5f);
            if (lane == 0) __builtin_amdgcn_raw_buffer_store_b128((u32x4){__float_as_uint(mean), tagLo, __float_as_uint(rstd), tagHi}, wsr, (unsigned)(WS_ST + (128 * c.rb + 8 * c.cg + w) * 16), 0, 16); }
        if (f == 0) { const int rho = lane >> 4, col = 8 * (lane & 3) + 4 * (rho >> 1) + 2 * (rho & 1);
            const unsigned off = pvalid ? (unsigned)(WS_CS + (((c.cg * 9 + ptile) * 8 + (c.rb >> 1)) * 32 + col) * 8) : (unsigned)(WS_SCR + blockIdx.x * 256 + col * 8);
            if ((lane & 12) == 0) __builtin_amdgcn_raw_buffer_store_b128((u32x4){__float_as_uint(av[0]), tagLo, __float_as_uint(av[1]), tagLo}, wsr, off, 0, 16); }
    };

        stage<G, 0, true, true>(st, [&](int f_) { aux0(f_); }); SYNCV(2);
        stage<G, 1, false, true>(st, [&](int f_) { if (f_ == 1) { issue_w<3>(c); issue_w<4>(c); issue_x(c, 4); } if (f_ == 4) aux1(0); }); SYNCV(7);
        stage<G, 2, false, true>(st, H1(issue_w<5>(c); issue_x(c, 5);)); SYNCV(7);
        stage<G, 3, false, true>(st, H1(issue_w<6>(c); issue_x(c, 6);)); SYNCV(5);
        stage<G, 4, false, true>(st, H1(issue_w<7>(c); issue_x(c, 7);)); SYNCV(5);
        STAMP(2);
        { const int ln = opaque(lane); c.vq = (ln >> 2) * 3072 + (ln & 3) * 16; c.vd = (ln >> 3) * 1024 + (((ln & 7) ^ (((ln >> 4) & 1) << 2)) * 16); }
        stage<G, 5, false, true>(st, [&](int f_) { if (f_ == 1) { issue_w<8>(c); issue_d(c, 0); } if (f_ == 9) GATHER_ISSUE(gt0, true); }); SYNCV(7);
        stage<G, 6, false, true>(st, H1(issue_w<9>(c);)); SYNCV(7);
        stage<G, 7, false, false>(st, [&](int f_) { if (f_ == 1) issue_w<10>(c); if (f_ == 8) { WAITV(6); GATHER_CHECK(gt0, true); } if (f_ == 9) GATHER_RED(gt0); });
        {
          lp xp = c.lds + L_KV + w * 4096 + c.vlin;
          *(LAS f32x4*)(xp) = F4(st.b0, 8 - 8 * G); *(LAS f32x4*)(xp + 1024) = F4(st.b0, 12 - 8 * G); *(LAS f32x4*)(xp + 2048) = F4(st.b1, 8 - 8 * G); *(LAS f32x4*)(xp + 3072) = F4(st.b1, 12 - 8 * G); }
        { const int ln = opaque(lane); const unsigned lb = c.lds0 + (8 * (ln >> 5) + ((ln & 15) >> 2)) * 64 + 32 * ((ln >> 4) & 1) + 8 * (ln & 3);
#pragma unroll
          for (int j = 0; j < 4; ++j) st.wb[j] = lb + ((j ^ c.tm) * 1024) + wslot(8); }
        SYNC(3);
        STAMP(3);
        {
          const int lane = opaque(c.lane), h = lane >> 5;
          const float a2 = ln_a * ln_a;
          { u32x4 y0, y1;
#pragma unroll
            for (int q = 0; q < 4; ++q) {
                const f32x4 cg_ = ldsf4((lcp)c.lds + L_CS + (3 + 4 * G) * 128 + (8 * q + 4 * h) * 4), cu_ = ldsf4((lcp)c.lds + L_CS + (4 + 4 * G) * 128 + (8 * q + 4 * h) * 4);
                float y[4];
#pragma unroll
                for (int i = 0; i < 4; ++i) { const float g_ = fmaxf(__builtin_fmaf(-ln_m, cg_[i], st.a0[4 * q + i]), 0.f), u_ = __builtin_fmaf(-ln_m, cu_[i], st.a1[4 * q + i]); y[i] = (g_ * a2) * u_; }
                const unsigned lo = pk2(y[0], y[1]), hi = pk2(y[2], y[3]);
                if (q < 2) { y0[2 * q] = lo; y0[2 * q + 1] = hi; } else { y1[2 * q - 4] = lo; y1[2 * q - 3] = hi; }
                if (q == 1) SB();
            }
            if (G == 0 ? v0 : v2) { const unsigned o_ = (unsigned)(WS_C + ((size_t)T * NKS + 2 * (fs + 2 * G)) * 1024) + lane * 16;
                __builtin_amdgcn_raw_buffer_store_b128(y0, wsr, o_, 0, 16); __builtin_amdgcn_raw_buffer_store_b128(y1, wsr, o_ + 1024, 0, 16); } }
          SB();
          { const lcp xp = (lcp)c.lds + L_KV + (w ^ 4) * 4096 + lane * 16;
            u32x4 y0;
#pragma unroll
            for (int q = 0; q < 2; ++q) {
                const f32x4 pg = ldsf4(xp + q * 1024), pu = ldsf4(xp + 2048 + q * 1024);
                const f32x4 cg_ = ldsf4((lcp)c.lds + L_CS + 5 * 128 + (16 * G + 8 * q + 4 * h) * 4), cu_ = ldsf4((lcp)c.lds + L_CS + 6 * 128 + (16 * G + 8 * q + 4 * h) * 4);
                float y[4];
#pragma unroll
                for (int i = 0; i < 4; ++i) { const float g_ = fmaxf(__builtin_fmaf(-ln_m, cg_[i], st.b0[8 * G + 4 * q + i] + pg[i]), 0.f), u_ = __builtin_fmaf(-ln_m, cu_[i], st.b1[8 * G + 4 * q + i] + pu[i]); y[i] = (g_ * a2) * u_; }
                y0[2 * q] = pk2(y[0], y[1]); y0[2 * q + 1] = pk2(y[2], y[3]);
                if (q == 0) SB();
            }
            __builtin_amdgcn_raw_buffer_store_b128(y0, wsr, (unsigned)(WS_C + ((size_t)T * NKS + 2 * (fs + 1) + G) * 1024) + lane * 16, 0, 16); }
          st.a0 = f32x16{}; st.b0 = f32x16{}; }
#pragma unroll
        for (int f = 0; f < 4; ++f) st.wq[f] = rdfrag<G, 8>(st, f);
        stage<G, 8, false, true>(st, H1(issue_w<11>(c);)); SYNCV(4);
        stage<G, 9, false, true>(st, H1(issue_d(c, 1); issue_d(c, 2); issue_d(c, 3);)); SYNCV(3);
        if (w == 0 && lane == 0) store_flag(ws, WS_HF + ((size_t)c.rb * 16 + c.cg) * 8, tag);
        STAMP(4);
        stage<G, 10, false, true>(st, [&](int f_) { if (f_ == 0) GATHER_ISSUE(2, false); if (f_ == 1) { issue_d(c, 4); issue_d(c, 5); issue_d(c, 6); } }); SYNCV(63);
        stage<G, 11, false, false>(st, [&](int f_) { if (f_ == 1) { issue_d(c, 7); issue_d(c, 8); issue_d(c, 9); } if (f_ == 5) { WAITV(6); GATHER_CHECK(2, false); GATHER_RED(2); } if (f_ == 6) aload_flag(hfl, P.ws, (unsigned)(WS_HF + rb2 * 128 + (opaque(lane) & 15) * 8)); });
        { lp xp = c.lds + L_KV + w * 2048 + c.vlin; *(LAS f32x4*)(xp) = F4(st.b0, 8 - 8 * G); *(LAS f32x4*)(xp + 1024) = F4(st.b0, 12 - 8 * G); }
        SYNC(63);
        STAMP(5);
        { const int lane = opaque(c.lane), h = lane >> 5, r32 = lane & 31;
          const unsigned o = (unsigned)(WS_KV + ((size_t)c.cg * 64 + T) * 4096) + lane * 16 + G * 2048;
          f32x16 y;
          if constexpr (G == 0) { f32x16 ck; RD_CS(ck, 0);
#pragma unroll
              for (int r = 0; r < 16; ++r) y[r] = ln_a * __builtin_fmaf(-ln_m, ck[r], st.a0[r]);
          } else {
              const float csv = *(const LAS float*)(c.lds + L_CS + 128 + r32 * 4);
              f32x16 aa, mm; SPREAD(aa, ln_a, 0); SPREAD(mm, ln_m, 1);
#pragma unroll
              for (int r = 0; r < 16; ++r) y[r] = aa[r] * __builtin_fmaf(-mm[r], csv, st.a0[r]);
          }
          const u32x4 k0 = pack8(y, 0), k1 = pack8(y, 8);
          *(LAS u32x4*)(c.lds + L_KVR + c.tm * 4096 + G * 2048 + lane * 16) = k0; *(LAS u32x4*)(c.lds + L_KVR + c.tm * 4096 + G * 2048 + 1024 + lane * 16) = k1;
          __builtin_amdgcn_raw_buffer_store_b128(k0, wsr, o, 0, 16); __builtin_amdgcn_raw_buffer_store_b128(k1, wsr, o + 1024, 0, 16);
          const lcp xp = (lcp)c.lds + L_KV + (w ^ 4) * 2048 + lane * 16;
          const f32x4 p0 = ldsf4(xp), p1 = ldsf4(xp + 1024);
          const f32x4 c0 = ldsf4((lcp)c.lds + L_CS + 2 * 128 + (16 * G + 4 * h) * 4), c1 = ldsf4((lcp)c.lds + L_CS + 2 * 128 + (16 * G + 8 + 4 * h) * 4);
          const float sc = ln_a * (0.25f * 1.4426950408889634f);
#pragma unroll
          for (int i = 0; i < 4; ++i) { y[i] = sc * __builtin_fmaf(-ln_m, c0[i], st.b0[8 * G + i] + p0[i]); y[4 + i] = sc * __builtin_fmaf(-ln_m, c1[i], st.b0[8 * G + 4 + i] + p1[i]); }
          qf = __builtin_bit_cast(bf16x8, pack8(y, 0)); }
    };
    if (c.g == 0) gemm1(std::integral_constant<int, 0>{}); else gemm1(std::integral_constant<int, 1>{});

    const int lane2 = opaque(lane);
    const unsigned cbase = (unsigned)(WS_C + ((size_t)T2 * NKS + kq) * 1024) + lane2 * 16;
    bf16x8 hf[22];
    f32x16 acc0 = f32x16{}, acc1 = f32x16{};
    unsigned wl0, wl1;
    { const int lane = lane2, h = lane >> 5, q4 = (lane & 15) >> 2, p4 = lane & 3, b16 = (lane >> 4) & 1;
      const unsigned wl = c.lds0 + kq * 2048 + (4 * h + q4) * 128 + 32 * b16 + 8 * p4, swz = ((q4 >> 1) & 1) << 6;
      wl0 = wl + (0u ^ swz); wl1 = wl + (64u ^ swz); }
    bf16x8 wa[3], wb_[3];
#define LDH(i) do { const bool ok_ = ((i) < 21) || (kq < 2); hf[i] = __builtin_bit_cast(bf16x8, __builtin_amdgcn_raw_buffer_load_b128(wsr, ok_ ? cbase + (i) * 4096 : cbase, 0, 16)); } while (0)
#define RDW(u) do { wa[(u) % 3] = trfrag<1024>((lcp)(uintptr_t)(wl0 + dloc(u))); wb_[(u) % 3] = trfrag<1024>((lcp)(uintptr_t)(wl1 + dloc(u))); } while (0)
#define G2STEP(u, frag, nxt) do { acc0 = mfma(wa[(u) % 3], frag, acc0); acc1 = mfma(wb_[(u) % 3], frag, acc1); SB(); if ((nxt) >= 0) RDW((nxt) < 0 ? 0 : (nxt)); SB(); } while (0)
    issue_d(c, 10); issue_d(c, 11); issue_d(c, 12);
    WAITV(5); asm volatile("" : "+v"(hfl));
    if (!__all(hfl == tag)) poll_flags((const u64*)(ws + WS_HF) + rb2 * 16, 16, tag, lane2, ws);
#pragma unroll
    for (int i = 0; i < 13; ++i) LDH(i);
    SYNC(63);
    STAMP(6);
    RDW(0); RDW(1); RDW(2);
    G2STEP(0, hf[0], 3); G2STEP(1, hf[1], 4); G2STEP(2, hf[2], 5); G2STEP(3, hf[3], 6);
    SYNCV(13);
    if (w == 0 && lane2 == 0) store_flag(ws, WS_KVF + ((size_t)c.cg * 16 + c.rb) * 8, tag);
    G2STEP(4, hf[4], 7); G2STEP(5, hf[5], 8); G2STEP(6, hf[6], 9); G2STEP(7, hf[7], 10);
    u64 kf_; aload_flag(kf_, P.ws, (unsigned)(WS_KVF + c.cg * 128 + ((c.rb >= 2 ? c.rb - 2 : 0) + (lane2 & 1)) * 8));
    G2STEP(8, hf[8], 11); G2STEP(9, hf[9], 12); G2STEP(10, hf[10], -1);
    WAITV(0); asm volatile("" : "+v"(kf_));
    if (c.rb >= 1) {
        const bool need = c.rb >= 2 || (lane2 & 1) == 0;
        if (!__all(!need || kf_ == tag)) poll_flags((const u64*)(ws + WS_KVF) + c.cg * 16 + (c.rb >= 2 ? c.rb - 2 : 0), c.rb >= 2 ? 2 : 1, tag, lane2, ws);
        if (c.rb >= 2 || w >= 4) {
            const char* base = ws + WS_KV + ((size_t)c.cg * 64 + (4 * c.rb - 8 + w)) * 4096; const unsigned dst = c.lds0 + L_KV + w * 4096;
            dma16_sc1(c.vlin, base, dst); dma16_sc1(c.vlin, base + 1024, dst + 1024); dma16_sc1(c.vlin, base + 2048, dst + 2048); dma16_sc1(c.vlin, base + 3072, dst + 3072);
        }
    }
    G2STEP(11, hf[11], -1); G2STEP(12, hf[12], -1);
    if (blockIdx.x == 0 && w == 7) {
        const GAS u64* pp = (const GAS u64*)(ws + WS_HF) + lane2 * 4; bool ok = true;
#pragma unroll
        for (int j = 0; j < 4; ++j) ok &= __hip_atomic_load(pp + j, __ATOMIC_RELAXED, __HIP_MEMORY_SCOPE_AGENT) == tag;
        if (__all(ok)) { if (lane2 == 0) { store_flag(ws, WS_EPOCH, tag); *(LAS unsigned*)(c.lds + L_CNT + 8) = 1u; } }
    }
    STAMP(7);
    SYNC(0);
    STAMP(8);
    {
        const int lane = lane2, h = lane >> 5, r32 = lane & 31;
        const int i0 = T >= 8 ? 0 : 8 - T;
        const unsigned kb = c.lds0 + L_KV + c.tm * 4096 + c.g * 1024 + lane * 16;
        const unsigned lanebit = r32 < 16 ? 1u : 0u;
        const unsigned vb = c.lds0 + (r32 < 16 ? (unsigned)(((r32 & 15) + 16 * c.g + 32 * h) * 16) : (unsigned)L_ONES);
        auto koff = [&](int i) -> unsigned { return (unsigned)((i >= i0 ? i : 7) * 4096); };
        auto rdk = [&](int i) -> bf16x8 { return ldsf((lcp)(uintptr_t)(kb + koff(i))); };
        auto vaddr = [&](int i) -> unsigned { const bool valid = i >= i0; const unsigned d = valid ? 0u : (unsigned)(L_ZERO - L_ONES), A = valid ? (unsigned)(L_KV + 2048 + (c.tm + i) * 4096) : (unsigned)L_ZERO;
                                              return lanebit * (A - d) + (vb + d); };
        auto dmask = [&](int r) -> bool { u64 mk = 0;
#pragma unroll
            for (int l = 0; l < 64; ++l) { const int kl = (r & 3) + 8 * (r >> 2) + 4 * (l >> 5); if (kl <= (l & 31)) mk |= 1ull << l; }
            return __builtin_amdgcn_inverse_ballot_w64(mk); };
        const f32x16 zero = f32x16{};
        float m0 = -1e30f, m1 = -1e30f;
#define MAXT(S) do { _Pragma("unroll") for (int r = 0; r < 16; r += 4) { m0 = fmaxf(fmaxf(m0, S[r]), S[r + 1]); m1 = fmaxf(fmaxf(m1, S[r + 2]), S[r + 3]); } PIN(m0); PIN(m1); } while (0)
#define EXPT(S) do { _Pragma("unroll") for (int r = 0; r < 16; ++r) S[r] = __builtin_amdgcn_exp2f(S[r]); } while (0)
        f32x16 o = f32x16{};
        float m;
        if (T == 0) {
#pragma unroll
            for (int i = 13; i < 22; ++i) LDH(i);
#pragma unroll
            for (int u = 13; u < 26; ++u) issue_d(c, u);
            f32x16 s = mfma(ldsf((lcp)(uintptr_t)(kb + 8 * 4096)), qf, zero);
#pragma unroll
            for (int r = 0; r < 16; ++r) s[r] = dmask(r) ? s[r] : -1e30f;
            MAXT(s);
            m = fmaxf(m0, m1);
            { auto rr = __builtin_amdgcn_permlane32_swap(__float_as_uint(m), __float_as_uint(m), false, false); m = fmaxf(__uint_as_float(rr[0]), __uint_as_float(rr[1])); }
#pragma unroll
            for (int r = 0; r < 16; ++r) s[r] = __builtin_amdgcn_exp2f(s[r] - m);
            const unsigned va = vaddr(8);
            o = mfma(ldsf((lcp)(uintptr_t)va), __builtin_bit_cast(bf16x8, pack8(s, 0)), o);
            o = mfma(ldsf((lcp)(uintptr_t)(va + 1024)), __builtin_bit_cast(bf16x8, pack8(s, 8)), o);
        } else {
            bf16x8 k0 = rdk(0), k8 = rdk(8), k1 = rdk(1), k2 = rdk(2), k3 = rdk(3);
            f32x16 sA, sB, sC;
            sA = mfma(k0, qf, zero); sB = mfma(k8, qf, zero); SB();
            k0 = rdk(4); k8 = rdk(5); sC = mfma(k1, qf, zero); issue_d(c, 13); issue_d(c, 14); SB();
#pragma unroll
            for (int r = 0; r < 16; ++r) sA[r] = dmask(r) ? sB[r] : sA[r];
            PIN(sA); SB();
            sB = mfma(k2, qf, zero); k1 = rdk(6); k2 = rdk(7); SB();
            MAXT(sA); SB();
            sA = mfma(k3, qf, zero); issue_d(c, 15); SB(); MAXT(sC); SB();
            sC = mfma(k0, qf, zero); issue_d(c, 16); SB(); MAXT(sB); SB();
            sB = mfma(k8, qf, zero); issue_d(c, 17); SB(); MAXT(sA); SB();
            sA = mfma(k1, qf, zero); issue_d(c, 18); SB(); MAXT(sC); SB();
            sC = mfma(k2, qf, zero); k0 = rdk(0); k8 = rdk(8); k1 = rdk(1); SB(); MAXT(sB); SB();
            MAXT(sA); SB(); MAXT(sC);
            m = fmaxf(m0, m1);
            { auto rr = __builtin_amdgcn_permlane32_swap(__float_as_uint(m), __float_as_uint(m), false, false); m = fmaxf(__uint_as_float(rr[0]), __uint_as_float(rr[1])); }
            STAMP(9);
            f32x16 cm;
#pragma unroll
            for (int r = 0; r < 16; ++r) cm[r] = -m;
            PIN(cm);
            sA = mfma(k0, qf, cm); sB = mfma(k8, qf, cm); sC = mfma(k1, qf, cm);
            bf16x8 va0, va1, vb0, vb1;
            { const unsigned a0 = vaddr(0), a8 = vaddr(8); va0 = ldsf((lcp)(uintptr_t)a0); va1 = ldsf((lcp)(uintptr_t)(a0 + 1024)); vb0 = ldsf((lcp)(uintptr_t)a8); vb1 = ldsf((lcp)(uintptr_t)(a8 + 1024)); }
            k0 = rdk(2);
            unsigned mp[8];
#pragma unroll
            for (int j = 0; j < 8; ++j) mp[j] = (dmask(2 * j) ? 0xffffu : 0u) | (dmask(2 * j + 1) ? 0xffff0000u : 0u);
            SB();
#pragma unroll
            for (int r = 0; r < 16; ++r) sA[r] = dmask(r) ? sB[r] : sA[r];
            LDH(13); issue_d(c, 19);
            EXPT(sA);
            { const u32x4 p0 = pack8(sA, 0), p1 = pack8(sA, 8); u32x4 d0, d1, o0, o1;
#pragma unroll
              for (int j = 0; j < 4; ++j) { d0[j] = p0[j] & mp[j]; d1[j] = p1[j] & mp[4 + j]; o0[j] = p0[j] & ~mp[j]; o1[j] = p1[j] & ~mp[4 + j]; }
              o = mfma(va0, __builtin_bit_cast(bf16x8, o0), o); o = mfma(va1, __builtin_bit_cast(bf16x8, o1), o);
              o = mfma(vb0, __builtin_bit_cast(bf16x8, d0), o); o = mfma(vb1, __builtin_bit_cast(bf16x8, d1), o); }
            { const unsigned a1 = vaddr(1), a2 = vaddr(2); va0 = ldsf((lcp)(uintptr_t)a1); va1 = ldsf((lcp)(uintptr_t)(a1 + 1024)); vb0 = ldsf((lcp)(uintptr_t)a2); vb1 = ldsf((lcp)(uintptr_t)(a2 + 1024)); }
            SB();
#define TILE2(i, S, V0, V1, SN) do { LDH(13 + (i)); if ((i) < 7) issue_d(c, 19 + (i)); \
                if ((i) + 1 <= 7) { SN = mfma(k0, qf, cm); if ((i) + 2 <= 7) k0 = rdk((i) + 2); } \
                EXPT(S); o = mfma(V0, __builtin_bit_cast(bf16x8, pack8(S, 0)), o); o = mfma(V1, __builtin_bit_cast(bf16x8, pack8(S, 8)), o); \
                if ((i) + 2 <= 7) { const unsigned a_ = vaddr((i) + 2); V0 = ldsf((lcp)(uintptr_t)a_); V1 = ldsf((lcp)(uintptr_t)(a_ + 1024)); } SB(); } while (0)
            TILE2(1, sC, va0, va1, sB); TILE2(2, sB, vb0, vb1, sC); TILE2(3, sC, va0, va1, sB); TILE2(4, sB, vb0, vb1, sC);
            TILE2(5, sC, va0, va1, sB); TILE2(6, sB, vb0, vb1, sC); TILE2(7, sC, va0, va1, sB);
            LDH(21);
        }
        const float inv = __builtin_amdgcn_rcpf(o[8]);
        f32x16 y;
#pragma unroll
        for (int r = 0; r < 8; ++r) y[r] = o[r] * inv;
        const unsigned oo = (unsigned)(WS_C + ((size_t)T * NKS + 86 + 2 * c.cg + c.g) * 1024) + lane * 16;
        WAITV(0);
        __builtin_amdgcn_raw_buffer_store_b128(pack8(y, 0), wsr, oo, 0, 16);
    }
    STAMP(10);
    {
        const int lane = opaque(lane2), h = lane >> 5, r32 = lane & 31;
        const int tok = 64 * c.tb + 32 * th + r32;
        SYNC(63);
        issue_d(c, 26); issue_d(c, 27); issue_d(c, 28); issue_d(c, 29);
        STAMP(11);
        RDW(13); RDW(14); RDW(15);
        uint2 xa, xb2;
        const int nt = (w >> 1) & 1, rh = w >> 2;
        const int n0 = 64 * c.nb + 32 * nt + 16 * rh + 4 * h;
        G2STEP(13, hf[13], 16); G2STEP(14, hf[14], 17); G2STEP(15, hf[15], 18);
        WAITV(4);
        { unsigned old = 0; if (lane == 0) old = __hip_atomic_fetch_add((LAS unsigned*)(c.lds + L_CNT + 4), 1u, __ATOMIC_RELAXED, __HIP_MEMORY_SCOPE_WORKGROUP);
          if (lane == 0 && old == 7) store_flag(ws, WS_OF + ((size_t)c.rb * 16 + c.cg) * 8, tag); }
        SB();
        { const u16* xr = P.x + (size_t)tok * 512 + n0; xa = *(const uint2*)xr; xb2 = *(const uint2*)(xr + 8); }
        G2STEP(16, hf[16], 19); G2STEP(17, hf[17], 20); G2STEP(18, hf[18], 21); G2STEP(19, hf[19], 22); G2STEP(20, hf[20], 23);
        if (kq < 2) { acc0 = mfma(wa[21 % 3], hf[21], acc0); acc1 = mfma(wb_[21 % 3], hf[21], acc1); }
        SB(); RDW(24); SB();
        STAMP(12);
        poll_flags((const u64*)(ws + WS_OF) + rb2 * 16, 16, tag, lane, ws);
        bf16x8 of[8];
#pragma unroll
        for (int i = 0; i < 8; ++i) of[i] = __builtin_bit_cast(bf16x8, __builtin_amdgcn_raw_buffer_load_b128(wsr, cbase + (86 + 4 * i) * 1024, 0, 16));
        SYNC(63);
        STAMP(13);
        G2STEP(22, of[0], 25); G2STEP(23, of[1], 26); G2STEP(24, of[2], 27); G2STEP(25, of[3], 28); G2STEP(26, of[4], 29); G2STEP(27, of[5], -1); G2STEP(28, of[6], -1); G2STEP(29, of[7], -1);
        SYNC(63);
        { lp rbp = c.lds + (kq * 2 + th) * 8192 + lane * 16;
#pragma unroll
          for (int q = 0; q < 4; ++q) { *(LAS f32x4*)(rbp + q * 1024) = F4(acc0, 4 * q); *(LAS f32x4*)(rbp + 4096 + q * 1024) = F4(acc1, 4 * q); } }
        SYNC(63);
        { f32x4 s0 = {0.f, 0.f, 0.f, 0.f}, s1 = s0;
#pragma unroll
          for (int k = 0; k < 4; ++k) { const lcp bp = (lcp)c.lds + (k * 2 + th) * 8192 + nt * 4096 + 2 * rh * 1024 + lane * 16; s0 += ldsf4(bp); s1 += ldsf4(bp + 1024); }
          uint2 oa, ob;
          oa.x = pk2(s0[0] + blo(xa.x), s0[1] + bhi(xa.x)); oa.y = pk2(s0[2] + blo(xa.y), s0[3] + bhi(xa.y));
          ob.x = pk2(s1[0] + blo(xb2.x), s1[1] + bhi(xb2.x)); ob.y = pk2(s1[2] + blo(xb2.y), s1[3] + bhi(xb2.y));
          u16* orow = P.out + (size_t)tok * 512 + n0;
          *(uint2*)orow = oa; *(uint2*)(orow + 8) = ob; }
    }
    STAMP(14);
    if (blockIdx.x == 0 && w == 7 && *(volatile LAS unsigned*)(c.lds + L_CNT + 8) == 0u) {
#pragma unroll
        for (int j = 0; j < 4; ++j) poll_flags((const u64*)(ws + WS_HF) + 64 * j, 64, tag, lane, ws);
        if (lane == 0) store_flag(ws, WS_EPOCH, tag);
    }
}

extern "C" void kernel_launch(void* const* d_in, const int* in_sizes, int n_in, void* d_out, int out_size, void* d_ws, size_t ws_size, hipStream_t stream) {
    Params p;
    p.x = (const u16*)d_in[0]; p.wqkv = (const u16*)d_in[1]; p.wo = (const u16*)d_in[2];
    p.wg = (const u16*)d_in[4]; p.wu = (const u16*)d_in[5]; p.wd = (const u16*)d_in[6];
    p.out = (u16*)d_out; p.ws = (char*)d_ws;
    (void)hipFuncSetAttribute((const void*)fused_kernel, hipFuncAttributeMaxDynamicSharedMemorySize, LDS_BYTES);
    fused_kernel<<<256, 512, LDS_BYTES, stream>>>(p);
}
```

```cpp
#include <hip/hip_runtime.h>
#include <stdint.h>
#include <type_traits>

#define FI __device__ __forceinline__
#define LAS __attribute__((address_space(3)))
#define GAS __attribute__((address_space(1)))
typedef unsigned short u16;
typedef unsigned long long u64;
typedef short bf16x8 __attribute__((ext_vector_type(8)));
typedef short s16x4 __attribute__((ext_vector_type(4)));
typedef float f32x16 __attribute__((ext_vector_type(16)));
typedef float f32x4 __attribute__((ext_vector_type(4)));
typedef float f32x2 __attribute__((ext_vector_type(2)));
typedef unsigned u32x4 __attribute__((ext_vector_type(4)));
typedef __bf16 bf16x2_t __attribute__((ext_vector_type(2)));
typedef LAS const char* lcp;
typedef LAS char* lp;

constexpr int GR = 8192;
constexpr int L_KV = 13 * GR;
constexpr int L_KVR = 17 * GR;
constexpr int L_MISC = 19 * GR;
constexpr int L_CS = L_MISC;
constexpr int L_CSW = L_MISC + 2048;
constexpr int L_ZERO = L_MISC + 4096;
constexpr int L_PART = 6 * GR;
constexpr int L_ONES = L_MISC + 6144;
constexpr int L_CNT = L_ONES + 64;
constexpr int L_FINE = L_MISC + 6400;
constexpr int LDS_BYTES = 163840;

constexpr size_t WS_EPOCH = 0, WS_TMO = 64;
constexpr size_t WS_SF = 4096;
constexpr size_t WS_KVF = 8192;
constexpr size_t WS_HF = 24576;
constexpr size_t WS_OF = 40960;
constexpr size_t WS_ST = 65536;
constexpr size_t WS_CS = 131072;
constexpr size_t WS_SCR = 1u << 19;
constexpr size_t WS_KV = 1u << 20;
constexpr size_t WS_C = 8u << 20;
constexpr size_t WS_DBG = 32u << 20;
constexpr int NKS = 118;
constexpr unsigned CODE_PF_BYTES = 43008;

FI f32x16 mfma(bf16x8 a, bf16x8 b, f32x16 c) { return __builtin_amdgcn_mfma_f32_32x32x16_bf16(a, b, c, 0, 0, 0); }
FI f32x16 mfma_pin(bf16x8 a, bf16x8 b, const f32x16& c) { f32x16 d; asm volatile("v_mfma_f32_32x32x16_bf16 %0, %1, %2, %3" : "=&v"(d) : "v"(a), "v"(b), "v"(c)); return d; }
FI f32x16 mfma_pin0(bf16x8 a, bf16x8 b) { f32x16 d; asm volatile("v_mfma_f32_32x32x16_bf16 %0, %1, %2, 0" : "=&v"(d) : "v"(a), "v"(b)); return d; }
FI unsigned pk2(float lo, float hi) { f32x2 v = {lo, hi}; bf16x2_t r = __builtin_convertvector(v, bf16x2_t); return __builtin_bit_cast(unsigned, r); }
FI u32x4 pack8(const f32x16& y, int b) { u32x4 r; r[0] = pk2(y[b], y[b + 1]); r[1] = pk2(y[b + 2], y[b + 3]); r[2] = pk2(y[b + 4], y[b + 5]); r[3] = pk2(y[b + 6], y[b + 7]); return r; }
typedef short v4i16_t __attribute__((ext_vector_type(4)));
FI s16x4 trd(lcp p) { return __builtin_bit_cast(s16x4, __builtin_amdgcn_ds_read_tr16_b64_v4i16((LAS v4i16_t*)p)); }
template <int D2> FI bf16x8 trfrag(lcp p) { s16x4 lo = trd(p), hi = trd(p + D2); return __builtin_shufflevector(lo, hi, 0, 1, 2, 3, 4, 5, 6, 7); }
FI bf16x8 ldsf(lcp p) { return *(const LAS bf16x8*)p; }
FI f32x4 ldsf4(lcp p) { return *(const LAS f32x4*)p; }

FI void dma16(unsigned voff, const void* sbase, unsigned dst) {
    asm volatile("s_mov_b32 m0, %2\n\ts_nop 0\n\tglobal_load_lds_dwordx4 %0, %1" :: "v"(voff), "s"(sbase), "s"(__builtin_amdgcn_readfirstlane(dst)) : "memory", "m0");
}
FI void dma16_sc1(unsigned voff, const void* sbase, unsigned dst) {
    asm volatile("s_mov_b32 m0, %2\n\ts_nop 0\n\tglobal_load_lds_dwordx4 %0, %1 sc1" :: "v"(voff), "s"(sbase), "s"(__builtin_amdgcn_readfirstlane(dst)) : "memory", "m0");
}
FI void aload16(u32x4& v, const void* base, unsigned voff) { asm volatile("global_load_dwordx4 %0, %1, %2" : "=v"(v) : "v"(voff), "s"(base) : "memory"); }
FI void aload16_sc1(u32x4& v, const void* base, unsigned voff) { asm volatile("global_load_dwordx4 %0, %1, %2 sc1" : "=v"(v) : "v"(voff), "s"(base) : "memory"); }
FI void aload_flag(u64& v, const char* base, unsigned voff) { asm volatile("global_load_dwordx2 %0, %1, %2 sc1" : "=v"(v) : "v"(voff), "s"(base) : "memory"); }

FI void aprefetch(unsigned& d, const char* p) { asm volatile("global_load_dword %0, %1, off" : "+v"(d) : "v"(p) : "memory"); }
#ifndef EXIT_AT
#define EXIT_AT 99
#endif
#define SB() __builtin_amdgcn_sched_barrier(0)
#define PIN(x) asm volatile("" : "+v"(x))
FI int opaque(int v) { asm volatile("" : "+v"(v)); return v; }
#define FST() do {} while (0)
#define SYNC(N) do { SB(); FST(); asm volatile("s_waitcnt vmcnt(" #N ") lgkmcnt(0)\n\ts_barrier" ::: "memory"); FST(); SB(); } while (0)
#define SYNCV(N) do { SB(); FST(); asm volatile("s_waitcnt vmcnt(" #N ")\n\ts_barrier" ::: "memory"); FST(); SB(); } while (0)
#define WAITV(N) do { SB(); asm volatile("s_waitcnt vmcnt(" #N ")" ::: "memory"); SB(); } while (0)

#define STAMP(k) do {} while (0)

struct Params {
    const u16* x; const u16* wqkv; const u16* wo; const u16* wg; const u16* wu; const u16* wd; u16* out; char* ws;
};

constexpr int wslot(int s) { return (s == 0 ? 4 : s == 1 ? 11 : s == 2 ? 0 : s == 3 ? 7 : s == 4 ? 10 : s == 5 ? 13 : s == 6 ? 0 : s == 7 ? 3 : s == 8 ? 6 : s == 9 ? 9 : s == 10 ? 0 : 3) * GR; }
constexpr int xslot(int c) { return (c == 0 ? 0 : c == 1 ? 2 : c == 2 ? 7 : c == 3 ? 9 : c == 4 ? 14 : c == 5 ? 17 : c == 6 ? 3 : 5) * GR; }
FI constexpr int dloc0(int u) { return (u == 0 ? 12 : u < 4 ? 6 + (u - 1) : u < 7 ? 9 + (u - 4) : u < 9 ? u - 7 : 2 + (u - 9)) * GR; }
FI constexpr int dloc(int u) { return u < 13 ? dloc0(u) : u < 19 ? L_KV + (u - 13) * GR : dloc0(u - 19); }

struct Ctx {
    const Params* p;
    unsigned lds0;
    lp lds;
    int lane, w, tm, g, rb, cg, tb, nb;
    unsigned vq, vf0, vf1, vf2, vx, vlin, vd;
    const char *px, *pq, *pf, *pwd, *pwo;
};

FI void issue_x1(const Ctx& c, int chunk, int k) {
    dma16(k ? c.vx ^ 64u : c.vx, c.px + chunk * 128 + k * 8 * 1024, c.lds0 + xslot(chunk) + 16 * c.w * 128 + k * 8 * 128);
}
FI void issue_x(const Ctx& c, int chunk) { issue_x1(c, chunk, 0); issue_x1(c, chunk, 1); }
template <int S> FI void issue_w1(const Ctx& c, int k) {
    if constexpr (S >= 4) {
        const char* base = c.pf + (size_t)(64 * (S - 4)) * 2752;
        dma16(k == 0 ? c.vf0 : k == 1 ? c.vf1 : c.vf2, base, c.lds0 + wslot(S) + c.g * 12288 + c.tm * 3072 + k * 1024);
    } else {
        const char* base = c.pq + (size_t)(128 * S) * 3072;
        dma16(c.vq, base + (k == 0 ? 1024 : k == 1 ? 2048 : 0), c.lds0 + wslot(S) + c.w * 1024 + k * 8192);
    }
}
template <int S> FI void issue_w(const Ctx& c) { issue_w1<S>(c, 0); issue_w1<S>(c, 1); issue_w1<S>(c, 2); }
FI void issue_d(const Ctx& c, int u) {
    const char* base;
    if (u < 21) base = c.pwd + (size_t)u * 65536;
    else if (u == 21) base = c.pwd + (size_t)21 * 65536 - (c.w >= 4 ? 32768 : 0);
    else base = c.pwo + (size_t)(u - 22) * 65536;
    dma16(c.vd, base, c.lds0 + dloc(u) + c.w * 1024);
}

FI float blo(unsigned d) { return __uint_as_float(d << 16); }
FI float bhi(unsigned d) { return __uint_as_float(d & 0xffff0000u); }
template <int CTRL> FI float dppadd(float x) { return x + __builtin_bit_cast(float, __builtin_amdgcn_update_dpp(0, __builtin_bit_cast(int, x), CTRL, 0xf, 0xf, true)); }
FI void swap32add(float& u, float v) { auto rr = __builtin_amdgcn_permlane32_swap(__float_as_uint(u), __float_as_uint(v), false, false); u = __uint_as_float(rr[0]) + __uint_as_float(rr[1]); }
FI void swap16add(float& u, float v) { auto rr = __builtin_amdgcn_permlane16_swap(__float_as_uint(u), __float_as_uint(v), false, false); u = __uint_as_float(rr[0]) + __uint_as_float(rr[1]); }

FI f32x4 m4(s16x4 a, s16x4 b, f32x4 c) { return __builtin_amdgcn_mfma_f32_4x4x4bf16_1k(a, b, c, 0, 0, 0); }
FI s16x4 lo4(const bf16x8& f) { return __builtin_shufflevector(f, f, 0, 1, 2, 3); }
FI s16x4 hi4(const bf16x8& f) { return __builtin_shufflevector(f, f, 4, 5, 6, 7); }
constexpr s16x4 ONES4 = {0x3f80, 0x3f80, 0x3f80, 0x3f80};
FI void stat4(const bf16x8& f, f32x4& s, f32x4& q) { s = m4(ONES4, lo4(f), s); q = m4(lo4(f), lo4(f), q); s = m4(ONES4, hi4(f), s); q = m4(hi4(f), hi4(f), q); }
FI void sum4(const bf16x8& f, f32x4& s) { s = m4(ONES4, lo4(f), s); s = m4(ONES4, hi4(f), s); }
FI float halfsum(float v) { auto rr = __builtin_amdgcn_permlane32_swap(__float_as_uint(v), __float_as_uint(v), false, false); return __uint_as_float(rr[0]) + __uint_as_float(rr[1]); }
struct G1State {
    bf16x8 xf[32];
    bf16x8 wq[4];
    f32x16 a0, a1, b0, b1;
    unsigned wb[4];
    unsigned xb[4];
    f32x4 cs0, cs1;
};
constexpr int fkind(int G, int f) { return G == 0 ? (f < 8 ? (f & 3) : (f & 1)) : (f < 4 ? (f & 1) : f < 6 ? (f & 1) : f < 8 ? 2 + (f & 1) : f < 10 ? (f & 1) : 2 + (f & 1)); }
constexpr int fpos(int G, int f) { return G == 0 ? (f < 8 ? (f >> 2) : 2 + ((f - 8) >> 1)) : (f < 4 ? (f >> 1) : f < 8 ? 2 : 3); }
constexpr int qkind(int G, int f) { return G == 0 ? ((f == 1 || f == 3 || f == 7 || f == 9) ? 1 : 0) : ((f == 3 || f == 5 || f == 9 || f == 11) ? 1 : 0); }
constexpr int qpos(int G, int f) {
    return G == 0 ? (f == 0 ? 0 : f == 1 ? 0 : f == 2 ? 1 : f == 3 ? 1 : f == 4 ? 2 : f == 5 ? 3 : f == 6 ? 4 : f == 7 ? 4 : f == 8 ? 5 : f == 9 ? 5 : f == 10 ? 6 : 7)
                  : (f == 0 ? 0 : f == 1 ? 1 : f == 2 ? 2 : f == 3 ? 2 : f == 4 ? 3 : f == 5 ? 3 : f == 6 ? 4 : f == 7 ? 5 : f == 8 ? 6 : f == 9 ? 6 : f == 10 ? 7 : 7);
}
template <int G, int S> FI bf16x8 rdfrag(const G1State& st, int f) {
    if constexpr (S >= 4) { const int k = fkind(G, f); return trfrag<768>((lcp)(uintptr_t)(st.wb[fpos(G, f)] + (k & 1) * 12288 + (k >= 2 ? 64 : (G == 0 ? 0 : 128)))); }
    else { const int e = qpos(G, f); return trfrag<256>((lcp)(uintptr_t)(st.wb[e & 3] + (e >> 2) * 4096 + (qkind(G, f) ? 16384 : G * 8192))); }
}
template <int C> FI void rdx(G1State& st) {
#pragma unroll
    for (int e = 0; e < 4; ++e) { st.xf[4 * C + e] = ldsf((lcp)(uintptr_t)st.xb[e]); if (C < 7) st.xb[e] = (unsigned)opaque((int)(st.xb[e] + (xslot(C + 1) - xslot(C)))); }
}
template <int G, int S, bool FRESH, bool PF, class F>
FI void stage(G1State& st, F&& hook) {
    if constexpr (FRESH) {
        if constexpr (S < 4) rdx<(S < 4 ? 2 * S : 0)>(st);
#pragma unroll
        for (int f = 0; f < 4; ++f) st.wq[f] = rdfrag<G, S>(st, f);
        if constexpr (S < 4) rdx<(S < 4 ? 2 * S + 1 : 0)>(st);
        SB();
    }
#pragma unroll
    for (int f = 0; f < 12; ++f) {
        const bf16x8 w = st.wq[f & 3];
        if constexpr (S >= 4) {
            const int k = fkind(G, f); const bf16x8 x = st.xf[4 * (S - 4) + fpos(G, f)];
            if (k == 0) st.a0 = mfma(w, x, st.a0); else if (k == 1) st.a1 = mfma(w, x, st.a1); else if (k == 2) st.b0 = mfma(w, x, st.b0); else st.b1 = mfma(w, x, st.b1);
        } else {
            const bf16x8 x = st.xf[8 * S + qpos(G, f)];
            if (qkind(G, f)) st.b0 = mfma(w, x, st.b0); else if (G == 0) st.a0 = mfma(w, x, st.a0); else st.a0 = mfma(x, w, st.a0);
        }
        SB();
        if (f < 8) st.wq[f & 3] = rdfrag<G, S>(st, f + 4);
        else {
            if (f == 8 && S != 3 && S != 11) { _Pragma("unroll") for (int j = 0; j < 4; ++j) st.wb[j] = (unsigned)opaque((int)(st.wb[j] + (wslot(S + 1) - wslot(S)))); }
            if (PF) st.wq[f & 3] = rdfrag<G, (PF ? S + 1 : S)>(st, f - 8);
        }
        if constexpr (S < 4) {
            if (qkind(G, f) == 0 && (qpos(G, f) & 3) == 0) sum4(w, st.cs0);
            if (qkind(G, f) == 1 && qpos(G, f) == (G == 0 ? 0 : 6)) sum4(w, st.cs1);
        }
        hook(f);
        SB();
    }
}

FI u64 mix64(u64 z) { z += 0x9E3779B97F4A7C15ull; z = (z ^ (z >> 30)) * 0xBF58476D1CE4E5B9ull; z = (z ^ (z >> 27)) * 0x94D049BB133111EBull; return z ^ (z >> 31); }

constexpr unsigned SPIN_CAP = 1u << 18;
FI void set_timeout(char* ws, int lane) { if (lane == 0) __hip_atomic_store((GAS unsigned*)(ws + WS_TMO), 1u, __ATOMIC_RELAXED, __HIP_MEMORY_SCOPE_AGENT); }
FI void poll_flags(const u64* flags, int n, u64 tag, int lane, char* ws) {
    const GAS u64* pp = (const GAS u64*)flags + (lane < n ? lane : 0);
    for (unsigned spins = 0;; ++spins) {
        const u64 v = __hip_atomic_load(pp, __ATOMIC_RELAXED, __HIP_MEMORY_SCOPE_AGENT);
        if (__all(v == tag)) return;
        if (spins > SPIN_CAP) { set_timeout(ws, lane); return; }
        __builtin_amdgcn_s_sleep(2);
    }
}
FI void store_flag(char* ws, size_t off, u64 tag) { __hip_atomic_store((GAS u64*)(ws + off), tag, __ATOMIC_RELAXED, __HIP_MEMORY_SCOPE_AGENT); }

__global__ __launch_bounds__(512, 2) void fused_kernel(Params P) {
    extern __shared__ __attribute__((aligned(16))) char smem[];
    Ctx c; c.p = &P;
    const int tid = threadIdx.x; c.lane = tid & 63;
    unsigned pfd = 0;
    { const char* pc = (const char*)__builtin_amdgcn_s_getpc(); unsigned off = ((unsigned)(blockIdx.x >> 3) * 16u + (tid & 15)) * 128u; if (off > CODE_PF_BYTES) off = CODE_PF_BYTES; if (tid < 16) aprefetch(pfd, pc + off); }
    const int w = __builtin_amdgcn_readfirstlane(tid >> 6);
    c.w = w; c.tm = w & 3; c.g = w >> 2;
    c.lds = (lp)smem; c.lds0 = (unsigned)(uintptr_t)c.lds;
    const int xcd = blockIdx.x & 7, idx = blockIdx.x >> 3;
    c.rb = 4 * (xcd >> 1) + (idx >> 3); c.cg = 8 * (xcd & 1) + (idx & 7);
    c.tb = 4 * xcd + (idx >> 3); c.nb = idx & 7;
    const int fs = c.cg < 11 ? 3 * c.cg : c.cg < 15 ? 33 + 2 * (c.cg - 11) : 40;
    const bool v0 = c.cg != 15, v2 = c.cg < 11 || c.cg == 15;
    const int lane = c.lane, h = lane >> 5, r32 = lane & 31;
    char* ws = P.ws;
    STAMP(0);
    c.px = (const char*)P.x + (size_t)(128 * c.rb + 16 * w) * 1024;
    c.pf = (const char*)(c.g ? P.wu : P.wg) + (size_t)(16 * c.tm) * 2752 + fs * 64;
    u64 tag = 0; unsigned tagLo = 0, tagHi = 0; float ln_a = 0.f, ln_m = 0.f;
    const int T = 4 * c.rb + c.tm;
    const __amdgpu_buffer_rsrc_t wsr = __builtin_amdgcn_make_buffer_rsrc(ws, 0, 32u << 20, 0x00020000);
    c.pq = (const char*)P.wqkv + ((size_t)(16 * w) * 1536 + 32 * c.cg) * 2;
    c.pwd = (const char*)P.wd + (size_t)(8 * w) * 1024 + c.nb * 128;
    c.pwo = (const char*)P.wo + (size_t)(8 * w) * 1024 + c.nb * 128;
    u32x4 g0a, g0b;
#define GATHER_OFFS(tile) const int gl_ = opaque(lane); const unsigned go0 = (unsigned)(WS_CS + (c.cg * 9 + (tile)) * 2048 + (gl_ >> 3) * 256 + (gl_ & 7) * 32)
#define GATHER_ISSUE(tile, ST) do { GATHER_OFFS(tile); aload16_sc1(g0a, P.ws, go0); aload16_sc1(g0b, P.ws + 16, go0); } while (0)
#define GATHER_OK(ST) (g0a[1] == tagLo && g0a[3] == tagLo && g0b[1] == tagLo && g0b[3] == tagLo)
#define GATHER_RED(tile) do { const int gl_ = opaque(lane); float v0_ = __uint_as_float(g0a[0]), v1_ = __uint_as_float(g0a[2]), v2_ = __uint_as_float(g0b[0]), v3_ = __uint_as_float(g0b[2]); \
        swap32add(v0_, v2_); swap32add(v1_, v3_); swap16add(v0_, v1_); v0_ = dppadd<0x128>(v0_); \
        if ((gl_ & 8) == 0) *(LAS float*)(c.lds + L_CS + (tile) * 128 + (4 * (gl_ & 7) + 2 * (gl_ >> 5) + ((gl_ >> 4) & 1)) * 4) = v0_; } while (0)
#define GATHER_CHECK(tile, ST) do { asm volatile("" : "+v"(g0a), "+v"(g0b)); \
        if (!__all(GATHER_OK(ST))) { GATHER_OFFS(tile); \
            for (unsigned spins = 0;; ++spins) { __builtin_amdgcn_s_sleep(2); \
                g0a = __builtin_amdgcn_raw_buffer_load_b128(wsr, go0, 0, 16); g0b = __builtin_amdgcn_raw_buffer_load_b128(wsr, go0 + 16, 0, 16); __builtin_amdgcn_s_waitcnt(0x0F70); \
                if (__all(GATHER_OK(ST))) break; if (spins > SPIN_CAP) { set_timeout(ws, lane); break; } } } \
        } while (0)
#define RD_CS(dst, tile) do { _Pragma("unroll") for (int q_ = 0; q_ < 4; ++q_) { const f32x4 t_ = ldsf4((lcp)c.lds + L_CS + (tile) * 128 + (8 * q_ + 4 * h) * 4); \
            dst[4 * q_] = t_[0]; dst[4 * q_ + 1] = t_[1]; dst[4 * q_ + 2] = t_[2]; dst[4 * q_ + 3] = t_[3]; } } while (0)
#define SPREAD(dst, v, row) do { if (h == 0) *(LAS float*)(c.lds + L_CSW + c.tm * 256 + (row) * 128 + r32 * 4) = (v); \
        _Pragma("unroll") for (int q_ = 0; q_ < 4; ++q_) { const f32x4 t_ = ldsf4((lcp)c.lds + L_CSW + c.tm * 256 + (row) * 128 + (8 * q_ + 4 * h) * 4); \
            dst[4 * q_] = t_[0]; dst[4 * q_ + 1] = t_[1]; dst[4 * q_ + 2] = t_[2]; dst[4 * q_ + 3] = t_[3]; } } while (0)
#define F4(v, b) (f32x4){v[b], v[(b) + 1], v[(b) + 2], v[(b) + 3]}

    const int th = w & 1, kq = w >> 1;
    const int T2 = 2 * c.tb + th;
    bf16x8 qf; bool edone_ = false;
    auto gemm1 = [&](auto Gc) {
        constexpr int G = decltype(Gc)::value;
        const int lane = opaque(c.lane), h = lane >> 5, r32 = lane & 31, tid = w * 64 + lane;
        c.vq = (lane >> 2) * 3072 + (lane & 3) * 16;
        c.vx = (lane >> 3) * 1024 + (((lane & 7) ^ (lane >> 4)) * 16); c.vlin = lane * 16;
        G1State st;
        issue_x(c, 0); issue_x(c, 1); issue_w<0>(c);
        u64 ep; aload_flag(ep, ws + WS_EPOCH, 0u);
        u32x4 ax; aload16(ax, (const char*)P.x + (size_t)(128 * c.rb + 8 * c.cg + w) * 1024, c.vlin);
        u32x4 aw0, aw1, aw2, aw3;
        const int ptile = 3 + 2 * (w < 3 ? w : 0) + (c.rb & 1);
        { const char* tb = (const char*)((c.rb & 1) ? P.wu : P.wg) + 64 * (fs + (w < 3 ? w : 0)) + (size_t)(64 * (c.rb >> 1)) * 2752; const unsigned vo = (lane >> 2) * 2752 + (lane & 3) * 16;
          aw0 = aw1 = aw2 = aw3 = (u32x4){0u, 0u, 0u, 0u};
          if (w < 3) { aload16(aw0, tb, vo); aload16(aw1, tb + 16 * 2752, vo); aload16(aw2, tb + 32 * 2752, vo); aload16(aw3, tb + 48 * 2752, vo); } }
        issue_x(c, 2); issue_x(c, 3); issue_w<1>(c); issue_x(c, 4); issue_x(c, 5);
        unsigned pfw = 0;
        aprefetch(pfw, (const char*)P.wd);
        *(LAS unsigned*)(c.lds + L_ZERO + tid * 4) = 0u;
        if (tid < 4) { *(LAS unsigned*)(c.lds + L_ONES + tid * 4) = 0x3f803f80u; *(LAS unsigned*)(c.lds + L_ONES + 1024 + tid * 4) = 0x3f803f80u; }
        if (tid >= 64 && tid < 72) *(LAS unsigned*)(c.lds + L_CNT + (tid - 64) * 4) = 0u;
        c.vf0 = (lane / 12) * 2752 + (lane % 12) * 16; c.vf1 = ((lane + 64) / 12) * 2752 + ((lane + 64) % 12) * 16; c.vf2 = ((lane + 128) / 12) * 2752 + ((lane + 128) % 12) * 16;
        { const unsigned lb = c.lds0 + (8 * h + ((lane & 15) >> 2)) * 64 + 32 * ((lane >> 4) & 1) + 8 * (lane & 3);
#pragma unroll
          for (int j = 0; j < 4; ++j) st.wb[j] = lb + ((j ^ c.tm) * 1024) + wslot(0); }
        { const int row = 32 * c.tm + r32;
#pragma unroll
          for (int j = 0; j < 4; ++j) st.xb[j] = c.lds0 + xslot(0) + row * 128 + (((2 * (j ^ c.tm) + h) ^ ((row >> 1) & 7)) * 16); }
        st.a0 = f32x16{}; st.a1 = f32x16{}; st.b0 = f32x16{}; st.b1 = f32x16{}; st.cs0 = f32x4{}; st.cs1 = f32x4{};

        SYNC(12);
        PIN(pfd);
        if (EXIT_AT == 0) { tag = mix64(((u64)(unsigned)__builtin_amdgcn_readfirstlane((int)(unsigned)(ep >> 32)) << 32) | (unsigned)__builtin_amdgcn_readfirstlane((int)(unsigned)ep)); if (w == 0 && lane == 0) store_flag(ws, WS_SF + (size_t)blockIdx.x * 8, tag); return; }
        asm volatile("" : "+v"(ep), "+v"(ax), "+v"(aw0), "+v"(aw1), "+v"(aw2), "+v"(aw3));
        tag = mix64(((u64)(unsigned)__builtin_amdgcn_readfirstlane((int)(unsigned)(ep >> 32)) << 32) | (unsigned)__builtin_amdgcn_readfirstlane((int)(unsigned)ep));
        tagLo = (unsigned)tag; tagHi = (unsigned)(tag >> 32);
        STAMP(1);

        float as_ = 0.f, aq_ = 0.f, av[8];
        auto aux0 = [&](int f) {
            if (f < 4) { const float a = blo(ax[f]), b = bhi(ax[f]); as_ += a + b; aq_ = __builtin_fmaf(a, a, aq_); aq_ = __builtin_fmaf(b, b, aq_); PIN(as_); PIN(aq_); }
            if (f == 4) { as_ = dppadd<0x121>(as_); aq_ = dppadd<0x121>(aq_); as_ = dppadd<0x122>(as_); aq_ = dppadd<0x122>(aq_); PIN(as_); PIN(aq_); }
            if (f == 5) { as_ = dppadd<0x124>(as_); aq_ = dppadd<0x124>(aq_); as_ = dppadd<0x128>(as_); aq_ = dppadd<0x128>(aq_); PIN(as_); PIN(aq_); }
            if (f == 6) { swap16add(as_, as_); swap16add(aq_, aq_); swap32add(as_, as_); swap32add(aq_, aq_); PIN(as_); PIN(aq_); }
            if (f == 7) { const float mean = as_ * (1.f / 512.f), var = aq_ * (1.f / 512.f) - mean * mean, rstd = __builtin_amdgcn_rsqf(fmaxf(var, 0.f) + 1e-5f);
                if (lane == 0) __builtin_amdgcn_raw_buffer_store_b128((u32x4){__float_as_uint(mean), tagLo, __float_as_uint(rstd), tagHi}, wsr, (unsigned)(WS_ST + (128 * c.rb + 8 * c.cg + w) * 16), 0, 16); }
            if (f >= 8) { const int i = f - 8; av[2 * i] = (blo(aw0[i]) + blo(aw1[i])) + (blo(aw2[i]) + blo(aw3[i])); av[2 * i + 1] = (bhi(aw0[i]) + bhi(aw1[i])) + (bhi(aw2[i]) + bhi(aw3[i])); PIN(av[2 * i]); PIN(av[2 * i + 1]); }
        };
        auto aux1 = [&](int f) {
            if (f == 8) { swap32add(av[0], av[4]); swap32add(av[1], av[5]); swap32add(av[2], av[6]); swap32add(av[3], av[7]); PIN(av[0]); PIN(av[1]); PIN(av[2]); PIN(av[3]); }
            if (f == 9) { swap16add(av[0], av[2]); swap16add(av[1], av[3]); av[0] = dppadd<0x124>(av[0]); av[1] = dppadd<0x124>(av[1]); av[0] = dppadd<0x128>(av[0]); av[1] = dppadd<0x128>(av[1]); PIN(av[0]); PIN(av[1]); }
        };
        u32x4 gs;
        const int gt2 = w < 6 ? 3 + w : w - 3;
        stage<G, 0, true, false>(st, [&](int f_) { aux0(f_); }); SYNCV(6);
        stage<G, 1, true, false>(st, [&](int f_) { if (f_ < 3) issue_w1<2>(c, f_); else if (f_ < 5) issue_x1(c, 6, f_ - 3); else if (f_ < 7) issue_x1(c, 7, f_ - 5); aux1(f_); }); SYNCV(4);
        PIN(pfw);
        stage<G, 2, true, false>(st, [&](int f_) { if (f_ < 3) issue_w1<3>(c, f_); else if (f_ < 6) issue_w1<4>(c, f_ - 3);
            if (f_ == 9) aload16_sc1(gs, P.ws, (unsigned)(WS_ST + (128 * c.rb + 32 * c.tm + (opaque(lane) & 31)) * 16)); }); SYNCV(4);
        stage<G, 3, true, false>(st, [&](int f_) { if (f_ < 3) issue_w1<5>(c, f_); else if (f_ < 6) issue_w1<6>(c, f_ - 3);
            if (f_ == 10) { WAITV(6); PIN(gs);
                if (!__all(gs[1] == tagLo && gs[3] == tagHi)) { const unsigned gso = (unsigned)(WS_ST + (128 * c.rb + 32 * c.tm + (opaque(lane) & 31)) * 16);
                    for (unsigned spins = 0;; ++spins) { __builtin_amdgcn_s_sleep(2); gs = __builtin_amdgcn_raw_buffer_load_b128(wsr, gso, 0, 16); __builtin_amdgcn_s_waitcnt(0x0F70);
                        if (__all(gs[1] == tagLo && gs[3] == tagHi)) break; if (spins > SPIN_CAP) { set_timeout(ws, lane); break; } } }
                ln_m = __uint_as_float(gs[0]); ln_a = __uint_as_float(gs[2]); PIN(ln_m); PIN(ln_a); } });
        SYNCV(63);
        { lp xp = c.lds + (G == 0 ? 9 : 16) * GR + c.tm * 2048 + c.vlin; *(LAS f32x4*)(xp) = F4(st.b0, 8 - 8 * G); *(LAS f32x4*)(xp + 1024) = F4(st.b0, 12 - 8 * G);
          const float s0 = halfsum(st.cs0[0]), s1 = halfsum(st.cs1[0]);
          if (h == 0) { *(LAS float*)(c.lds + L_PART + (G * 4 + c.tm) * 128 + r32 * 4) = s0; *(LAS float*)(c.lds + L_PART + 1024 + (G * 4 + c.tm) * 128 + r32 * 4) = s1;
                        if constexpr (G == 1) { *(LAS float*)(c.lds + L_CSW + c.tm * 256 + r32 * 4) = ln_m; *(LAS float*)(c.lds + L_CSW + c.tm * 256 + 128 + r32 * 4) = ln_a; } } }
        { const int ln = opaque(lane); const unsigned lb = c.lds0 + (8 * (ln >> 5) + ((ln & 15) >> 2)) * 192 + 32 * ((ln >> 4) & 1) + 8 * (ln & 3);
#pragma unroll
          for (int j = 0; j < 4; ++j) st.wb[j] = lb + ((j ^ c.tm) * 3072) + wslot(4); }
        SYNC(6);
        STAMP(2);
        issue_w<7>(c);
        { const int lane = opaque(c.lane), h = lane >> 5, r32 = lane & 31;
          const unsigned o = (unsigned)(WS_KV + ((size_t)c.cg * 64 + T) * 4096) + lane * 16 + G * 2048;
          f32x16 y;
          if constexpr (G == 0) {
#pragma unroll
              for (int q = 0; q < 4; ++q) { f32x4 cs = ldsf4((lcp)c.lds + L_PART + (8 * q + 4 * h) * 4);
#pragma unroll
                  for (int t = 1; t < 4; ++t) cs += ldsf4((lcp)c.lds + L_PART + t * 128 + (8 * q + 4 * h) * 4);
#pragma unroll
                  for (int i = 0; i < 4; ++i) y[4 * q + i] = ln_a * __builtin_fmaf(-ln_m, cs[i], st.a0[4 * q + i]); }
          } else {
              const lcp pp = (lcp)c.lds + L_PART + 512 + r32 * 4;
              const float cso = (*(const LAS float*)pp + *(const LAS float*)(pp + 128)) + (*(const LAS float*)(pp + 256) + *(const LAS float*)(pp + 384));
#pragma unroll
              for (int q = 0; q < 4; ++q) { const f32x4 mm = ldsf4((lcp)c.lds + L_CSW + c.tm * 256 + (8 * q + 4 * h) * 4), aa = ldsf4((lcp)c.lds + L_CSW + c.tm * 256 + 128 + (8 * q + 4 * h) * 4);
#pragma unroll
                  for (int i = 0; i < 4; ++i) y[4 * q + i] = aa[i] * __builtin_fmaf(-mm[i], cso, st.a0[4 * q + i]); }
          }
          const u32x4 k0 = pack8(y, 0), k1 = pack8(y, 8);
          __builtin_amdgcn_raw_buffer_store_b128(k0, wsr, o, 0, 16); __builtin_amdgcn_raw_buffer_store_b128(k1, wsr, o + 1024, 0, 16);
          if (w < 3) { const int rho = lane >> 4, col = 8 * (lane & 3) + 4 * (rho >> 1) + 2 * (rho & 1);
            if ((lane & 12) == 0) __builtin_amdgcn_raw_buffer_store_b128((u32x4){__float_as_uint(av[0]), tagLo, __float_as_uint(av[1]), tagLo}, wsr, (unsigned)(WS_CS + (((c.cg * 9 + ptile) * 8 + (c.rb >> 1)) * 32 + col) * 8), 0, 16); }
          if (w == 0 && lane == 0) store_flag(ws, WS_SF + (size_t)blockIdx.x * 8, tag);
          *(LAS u32x4*)(c.lds + L_KVR + c.tm * 4096 + G * 2048 + lane * 16) = k0; *(LAS u32x4*)(c.lds + L_KVR + c.tm * 4096 + G * 2048 + 1024 + lane * 16) = k1;
          SB();
          f32x4 c0 = ldsf4((lcp)c.lds + L_PART + 1024 + (16 * G + 4 * h) * 4), c1 = ldsf4((lcp)c.lds + L_PART + 1024 + (16 * G + 8 + 4 * h) * 4);
#pragma unroll
          for (int t = 1; t < 8; ++t) { c0 += ldsf4((lcp)c.lds + L_PART + 1024 + t * 128 + (16 * G + 4 * h) * 4); c1 += ldsf4((lcp)c.lds + L_PART + 1024 + t * 128 + (16 * G + 8 + 4 * h) * 4); }
          const lcp xp = (lcp)c.lds + (G == 0 ? 16 : 9) * GR + c.tm * 2048 + lane * 16;
          const f32x4 p0 = ldsf4(xp), p1 = ldsf4(xp + 1024);
          const float sc = ln_a * (0.25f * 1.4426950408889634f);
#pragma unroll
          for (int i = 0; i < 4; ++i) { y[i] = sc * __builtin_fmaf(-ln_m, c0[i], st.b0[8 * G + i] + p0[i]); y[4 + i] = sc * __builtin_fmaf(-ln_m, c1[i], st.b0[8 * G + 4 + i] + p1[i]); }
          qf = __builtin_bit_cast(bf16x8, pack8(y, 0)); PIN(qf);
          st.a0 = f32x16{}; st.b0 = f32x16{}; }
        if (EXIT_AT == 1) return;
        stage<G, 4, true, false>(st, [&](int) {}); SYNC(5);
        stage<G, 5, true, true>(st, [&](int f_) { if (f_ >= 1 && f_ < 4) issue_w1<8>(c, f_ - 1); else if (f_ >= 4 && f_ < 7) issue_w1<9>(c, f_ - 4); }); SYNCV(8);
        { const int ln = opaque(lane); c.vd = (ln >> 3) * 1024 + (((ln & 7) ^ (((ln >> 4) & 1) << 2)) * 16); }
        stage<G, 6, false, true>(st, [&](int f_) { if (f_ == 1) issue_d(c, 0);
            if (f_ == 8) { WAITV(7); if (lane == 0) store_flag(ws, WS_KVF + ((size_t)(c.cg * 64 + T) * 2 + G) * 8, tag); } }); SYNCV(5);
        stage<G, 7, false, true>(st, [&](int f_) { if (f_ >= 1 && f_ < 4) issue_w1<10>(c, f_ - 1); }); SYNCV(5);
        STAMP(3);
        u64 kf_;
        stage<G, 8, false, true>(st, [&](int f_) { if (f_ >= 1 && f_ < 4) issue_w1<11>(c, f_ - 1); if (f_ == 7) GATHER_ISSUE(gt2, false);
            if (f_ == 8) { const int tl = 4 * c.rb - 8 + w; aload_flag(kf_, P.ws, (unsigned)(WS_KVF + ((c.cg * 64 + (tl < 0 ? 0 : tl)) * 2 + (opaque(lane) & 1)) * 8)); } }); SYNCV(6);
        stage<G, 9, false, true>(st, [&](int f_) { if (f_ >= 1 && f_ < 4) issue_d(c, f_);
            if (f_ == 6) { WAITV(3); GATHER_CHECK(gt2, false); GATHER_RED(gt2); }
            if (f_ == 8) { asm volatile("" : "+v"(kf_)); const int tl = 4 * c.rb - 8 + w;
                if (tl >= 0) {
                    if (!__all(kf_ == tag)) poll_flags((const u64*)(ws + WS_KVF) + (c.cg * 64 + tl) * 2, 2, tag, lane, ws);
                    const char* base = ws + WS_KV + ((size_t)c.cg * 64 + tl) * 4096; const unsigned dst = c.lds0 + L_KV + w * 4096;
                    dma16_sc1(c.vlin, base, dst); dma16_sc1(c.vlin, base + 1024, dst + 1024); dma16_sc1(c.vlin, base + 2048, dst + 2048); dma16_sc1(c.vlin, base + 3072, dst + 3072); } } }); SYNCV(63);
        stage<G, 10, false, true>(st, [&](int f_) { if (f_ >= 1 && f_ < 4) issue_d(c, 3 + f_); }); SYNCV(63);
        STAMP(4);
        stage<G, 11, false, false>(st, [&](int f_) { if (f_ >= 1 && f_ < 3) issue_d(c, 6 + f_); });
        {
          lp xp = c.lds + 2 * GR + w * 4096 + c.vlin;
          SYNCV(63);
          *(LAS f32x4*)(xp) = F4(st.b0, 8 - 8 * G); *(LAS f32x4*)(xp + 1024) = F4(st.b0, 12 - 8 * G); *(LAS f32x4*)(xp + 2048) = F4(st.b1, 8 - 8 * G); *(LAS f32x4*)(xp + 3072) = F4(st.b1, 12 - 8 * G); }
        SYNC(5);
        STAMP(5);
        {
          const int lane = opaque(c.lane), h = lane >> 5;
          const float a2 = ln_a * ln_a;
          { u32x4 y0, y1;
#pragma unroll
            for (int q = 0; q < 4; ++q) {
                const f32x4 cg_ = ldsf4((lcp)c.lds + L_CS + (3 + 4 * G) * 128 + (8 * q + 4 * h) * 4), cu_ = ldsf4((lcp)c.lds + L_CS + (4 + 4 * G) * 128 + (8 * q + 4 * h) * 4);
                float y[4];
#pragma unroll
                for (int i = 0; i < 4; ++i) { const float g_ = fmaxf(__builtin_fmaf(-ln_m, cg_[i], st.a0[4 * q + i]), 0.f), u_ = __builtin_fmaf(-ln_m, cu_[i], st.a1[4 * q + i]); y[i] = (g_ * a2) * u_; }
                const unsigned lo = pk2(y[0], y[1]), hi = pk2(y[2], y[3]);
                if (q < 2) { y0[2 * q] = lo; y0[2 * q + 1] = hi; } else { y1[2 * q - 4] = lo; y1[2 * q - 3] = hi; }
            }
            if (G == 0 ? v0 : v2) { const unsigned o_ = (unsigned)(WS_C + ((size_t)T * NKS + 2 * (fs + 2 * G)) * 1024) + lane * 16;
                __builtin_amdgcn_raw_buffer_store_b128(y0, wsr, o_, 0, 16); __builtin_amdgcn_raw_buffer_store_b128(y1, wsr, o_ + 1024, 0, 16); } }
          { const lcp xp = (lcp)c.lds + 2 * GR + (w ^ 4) * 4096 + lane * 16;
            u32x4 y0;
#pragma unroll
            for (int q = 0; q < 2; ++q) {
                const f32x4 pg = ldsf4(xp + q * 1024), pu = ldsf4(xp + 2048 + q * 1024);
                const f32x4 cg_ = ldsf4((lcp)c.lds + L_CS + 5 * 128 + (16 * G + 8 * q + 4 * h) * 4), cu_ = ldsf4((lcp)c.lds + L_CS + 6 * 128 + (16 * G + 8 * q + 4 * h) * 4);
                float y[4];
#pragma unroll
                for (int i = 0; i < 4; ++i) { const float g_ = fmaxf(__builtin_fmaf(-ln_m, cg_[i], st.b0[8 * G + 4 * q + i] + pg[i]), 0.f), u_ = __builtin_fmaf(-ln_m, cu_[i], st.b1[8 * G + 4 * q + i] + pu[i]); y[i] = (g_ * a2) * u_; }
                y0[2 * q] = pk2(y[0], y[1]); y0[2 * q + 1] = pk2(y[2], y[3]);
            }
            __builtin_amdgcn_raw_buffer_store_b128(y0, wsr, (unsigned)(WS_C + ((size_t)T * NKS + 2 * (fs + 1) + G) * 1024) + lane * 16, 0, 16); } }
        SYNC(63);
        if constexpr (G == 1) __builtin_amdgcn_s_setprio(1);
    };
    if (c.g == 0) gemm1(std::integral_constant<int, 0>{}); else gemm1(std::integral_constant<int, 1>{});
    STAMP(6);
    if (EXIT_AT <= 2) goto done;
    {
    const int lane2 = opaque(lane);
    const unsigned cbase = (unsigned)(WS_C + ((size_t)T2 * NKS + kq) * 1024) + lane2 * 16;
    bf16x8 hf[22];
    u64 hfl;
#define LDH(i) do { const bool ok_ = ((i) < 21) || (kq < 2); hf[i] = __builtin_bit_cast(bf16x8, __builtin_amdgcn_raw_buffer_load_b128(wsr, ok_ ? cbase + (i) * 4096 : cbase, 0, 16)); } while (0)
    {
        const int lane = lane2, h = lane >> 5, r32 = lane & 31;
        const int i0 = T >= 8 ? 0 : 8 - T;
        const unsigned kb = c.lds0 + L_KV + c.tm * 4096 + c.g * 1024 + lane * 16;
        const unsigned lanebit = r32 < 16 ? 1u : 0u;
        const unsigned vb = c.lds0 + (r32 < 16 ? (unsigned)(((r32 & 15) + 16 * c.g + 32 * h) * 16) : (unsigned)L_ONES);
        auto koff = [&](int i) -> unsigned { return (unsigned)((i >= i0 ? i : 7) * 4096); };
        auto rdk = [&](int i) -> bf16x8 { return ldsf((lcp)(uintptr_t)(kb + koff(i))); };
        auto vaddr = [&](int i) -> unsigned { const bool valid = i >= i0; const unsigned d = valid ? 0u : (unsigned)(L_ZERO - L_ONES), A = valid ? (unsigned)(L_KV + 2048 + (c.tm + i) * 4096) : (unsigned)L_ZERO;
                                              return lanebit * (A - d) + (vb + d); };
        auto dmask = [&](int r) -> bool { u64 mk = 0;
#pragma unroll
            for (int l = 0; l < 64; ++l) { const int kl = (r & 3) + 8 * (r >> 2) + 4 * (l >> 5); if (kl <= (l & 31)) mk |= 1ull << l; }
            return __builtin_amdgcn_inverse_ballot_w64(mk); };
        const f32x16 zero = f32x16{};
        float m0 = -1e30f, m1 = -1e30f;
#define MAXT(S) do { _Pragma("unroll") for (int r = 0; r < 16; r += 4) { m0 = fmaxf(fmaxf(m0, S[r]), S[r + 1]); m1 = fmaxf(fmaxf(m1, S[r + 2]), S[r + 3]); } PIN(m0); PIN(m1); } while (0)
#define EXPT(S) do { _Pragma("unroll") for (int r = 0; r < 16; ++r) S[r] = __builtin_amdgcn_exp2f(S[r]); } while (0)
#define H_PUBLISH() do { STAMP(17); WAITV(4); STAMP(18); if (lane == 0) store_flag(ws, WS_HF + ((size_t)T * 32 + 2 * c.cg + c.g) * 8, tag); } while (0)
#define H_FLAGS() aload_flag(hfl, P.ws, (unsigned)(WS_HF + (T2 * 32 + (lane & 31)) * 8))
        f32x16 o = f32x16{};
        float m;
        if (T == 0) {
            f32x16 s = mfma(ldsf((lcp)(uintptr_t)(kb + 8 * 4096)), qf, zero);
#pragma unroll
            for (int r = 0; r < 16; ++r) s[r] = dmask(r) ? s[r] : -1e30f;
            MAXT(s);
            m = fmaxf(m0, m1);
            { auto rr = __builtin_amdgcn_permlane32_swap(__float_as_uint(m), __float_as_uint(m), false, false); m = fmaxf(__uint_as_float(rr[0]), __uint_as_float(rr[1])); }
#pragma unroll
            for (int r = 0; r < 16; ++r) s[r] = __builtin_amdgcn_exp2f(s[r] - m);
            const unsigned va = vaddr(8);
            o = mfma(ldsf((lcp)(uintptr_t)va), __builtin_bit_cast(bf16x8, pack8(s, 0)), o);
            o = mfma(ldsf((lcp)(uintptr_t)(va + 1024)), __builtin_bit_cast(bf16x8, pack8(s, 8)), o);
            issue_d(c, 9); issue_d(c, 10); issue_d(c, 11); issue_d(c, 12);
            H_PUBLISH(); H_FLAGS();
        } else {
            bf16x8 k0 = rdk(0), k8 = rdk(8), k1 = rdk(1), k2 = rdk(2), k3 = rdk(3);
            f32x16 sA, sB, sC;
            sA = mfma_pin0(k0, qf); sB = mfma_pin0(k8, qf); SB();
            k0 = rdk(4); k8 = rdk(5); sC = mfma_pin0(k1, qf); k1 = rdk(6); SB();
            asm volatile("s_nop 7" ::: );
#pragma unroll
            for (int r = 0; r < 16; ++r) sA[r] = dmask(r) ? sB[r] : sA[r];
            PIN(sA); SB();
            sB = mfma_pin0(k2, qf); k2 = rdk(7); SB();
            MAXT(sA); SB();
            sA = mfma_pin0(k3, qf); issue_d(c, 9); SB(); MAXT(sC); SB();
            sC = mfma_pin0(k0, qf); issue_d(c, 10); SB(); MAXT(sB); SB();
            sB = mfma_pin0(k8, qf); issue_d(c, 11); SB(); MAXT(sA); SB();
            sA = mfma_pin0(k1, qf); issue_d(c, 12); SB(); MAXT(sC); SB();
            sC = mfma_pin0(k2, qf); k0 = rdk(0); k8 = rdk(8); k1 = rdk(1); SB(); MAXT(sB); SB();
            MAXT(sA); SB(); MAXT(sC);
            m = fmaxf(m0, m1);
            { auto rr = __builtin_amdgcn_permlane32_swap(__float_as_uint(m), __float_as_uint(m), false, false); m = fmaxf(__uint_as_float(rr[0]), __uint_as_float(rr[1])); }
            STAMP(9);
            f32x16 cm;
#pragma unroll
            for (int r = 0; r < 16; ++r) cm[r] = -m;
            PIN(cm);
            asm volatile("s_nop 1" ::: ); sA = mfma_pin(k0, qf, cm); sB = mfma_pin(k8, qf, cm); sC = mfma_pin(k1, qf, cm); SB();
            bf16x8 va0, va1, vb0, vb1;
            { const unsigned a0 = vaddr(0), a8 = vaddr(8); va0 = ldsf((lcp)(uintptr_t)a0); va1 = ldsf((lcp)(uintptr_t)(a0 + 1024)); vb0 = ldsf((lcp)(uintptr_t)a8); vb1 = ldsf((lcp)(uintptr_t)(a8 + 1024)); }
            k0 = rdk(2);
            unsigned mp[8];
#pragma unroll
            for (int j = 0; j < 8; ++j) mp[j] = (dmask(2 * j) ? 0xffffu : 0u) | (dmask(2 * j + 1) ? 0xffff0000u : 0u);
            SB();
#pragma unroll
            for (int r = 0; r < 16; ++r) sA[r] = dmask(r) ? sB[r] : sA[r];
            EXPT(sA);
            { const u32x4 p0 = pack8(sA, 0), p1 = pack8(sA, 8); u32x4 d0, d1, o0, o1;
#pragma unroll
              for (int j = 0; j < 4; ++j) { d0[j] = p0[j] & mp[j]; d1[j] = p1[j] & mp[4 + j]; o0[j] = p0[j] & ~mp[j]; o1[j] = p1[j] & ~mp[4 + j]; }
              o = mfma(va0, __builtin_bit_cast(bf16x8, o0), o); o = mfma(va1, __builtin_bit_cast(bf16x8, o1), o);
              o = mfma(vb0, __builtin_bit_cast(bf16x8, d0), o); o = mfma(vb1, __builtin_bit_cast(bf16x8, d1), o); }
            { const unsigned a1 = vaddr(1), a2 = vaddr(2); va0 = ldsf((lcp)(uintptr_t)a1); va1 = ldsf((lcp)(uintptr_t)(a1 + 1024)); vb0 = ldsf((lcp)(uintptr_t)a2); vb1 = ldsf((lcp)(uintptr_t)(a2 + 1024)); }
            SB();
#define EXP8(S, b) do { _Pragma("unroll") for (int r = (b); r < (b) + 8; ++r) S[r] = __builtin_amdgcn_exp2f(S[r]); } while (0)
#define PK4(S, b) (u32x4){pk2(S[b], S[(b) + 1]), pk2(S[(b) + 2], S[(b) + 3]), pk2(S[(b) + 4], S[(b) + 5]), pk2(S[(b) + 6], S[(b) + 7])}
#define TILE2(i, S, V0, V1, SN) do { \
                if ((i) + 1 <= 7) { SN = mfma_pin(k0, qf, cm); SB(); if ((i) + 2 <= 7) k0 = rdk((i) + 2); } \
                EXP8(S, 0); { u32x4 p_ = PK4(S, 0); PIN(p_); SB(); o = mfma(V0, __builtin_bit_cast(bf16x8, p_), o); SB(); } \
                EXP8(S, 8); { u32x4 p_ = PK4(S, 8); PIN(p_); SB(); o = mfma(V1, __builtin_bit_cast(bf16x8, p_), o); SB(); } \
                if ((i) + 2 <= 7) { const unsigned a_ = vaddr((i) + 2); V0 = ldsf((lcp)(uintptr_t)a_); V1 = ldsf((lcp)(uintptr_t)(a_ + 1024)); } SB(); } while (0)
            H_PUBLISH();
            TILE2(1, sC, va0, va1, sB); TILE2(2, sB, vb0, vb1, sC); TILE2(3, sC, va0, va1, sB); TILE2(4, sB, vb0, vb1, sC);
            TILE2(5, sC, va0, va1, sB);
            H_FLAGS();
            TILE2(6, sB, vb0, vb1, sC); TILE2(7, sC, va0, va1, sB);
        }
        const float inv = __builtin_amdgcn_rcpf(o[8]);
        f32x16 y;
#pragma unroll
        for (int r = 0; r < 8; ++r) y[r] = o[r] * inv;
        const unsigned oo = (unsigned)(WS_C + ((size_t)T * NKS + 86 + 2 * c.cg + c.g) * 1024) + lane * 16;
        const u32x4 ov = pack8(y, 0);
        STAMP(28);
        if (EXIT_AT == 3) { __builtin_amdgcn_raw_buffer_store_b128(ov, wsr, oo, 0, 16); goto done; }
        __builtin_amdgcn_raw_buffer_store_b128(ov, wsr, oo, 0, 16);
        WAITV(1); asm volatile("" : "+v"(hfl));
        STAMP(15);
        if (!__all(hfl == tag)) poll_flags((const u64*)(ws + WS_HF) + T2 * 32, 32, tag, lane, ws);
        STAMP(16);
#pragma unroll
        for (int i = 0; i < 22; ++i) LDH(i);
        __builtin_amdgcn_s_setprio(0);
    }
    STAMP(10);
    {
        const int lane = opaque(lane2), h = lane >> 5, r32 = lane & 31, q4 = (lane & 15) >> 2, p4 = lane & 3, b16 = (lane >> 4) & 1;
        const int tok = 64 * c.tb + 32 * th + r32;
        f32x16 acc0 = f32x16{}, acc1 = f32x16{};
        const unsigned wl = c.lds0 + kq * 2048 + (4 * h + q4) * 128 + 32 * b16 + 8 * p4, swz = ((q4 >> 1) & 1) << 6;
        const unsigned wl0 = wl + (0u ^ swz), wl1 = wl + (64u ^ swz);
        bf16x8 wa[3], wb_[3];
#define RDW(u) do { wa[(u) % 3] = trfrag<1024>((lcp)(uintptr_t)(wl0 + dloc(u))); wb_[(u) % 3] = trfrag<1024>((lcp)(uintptr_t)(wl1 + dloc(u))); } while (0)
#define G2STEP(u, frag, nxt) do { acc0 = mfma(wa[(u) % 3], frag, acc0); acc1 = mfma(wb_[(u) % 3], frag, acc1); SB(); if ((nxt) >= 0) RDW((nxt) < 0 ? 0 : (nxt)); SB(); } while (0)
        SYNC(63);
        STAMP(11);
        if (EXIT_AT == 4) goto done;
        const bool ew = blockIdx.x == 0 && w == 7; u32x4 sf0 = {0u, 0u, 0u, 0u}, sf1 = sf0; bool edone = false;
        if (ew) { aload16_sc1(sf0, P.ws + WS_SF, (unsigned)(lane * 32)); aload16_sc1(sf1, P.ws + WS_SF + 16, (unsigned)(lane * 32)); }
        RDW(0); RDW(1); RDW(2);
        G2STEP(0, hf[0], 3);
        if (lane == 0) store_flag(ws, WS_OF + ((size_t)T * 32 + 2 * c.cg + c.g) * 8, tag);
        issue_d(c, 13); G2STEP(1, hf[1], 4); issue_d(c, 14); G2STEP(2, hf[2], 5); issue_d(c, 15); G2STEP(3, hf[3], 6); issue_d(c, 16); G2STEP(4, hf[4], 7); issue_d(c, 17); G2STEP(5, hf[5], 8); issue_d(c, 18);
        SYNCV(63);
        G2STEP(6, hf[6], 9); issue_d(c, 19); G2STEP(7, hf[7], 10); issue_d(c, 20); G2STEP(8, hf[8], 11); issue_d(c, 21); G2STEP(9, hf[9], 12); issue_d(c, 22);
        G2STEP(10, hf[10], -1); issue_d(c, 23); G2STEP(11, hf[11], -1); issue_d(c, 24); G2STEP(12, hf[12], -1);
        STAMP(24);
        SYNCV(6);
        STAMP(25);
        if (ew) { asm volatile("" : "+v"(sf0), "+v"(sf1));
            edone = __all(sf0[0] == tagLo && sf0[1] == tagHi && sf0[2] == tagLo && sf0[3] == tagHi && sf1[0] == tagLo && sf1[1] == tagHi && sf1[2] == tagLo && sf1[3] == tagHi);
            if (edone && lane == 0) store_flag(ws, WS_EPOCH, tag); }
        edone_ = edone;
        RDW(13); RDW(14); RDW(15);
        uint2 xa, xb2;
        const int nt = (w >> 1) & 1, rh = w >> 2;
        const int n0 = 64 * c.nb + 32 * nt + 16 * rh + 4 * h;
        u64 ofl; aload_flag(ofl, P.ws, (unsigned)(WS_OF + (T2 * 32 + (lane & 31)) * 8));
        issue_d(c, 25); G2STEP(13, hf[13], 16); issue_d(c, 26); G2STEP(14, hf[14], 17); issue_d(c, 27); G2STEP(15, hf[15], 18); issue_d(c, 28); G2STEP(16, hf[16], -1); issue_d(c, 29);
        WAITV(5); asm volatile("" : "+v"(ofl));
        if (!__all(ofl == tag)) poll_flags((const u64*)(ws + WS_OF) + T2 * 32, 32, tag, lane, ws);
        STAMP(26);
        bf16x8 of[8];
#pragma unroll
        for (int i = 0; i < 8; ++i) of[i] = __builtin_bit_cast(bf16x8, __builtin_amdgcn_raw_buffer_load_b128(wsr, cbase + (86 + 4 * i) * 1024, 0, 16));
        { const u16* xr = P.x + (size_t)tok * 512 + n0; xa = *(const uint2*)xr; xb2 = *(const uint2*)(xr + 8); }
        G2STEP(17, hf[17], -1); G2STEP(18, hf[18], -1);
        STAMP(12);
        SYNCV(63);
        RDW(19); RDW(20); RDW(21);
        G2STEP(19, hf[19], 22); G2STEP(20, hf[20], 23);
        if (kq < 2) { acc0 = mfma(wa[21 % 3], hf[21], acc0); acc1 = mfma(wb_[21 % 3], hf[21], acc1); }
        SB(); RDW(24); SB();
        STAMP(13);
        G2STEP(22, of[0], -1); G2STEP(23, of[1], -1); G2STEP(24, of[2], -1);
        SYNCV(63);
        RDW(25); RDW(26); RDW(27);
        G2STEP(25, of[3], 28); G2STEP(26, of[4], 29); G2STEP(27, of[5], -1); G2STEP(28, of[6], -1); G2STEP(29, of[7], -1);
        STAMP(27);
        { lp rbp = c.lds + 4 * GR + (kq * 2 + th) * 8192 + lane * 16;
          SYNCV(63);
#pragma unroll
          for (int q = 0; q < 4; ++q) { *(LAS f32x4*)(rbp + q * 1024) = F4(acc0, 4 * q); *(LAS f32x4*)(rbp + 4096 + q * 1024) = F4(acc1, 4 * q); } }
        SYNC(63);
        { f32x4 s0 = {0.f, 0.f, 0.f, 0.f}, s1 = s0;
#pragma unroll
          for (int k = 0; k < 4; ++k) { const lcp bp = (lcp)c.lds + 4 * GR + (k * 2 + th) * 8192 + nt * 4096 + 2 * rh * 1024 + lane * 16; s0 += ldsf4(bp); s1 += ldsf4(bp + 1024); }
          uint2 oa, ob;
          oa.x = pk2(s0[0] + blo(xa.x), s0[1] + bhi(xa.x)); oa.y = pk2(s0[2] + blo(xa.y), s0[3] + bhi(xa.y));
          ob.x = pk2(s1[0] + blo(xb2.x), s1[1] + bhi(xb2.x)); ob.y = pk2(s1[2] + blo(xb2.y), s1[3] + bhi(xb2.y));
          u16* orow = P.out + (size_t)tok * 512 + n0;
          *(uint2*)orow = oa; *(uint2*)(orow + 8) = ob; }
    }
    STAMP(14);
    }
done:
    if (blockIdx.x == 0 && w == 7 && !edone_) {
        const GAS u64* pp = (const GAS u64*)(ws + WS_SF) + lane * 4;
        for (unsigned spins = 0;; ++spins) { bool ok = true;
#pragma unroll
            for (int j = 0; j < 4; ++j) ok &= __hip_atomic_load(pp + j, __ATOMIC_RELAXED, __HIP_MEMORY_SCOPE_AGENT) == tag;
            if (__all(ok)) break; if (spins > SPIN_CAP) { set_timeout(ws, lane); break; } __builtin_amdgcn_s_sleep(2); }
        if (lane == 0) store_flag(ws, WS_EPOCH, tag);
    }
}

extern "C" void kernel_launch(void* const* d_in, const int* in_sizes, int n_in, void* d_out, int out_size, void* d_ws, size_t ws_size, hipStream_t stream) {
    Params p;
    p.x = (const u16*)d_in[0]; p.wqkv = (const u16*)d_in[1]; p.wo = (const u16*)d_in[2];
    p.wg = (const u16*)d_in[4]; p.wu = (const u16*)d_in[5]; p.wd = (const u16*)d_in[6];
    p.out = (u16*)d_out; p.ws = (char*)d_ws;
    (void)hipFuncSetAttribute((const void*)fused_kernel, hipFuncAttributeMaxDynamicSharedMemorySize, LDS_BYTES);
    fused_kernel<<<256, 512, LDS_BYTES, stream>>>(p);
}
```

```cpp
#include <hip/hip_runtime.h>
#include <stdint.h>
#include <type_traits>

#define FI __device__ __forceinline__
#define LAS __attribute__((address_space(3)))
#define GAS __attribute__((address_space(1)))
typedef unsigned short u16;
typedef unsigned long long u64;
typedef short bf16x8 __attribute__((ext_vector_type(8)));
typedef short s16x4 __attribute__((ext_vector_type(4)));
typedef float f32x16 __attribute__((ext_vector_type(16)));
typedef float f32x4 __attribute__((ext_vector_type(4)));
typedef float f32x2 __attribute__((ext_vector_type(2)));
typedef unsigned u32x4 __attribute__((ext_vector_type(4)));
typedef __bf16 bf16x2_t __attribute__((ext_vector_type(2)));
typedef LAS const char* lcp;
typedef LAS char* lp;

constexpr int GR = 8192;
constexpr int L_KV = 13 * GR;
constexpr int L_KVR = 17 * GR;
constexpr int L_MISC = 19 * GR;
constexpr int L_CS = L_MISC;
constexpr int L_CSW = L_MISC + 2048;
constexpr int L_ZERO = L_MISC + 4096;
constexpr int L_PART = 6 * GR;
constexpr int L_ONES = L_MISC + 6144;
constexpr int L_CNT = L_ONES + 64;
constexpr int L_FINE = L_MISC + 6400;
constexpr int LDS_BYTES = 163840;

constexpr size_t WS_EPOCH = 0, WS_TMO = 64;
constexpr size_t WS_SF = 4096;
constexpr size_t WS_KVF = 8192;
constexpr size_t WS_HF = 24576;
constexpr size_t WS_OF = 40960;
constexpr size_t WS_ST = 65536;
constexpr size_t WS_CS = 131072;
constexpr size_t WS_SCR = 1u << 19;
constexpr size_t WS_KV = 1u << 20;
constexpr size_t WS_C = 8u << 20;
constexpr size_t WS_DBG = 32u << 20;
constexpr int NKS = 118;
constexpr unsigned CODE_PF_BYTES = 43008;

FI f32x16 mfma(bf16x8 a, bf16x8 b, f32x16 c) { return __builtin_amdgcn_mfma_f32_32x32x16_bf16(a, b, c, 0, 0, 0); }
FI f32x16 mfma_pin(bf16x8 a, bf16x8 b, const f32x16& c) { f32x16 d; asm volatile("v_mfma_f32_32x32x16_bf16 %0, %1, %2, %3" : "=&v"(d) : "v"(a), "v"(b), "v"(c)); return d; }
FI f32x16 mfma_pin0(bf16x8 a, bf16x8 b) { f32x16 d; asm volatile("v_mfma_f32_32x32x16_bf16 %0, %1, %2, 0" : "=&v"(d) : "v"(a), "v"(b)); return d; }
FI unsigned pk2(float lo, float hi) { f32x2 v = {lo, hi}; bf16x2_t r = __builtin_convertvector(v, bf16x2_t); return __builtin_bit_cast(unsigned, r); }
FI u32x4 pack8(const f32x16& y, int b) { u32x4 r; r[0] = pk2(y[b], y[b + 1]); r[1] = pk2(y[b + 2], y[b + 3]); r[2] = pk2(y[b + 4], y[b + 5]); r[3] = pk2(y[b + 6], y[b + 7]); return r; }
typedef short v4i16_t __attribute__((ext_vector_type(4)));
FI s16x4 trd(lcp p) { return __builtin_bit_cast(s16x4, __builtin_amdgcn_ds_read_tr16_b64_v4i16((LAS v4i16_t*)p)); }
template <int D2> FI bf16x8 trfrag(lcp p) { s16x4 lo = trd(p), hi = trd(p + D2); return __builtin_shufflevector(lo, hi, 0, 1, 2, 3, 4, 5, 6, 7); }
FI bf16x8 ldsf(lcp p) { return *(const LAS bf16x8*)p; }
FI f32x4 ldsf4(lcp p) { return *(const LAS f32x4*)p; }

FI void dma16(unsigned voff, const void* sbase, unsigned dst) {
    asm volatile("s_mov_b32 m0, %2\n\ts_nop 0\n\tglobal_load_lds_dwordx4 %0, %1" :: "v"(voff), "s"(sbase), "s"(__builtin_amdgcn_readfirstlane(dst)) : "memory", "m0");
}
FI void dma16_sc1(unsigned voff, const void* sbase, unsigned dst) {
    asm volatile("s_mov_b32 m0, %2\n\ts_nop 0\n\tglobal_load_lds_dwordx4 %0, %1 sc1" :: "v"(voff), "s"(sbase), "s"(__builtin_amdgcn_readfirstlane(dst)) : "memory", "m0");
}
FI void aload16(u32x4& v, const void* base, unsigned voff) { asm volatile("global_load_dwordx4 %0, %1, %2" : "=v"(v) : "v"(voff), "s"(base) : "memory"); }
FI void aload16_sc1(u32x4& v, const void* base, unsigned voff) { asm volatile("global_load_dwordx4 %0, %1, %2 sc1" : "=v"(v) : "v"(voff), "s"(base) : "memory"); }
FI void aload_flag(u64& v, const char* base, unsigned voff) { asm volatile("global_load_dwordx2 %0, %1, %2 sc1" : "=v"(v) : "v"(voff), "s"(base) : "memory"); }

FI void aprefetch(unsigned& d, const char* p) { asm volatile("global_load_dword %0, %1, off" : "+v"(d) : "v"(p) : "memory"); }
#ifndef EXIT_AT
#define EXIT_AT 99
#endif
#define SB() __builtin_amdgcn_sched_barrier(0)
#define PIN(x) asm volatile("" : "+v"(x))
FI int opaque(int v) { asm volatile("" : "+v"(v)); return v; }
#define FST() do {} while (0)
#define SYNC(N) do { SB(); FST(); asm volatile("s_waitcnt vmcnt(" #N ") lgkmcnt(0)\n\ts_barrier" ::: "memory"); FST(); SB(); } while (0)
#define SYNCV(N) do { SB(); FST(); asm volatile("s_waitcnt vmcnt(" #N ")\n\ts_barrier" ::: "memory"); FST(); SB(); } while (0)
#define WAITV(N) do { SB(); asm volatile("s_waitcnt vmcnt(" #N ")" ::: "memory"); SB(); } while (0)

#define STAMP(k) do {} while (0)

struct Params {
    const u16* x; const u16* wqkv; const u16* wo; const u16* wg; const u16* wu; const u16* wd; u16* out; char* ws;
};

constexpr int wslot(int s) { return (s == 0 ? 4 : s == 1 ? 11 : s == 2 ? 0 : s == 3 ? 7 : s == 4 ? 10 : s == 5 ? 13 : s == 6 ? 0 : s == 7 ? 3 : s == 8 ? 6 : s == 9 ? 9 : s == 10 ? 0 : 3) * GR; }
constexpr int xslot(int c) { return (c == 0 ? 0 : c == 1 ? 2 : c == 2 ? 7 : c == 3 ? 9 : c == 4 ? 14 : c == 5 ? 17 : c == 6 ? 3 : 5) * GR; }
FI constexpr int dloc0(int u) { return (u == 0 ? 12 : u < 4 ? 6 + (u - 1) : u < 7 ? 9 + (u - 4) : u < 9 ? u - 7 : 2 + (u - 9)) * GR; }
FI constexpr int dloc(int u) { return u < 13 ? dloc0(u) : u < 19 ? L_KV + (u - 13) * GR : dloc0(u - 19); }

struct Ctx {
    const Params* p;
    unsigned lds0;
    lp lds;
    int lane, w, tm, g, rb, cg, tb, nb;
    unsigned vq, vf0, vf1, vf2, vx, vlin, vd;
    const char *px, *pq, *pf, *pwd, *pwo;
};

FI void issue_x1(const Ctx& c, int chunk, int k) {
    dma16(k ? c.vx ^ 64u : c.vx, c.px + chunk * 128 + k * 8 * 1024, c.lds0 + xslot(chunk) + 16 * c.w * 128 + k * 8 * 128);
}
FI void issue_x(const Ctx& c, int chunk) { issue_x1(c, chunk, 0); issue_x1(c, chunk, 1); }
template <int S> FI void issue_w1(const Ctx& c, int k) {
    if constexpr (S >= 4) {
        const char* base = c.pf + (size_t)(64 * (S - 4)) * 2752;
        dma16(k == 0 ? c.vf0 : k == 1 ? c.vf1 : c.vf2, base, c.lds0 + wslot(S) + c.g * 12288 + c.tm * 3072 + k * 1024);
    } else {
        const char* base = c.pq + (size_t)(128 * S) * 3072;
        dma16(c.vq, base + (k == 0 ? 1024 : k == 1 ? 2048 : 0), c.lds0 + wslot(S) + c.w * 1024 + k * 8192);
    }
}
template <int S> FI void issue_w(const Ctx& c) { issue_w1<S>(c, 0); issue_w1<S>(c, 1); issue_w1<S>(c, 2); }
FI void issue_d(const Ctx& c, int u) {
    const char* base;
    if (u < 21) base = c.pwd + (size_t)u * 65536;
    else if (u == 21) base = c.pwd + (size_t)21 * 65536 - (c.w >= 4 ? 32768 : 0);
    else base = c.pwo + (size_t)(u - 22) * 65536;
    dma16(c.vd, base, c.lds0 + dloc(u) + c.w * 1024);
}

FI float blo(unsigned d) { return __uint_as_float(d << 16); }
FI float bhi(unsigned d) { return __uint_as_float(d & 0xffff0000u); }
template <int CTRL> FI float dppadd(float x) { return x + __builtin_bit_cast(float, __builtin_amdgcn_update_dpp(0, __builtin_bit_cast(int, x), CTRL, 0xf, 0xf, true)); }
FI void swap32add(float& u, float v) { auto rr = __builtin_amdgcn_permlane32_swap(__float_as_uint(u), __float_as_uint(v), false, false); u = __uint_as_float(rr[0]) + __uint_as_float(rr[1]); }
FI void swap16add(float& u, float v) { auto rr = __builtin_amdgcn_permlane16_swap(__float_as_uint(u), __float_as_uint(v), false, false); u = __uint_as_float(rr[0]) + __uint_as_float(rr[1]); }

FI f32x4 m4(s16x4 a, s16x4 b, f32x4 c) { return __builtin_amdgcn_mfma_f32_4x4x4bf16_1k(a, b, c, 0, 0, 0); }
FI s16x4 lo4(const bf16x8& f) { return __builtin_shufflevector(f, f, 0, 1, 2, 3); }
FI s16x4 hi4(const bf16x8& f) { return __builtin_shufflevector(f, f, 4, 5, 6, 7); }
constexpr s16x4 ONES4 = {0x3f80, 0x3f80, 0x3f80, 0x3f80};
FI void stat4(const bf16x8& f, f32x4& s, f32x4& q) { s = m4(ONES4, lo4(f), s); q = m4(lo4(f), lo4(f), q); s = m4(ONES4, hi4(f), s); q = m4(hi4(f), hi4(f), q); }
FI void sum4(const bf16x8& f, f32x4& s) { s = m4(ONES4, lo4(f), s); s = m4(ONES4, hi4(f), s); }
FI float halfsum(float v) { auto rr = __builtin_amdgcn_permlane32_swap(__float_as_uint(v), __float_as_uint(v), false, false); return __uint_as_float(rr[0]) + __uint_as_float(rr[1]); }
struct G1State {
    bf16x8 xf[32];
    bf16x8 wq[4];
    f32x16 a0, a1, b0, b1;
    unsigned wb[4];
    unsigned xb[4];
    f32x4 cs0, cs1;
};
constexpr int fkind(int G, int f) { return G == 0 ? (f < 8 ? (f & 3) : (f & 1)) : (f < 4 ? (f & 1) : f < 6 ? (f & 1) : f < 8 ? 2 + (f & 1) : f < 10 ? (f & 1) : 2 + (f & 1)); }
constexpr int fpos(int G, int f) { return G == 0 ? (f < 8 ? (f >> 2) : 2 + ((f - 8) >> 1)) : (f < 4 ? (f >> 1) : f < 8 ? 2 : 3); }
constexpr int qkind(int G, int f) { return G == 0 ? ((f == 1 || f == 3 || f == 7 || f == 9) ? 1 : 0) : ((f == 3 || f == 5 || f == 9 || f == 11) ? 1 : 0); }
constexpr int qpos(int G, int f) {
    return G == 0 ? (f == 0 ? 0 : f == 1 ? 0 : f == 2 ? 1 : f == 3 ? 1 : f == 4 ? 2 : f == 5 ? 3 : f == 6 ? 4 : f == 7 ? 4 : f == 8 ? 5 : f == 9 ? 5 : f == 10 ? 6 : 7)
                  : (f == 0 ? 0 : f == 1 ? 1 : f == 2 ? 2 : f == 3 ? 2 : f == 4 ? 3 : f == 5 ? 3 : f == 6 ? 4 : f == 7 ? 5 : f == 8 ? 6 : f == 9 ? 6 : f == 10 ? 7 : 7);
}
template <int G, int S> FI bf16x8 rdfrag(const G1State& st, int f) {
    if constexpr (S >= 4) { const int k = fkind(G, f); return trfrag<768>((lcp)(uintptr_t)(st.wb[fpos(G, f)] + (k & 1) * 12288 + (k >= 2 ? 64 : (G == 0 ? 0 : 128)))); }
    else { const int e = qpos(G, f); return trfrag<256>((lcp)(uintptr_t)(st.wb[e & 3] + (e >> 2) * 4096 + (qkind(G, f) ? 16384 : G * 8192))); }
}
template <int C> FI void rdx(G1State& st) {
#pragma unroll
    for (int e = 0; e < 4; ++e) { st.xf[4 * C + e] = ldsf((lcp)(uintptr_t)st.xb[e]); if (C < 7) st.xb[e] = (unsigned)opaque((int)(st.xb[e] + (xslot(C + 1) - xslot(C)))); }
}
template <int G, int S, bool FRESH, bool PF, class F>
FI void stage(G1State& st, F&& hook) {
    if constexpr (FRESH) {
        if constexpr (S < 4) rdx<(S < 4 ? 2 * S : 0)>(st);
#pragma unroll
        for (int f = 0; f < 4; ++f) st.wq[f] = rdfrag<G, S>(st, f);
        if constexpr (S < 4) rdx<(S < 4 ? 2 * S + 1 : 0)>(st);
        SB();
    }
#pragma unroll
    for (int f = 0; f < 12; ++f) {
        const bf16x8 w = st.wq[f & 3];
        if constexpr (S >= 4) {
            const int k = fkind(G, f); const bf16x8 x = st.xf[4 * (S - 4) + fpos(G, f)];
            if (k == 0) st.a0 = mfma(w, x, st.a0); else if (k == 1) st.a1 = mfma(w, x, st.a1); else if (k == 2) st.b0 = mfma(w, x, st.b0); else st.b1 = mfma(w, x, st.b1);
        } else {
            const bf16x8 x = st.xf[8 * S + qpos(G, f)];
            if (qkind(G, f)) st.b0 = mfma(w, x, st.b0); else if (G == 0) st.a0 = mfma(w, x, st.a0); else st.a0 = mfma(x, w, st.a0);
        }
        SB();
        if (f < 8) st.wq[f & 3] = rdfrag<G, S>(st, f + 4);
        else {
            if (f == 8 && S != 3 && S != 11) { _Pragma("unroll") for (int j = 0; j < 4; ++j) st.wb[j] = (unsigned)opaque((int)(st.wb[j] + (wslot(S + 1) - wslot(S)))); }
            if (PF) st.wq[f & 3] = rdfrag<G, (PF ? S + 1 : S)>(st, f - 8);
        }
        if constexpr (S < 4) {
            if (qkind(G, f) == 0 && (qpos(G, f) & 3) == 0) sum4(w, st.cs0);
            if (qkind(G, f) == 1 && qpos(G, f) == (G == 0 ? 0 : 6)) sum4(w, st.cs1);
        }
        hook(f);
        SB();
    }
}

FI u64 mix64(u64 z) { z += 0x9E3779B97F4A7C15ull; z = (z ^ (z >> 30)) * 0xBF58476D1CE4E5B9ull; z = (z ^ (z >> 27)) * 0x94D049BB133111EBull; return z ^ (z >> 31); }

constexpr unsigned SPIN_CAP = 1u << 18;
FI void set_timeout(char* ws, int lane) { if (lane == 0) __hip_atomic_store((GAS unsigned*)(ws + WS_TMO), 1u, __ATOMIC_RELAXED, __HIP_MEMORY_SCOPE_AGENT); }
FI void poll_flags(const u64* flags, int n, u64 tag, int lane, char* ws) {
    const GAS u64* pp = (const GAS u64*)flags + (lane < n ? lane : 0);
    for (unsigned spins = 0;; ++spins) {
        const u64 v = __hip_atomic_load(pp, __ATOMIC_RELAXED, __HIP_MEMORY_SCOPE_AGENT);
        if (__all(v == tag)) return;
        if (spins > SPIN_CAP) { set_timeout(ws, lane); return; }
        __builtin_amdgcn_s_sleep(2);
    }
}
FI void store_flag(char* ws, size_t off, u64 tag) { __hip_atomic_store((GAS u64*)(ws + off), tag, __ATOMIC_RELAXED, __HIP_MEMORY_SCOPE_AGENT); }

__global__ __launch_bounds__(512, 2) void fused_kernel(Params P) {
    extern __shared__ __attribute__((aligned(16))) char smem[];
    Ctx c; c.p = &P;
    const int tid = threadIdx.x; c.lane = tid & 63;
    unsigned pfd = 0;
    { const char* pc = (const char*)__builtin_amdgcn_s_getpc(); unsigned off = ((unsigned)(blockIdx.x >> 3) * 16u + (tid & 15)) * 128u; if (off > CODE_PF_BYTES) off = CODE_PF_BYTES; if (tid < 16) aprefetch(pfd, pc + off); }
    const int w = __builtin_amdgcn_readfirstlane(tid >> 6);
    c.w = w; c.tm = w & 3; c.g = w >> 2;
    c.lds = (lp)smem; c.lds0 = (unsigned)(uintptr_t)c.lds;
    const int xcd = blockIdx.x & 7, idx = blockIdx.x >> 3;
    c.rb = 4 * (xcd >> 1) + (idx >> 3); c.cg = 8 * (xcd & 1) + (idx & 7);
    c.tb = 4 * xcd + (idx >> 3); c.nb = idx & 7;
    const int fs = c.cg < 11 ? 3 * c.cg : c.cg < 15 ? 33 + 2 * (c.cg - 11) : 40;
    const bool v0 = c.cg != 15, v2 = c.cg < 11 || c.cg == 15;
    const int lane = c.lane, h = lane >> 5, r32 = lane & 31;
    char* ws = P.ws;
    STAMP(0);
    c.px = (const char*)P.x + (size_t)(128 * c.rb + 16 * w) * 1024;
    c.pf = (const char*)(c.g ? P.wu : P.wg) + (size_t)(16 * c.tm) * 2752 + fs * 64;
    u64 tag = 0; unsigned tagLo = 0, tagHi = 0; float ln_a = 0.f, ln_m = 0.f;
    const int T = 4 * c.rb + c.tm;
    const __amdgpu_buffer_rsrc_t wsr = __builtin_amdgcn_make_buffer_rsrc(ws, 0, 32u << 20, 0x00020000);
    c.pq = (const char*)P.wqkv + ((size_t)(16 * w) * 1536 + 32 * c.cg) * 2;
    c.pwd = (const char*)P.wd + (size_t)(8 * w) * 1024 + c.nb * 128;
    c.pwo = (const char*)P.wo + (size_t)(8 * w) * 1024 + c.nb * 128;
    u32x4 g0a, g0b;
#define GATHER_OFFS(tile) const int gl_ = opaque(lane); const unsigned go0 = (unsigned)(WS_CS + (c.cg * 9 + (tile)) * 2048 + (gl_ >> 3) * 256 + (gl_ & 7) * 32)
#define GATHER_ISSUE(tile, ST) do { GATHER_OFFS(tile); aload16_sc1(g0a, P.ws, go0); aload16_sc1(g0b, P.ws + 16, go0); } while (0)
#define GATHER_OK(ST) (g0a[1] == tagLo && g0a[3] == tagLo && g0b[1] == tagLo && g0b[3] == tagLo)
#define GATHER_RED(tile) do { const int gl_ = opaque(lane); float v0_ = __uint_as_float(g0a[0]), v1_ = __uint_as_float(g0a[2]), v2_ = __uint_as_float(g0b[0]), v3_ = __uint_as_float(g0b[2]); \
        swap32add(v0_, v2_); swap32add(v1_, v3_); swap16add(v0_, v1_); v0_ = dppadd<0x128>(v0_); \
        if ((gl_ & 8) == 0) *(LAS float*)(c.lds + L_CS + (tile) * 128 + (4 * (gl_ & 7) + 2 * (gl_ >> 5) + ((gl_ >> 4) & 1)) * 4) = v0_; } while (0)
#define GATHER_CHECK(tile, ST) do { asm volatile("" : "+v"(g0a), "+v"(g0b)); \
        if (!__all(GATHER_OK(ST))) { GATHER_OFFS(tile); \
            for (unsigned spins = 0;; ++spins) { __builtin_amdgcn_s_sleep(2); \
                g0a = __builtin_amdgcn_raw_buffer_load_b128(wsr, go0, 0, 16); g0b = __builtin_amdgcn_raw_buffer_load_b128(wsr, go0 + 16, 0, 16); __builtin_amdgcn_s_waitcnt(0x0F70); \
                if (__all(GATHER_OK(ST))) break; if (spins > SPIN_CAP) { set_timeout(ws, lane); break; } } } \
        } while (0)
#define RD_CS(dst, tile) do { _Pragma("unroll") for (int q_ = 0; q_ < 4; ++q_) { const f32x4 t_ = ldsf4((lcp)c.lds + L_CS + (tile) * 128 + (8 * q_ + 4 * h) * 4); \
            dst[4 * q_] = t_[0]; dst[4 * q_ + 1] = t_[1]; dst[4 * q_ + 2] = t_[2]; dst[4 * q_ + 3] = t_[3]; } } while (0)
#define SPREAD(dst, v, row) do { if (h == 0) *(LAS float*)(c.lds + L_CSW + c.tm * 256 + (row) * 128 + r32 * 4) = (v); \
        _Pragma("unroll") for (int q_ = 0; q_ < 4; ++q_) { const f32x4 t_ = ldsf4((lcp)c.lds + L_CSW + c.tm * 256 + (row) * 128 + (8 * q_ + 4 * h) * 4); \
            dst[4 * q_] = t_[0]; dst[4 * q_ + 1] = t_[1]; dst[4 * q_ + 2] = t_[2]; dst[4 * q_ + 3] = t_[3]; } } while (0)
#define F4(v, b) (f32x4){v[b], v[(b) + 1], v[(b) + 2], v[(b) + 3]}

    const int th = w & 1, kq = w >> 1;
    const int T2 = 2 * c.tb + th;
    bf16x8 qf; bool edone_ = false;
    auto gemm1 = [&](auto Gc) {
        constexpr int G = decltype(Gc)::value;
        const int lane = opaque(c.lane), h = lane >> 5, r32 = lane & 31, tid = w * 64 + lane;
        c.vq = (lane >> 2) * 3072 + (lane & 3) * 16;
        c.vx = (lane >> 3) * 1024 + (((lane & 7) ^ (lane >> 4)) * 16); c.vlin = lane * 16;
        G1State st;
        issue_x(c, 0); issue_x(c, 1); issue_w<0>(c);
        u64 ep; aload_flag(ep, ws + WS_EPOCH, 0u);
        u32x4 ax; aload16(ax, (const char*)P.x + (size_t)(128 * c.rb + 8 * c.cg + w) * 1024, c.vlin);
        u32x4 aw0, aw1, aw2, aw3;
        const int ptile = 3 + 2 * (w < 3 ? w : 0) + (c.rb & 1);
        { const char* tb = (const char*)((c.rb & 1) ? P.wu : P.wg) + 64 * (fs + (w < 3 ? w : 0)) + (size_t)(64 * (c.rb >> 1)) * 2752; const unsigned vo = (lane >> 2) * 2752 + (lane & 3) * 16;
          aw0 = aw1 = aw2 = aw3 = (u32x4){0u, 0u, 0u, 0u};
          if (w < 3) { aload16(aw0, tb, vo); aload16(aw1, tb + 16 * 2752, vo); aload16(aw2, tb + 32 * 2752, vo); aload16(aw3, tb + 48 * 2752, vo); } }
        issue_x(c, 2); issue_x(c, 3); issue_w<1>(c); issue_x(c, 4); issue_x(c, 5);
        unsigned pfw = 0;
        { const char* p;
          if (tid < 256) p = ws + (size_t)tid * 65536;
          else if (tid < 288) p = (const char*)P.out + (size_t)(tid - 256) * 65536;
          else if (tid < 310) p = (const char*)P.wg + (size_t)(tid - 288) * 65536;
          else if (tid < 332) p = (const char*)P.wu + (size_t)(tid - 310) * 65536;
          else if (tid < 354) p = (const char*)P.wd + (size_t)(tid - 332) * 65536;
          else if (tid < 362) p = (const char*)P.wo + (size_t)(tid - 354) * 65536;
          else if (tid < 386) p = (const char*)P.wqkv + (size_t)(tid - 362) * 65536;
          else p = (const char*)P.x + (size_t)((tid - 386) & 31) * 65536;
          aprefetch(pfw, p); }
        *(LAS unsigned*)(c.lds + L_ZERO + tid * 4) = 0u;
        if (tid < 4) { *(LAS unsigned*)(c.lds + L_ONES + tid * 4) = 0x3f803f80u; *(LAS unsigned*)(c.lds + L_ONES + 1024 + tid * 4) = 0x3f803f80u; }
        if (tid >= 64 && tid < 72) *(LAS unsigned*)(c.lds + L_CNT + (tid - 64) * 4) = 0u;
        c.vf0 = (lane / 12) * 2752 + (lane % 12) * 16; c.vf1 = ((lane + 64) / 12) * 2752 + ((lane + 64) % 12) * 16; c.vf2 = ((lane + 128) / 12) * 2752 + ((lane + 128) % 12) * 16;
        { const unsigned lb = c.lds0 + (8 * h + ((lane & 15) >> 2)) * 64 + 32 * ((lane >> 4) & 1) + 8 * (lane & 3);
#pragma unroll
          for (int j = 0; j < 4; ++j) st.wb[j] = lb + ((j ^ c.tm) * 1024) + wslot(0); }
        { const int row = 32 * c.tm + r32;
#pragma unroll
          for (int j = 0; j < 4; ++j) st.xb[j] = c.lds0 + xslot(0) + row * 128 + (((2 * (j ^ c.tm) + h) ^ ((row >> 1) & 7)) * 16); }
        st.a0 = f32x16{}; st.a1 = f32x16{}; st.b0 = f32x16{}; st.b1 = f32x16{}; st.cs0 = f32x4{}; st.cs1 = f32x4{};

        SYNC(12);
        PIN(pfd);
        if (EXIT_AT == 0) { tag = mix64(((u64)(unsigned)__builtin_amdgcn_readfirstlane((int)(unsigned)(ep >> 32)) << 32) | (unsigned)__builtin_amdgcn_readfirstlane((int)(unsigned)ep)); if (w == 0 && lane == 0) store_flag(ws, WS_SF + (size_t)blockIdx.x * 8, tag); return; }
        asm volatile("" : "+v"(ep), "+v"(ax), "+v"(aw0), "+v"(aw1), "+v"(aw2), "+v"(aw3));
        tag = mix64(((u64)(unsigned)__builtin_amdgcn_readfirstlane((int)(unsigned)(ep >> 32)) << 32) | (unsigned)__builtin_amdgcn_readfirstlane((int)(unsigned)ep));
        tagLo = (unsigned)tag; tagHi = (unsigned)(tag >> 32);
        STAMP(1);

        float as_ = 0.f, aq_ = 0.f, av[8];
        auto aux0 = [&](int f) {
            if (f < 4) { const float a = blo(ax[f]), b = bhi(ax[f]); as_ += a + b; aq_ = __builtin_fmaf(a, a, aq_); aq_ = __builtin_fmaf(b, b, aq_); PIN(as_); PIN(aq_); }
            if (f == 4) { as_ = dppadd<0x121>(as_); aq_ = dppadd<0x121>(aq_); as_ = dppadd<0x122>(as_); aq_ = dppadd<0x122>(aq_); PIN(as_); PIN(aq_); }
            if (f == 5) { as_ = dppadd<0x124>(as_); aq_ = dppadd<0x124>(aq_); as_ = dppadd<0x128>(as_); aq_ = dppadd<0x128>(aq_); PIN(as_); PIN(aq_); }
            if (f == 6) { swap16add(as_, as_); swap16add(aq_, aq_); swap32add(as_, as_); swap32add(aq_, aq_); PIN(as_); PIN(aq_); }
            if (f == 7) { const float mean = as_ * (1.f / 512.f), var = aq_ * (1.f / 512.f) - mean * mean, rstd = __builtin_amdgcn_rsqf(fmaxf(var, 0.f) + 1e-5f);
                if (lane == 0) __builtin_amdgcn_raw_buffer_store_b128((u32x4){__float_as_uint(mean), tagLo, __float_as_uint(rstd), tagHi}, wsr, (unsigned)(WS_ST + (128 * c.rb + 8 * c.cg + w) * 16), 0, 16); }
            if (f >= 8) { const int i = f - 8; av[2 * i] = (blo(aw0[i]) + blo(aw1[i])) + (blo(aw2[i]) + blo(aw3[i])); av[2 * i + 1] = (bhi(aw0[i]) + bhi(aw1[i])) + (bhi(aw2[i]) + bhi(aw3[i])); PIN(av[2 * i]); PIN(av[2 * i + 1]); }
        };
        auto aux1 = [&](int f) {
            if (f == 8) { swap32add(av[0], av[4]); swap32add(av[1], av[5]); swap32add(av[2], av[6]); swap32add(av[3], av[7]); PIN(av[0]); PIN(av[1]); PIN(av[2]); PIN(av[3]); }
            if (f == 9) { swap16add(av[0], av[2]); swap16add(av[1], av[3]); av[0] = dppadd<0x124>(av[0]); av[1] = dppadd<0x124>(av[1]); av[0] = dppadd<0x128>(av[0]); av[1] = dppadd<0x128>(av[1]); PIN(av[0]); PIN(av[1]); }
        };
        u32x4 gs;
        const int gt2 = w < 6 ? 3 + w : w - 3;
        stage<G, 0, true, false>(st, [&](int f_) { aux0(f_); }); SYNCV(6);
        stage<G, 1, true, false>(st, [&](int f_) { if (f_ < 3) issue_w1<2>(c, f_); else if (f_ < 5) issue_x1(c, 6, f_ - 3); else if (f_ < 7) issue_x1(c, 7, f_ - 5); aux1(f_); }); SYNCV(4);
        PIN(pfw);
        stage<G, 2, true, false>(st, [&](int f_) { if (f_ < 3) issue_w1<3>(c, f_); else if (f_ < 6) issue_w1<4>(c, f_ - 3);
            if (f_ == 9) aload16_sc1(gs, P.ws, (unsigned)(WS_ST + (128 * c.rb + 32 * c.tm + (opaque(lane) & 31)) * 16)); }); SYNCV(4);
        stage<G, 3, true, false>(st, [&](int f_) { if (f_ < 3) issue_w1<5>(c, f_); else if (f_ < 6) issue_w1<6>(c, f_ - 3);
            if (f_ == 10) { WAITV(6); PIN(gs);
                if (!__all(gs[1] == tagLo && gs[3] == tagHi)) { const unsigned gso = (unsigned)(WS_ST + (128 * c.rb + 32 * c.tm + (opaque(lane) & 31)) * 16);
                    for (unsigned spins = 0;; ++spins) { __builtin_amdgcn_s_sleep(2); gs = __builtin_amdgcn_raw_buffer_load_b128(wsr, gso, 0, 16); __builtin_amdgcn_s_waitcnt(0x0F70);
                        if (__all(gs[1] == tagLo && gs[3] == tagHi)) break; if (spins > SPIN_CAP) { set_timeout(ws, lane); break; } } }
                ln_m = __uint_as_float(gs[0]); ln_a = __uint_as_float(gs[2]); PIN(ln_m); PIN(ln_a); } });
        SYNCV(63);
        { lp xp = c.lds + (G == 0 ? 9 : 16) * GR + c.tm * 2048 + c.vlin; *(LAS f32x4*)(xp) = F4(st.b0, 8 - 8 * G); *(LAS f32x4*)(xp + 1024) = F4(st.b0, 12 - 8 * G);
          const float s0 = halfsum(st.cs0[0]), s1 = halfsum(st.cs1[0]);
          if (h == 0) { *(LAS float*)(c.lds + L_PART + (G * 4 + c.tm) * 128 + r32 * 4) = s0; *(LAS float*)(c.lds + L_PART + 1024 + (G * 4 + c.tm) * 128 + r32 * 4) = s1;
                        if constexpr (G == 1) { *(LAS float*)(c.lds + L_CSW + c.tm * 256 + r32 * 4) = ln_m; *(LAS float*)(c.lds + L_CSW + c.tm * 256 + 128 + r32 * 4) = ln_a; } } }
        { const int ln = opaque(lane); const unsigned lb = c.lds0 + (8 * (ln >> 5) + ((ln & 15) >> 2)) * 192 + 32 * ((ln >> 4) & 1) + 8 * (ln & 3);
#pragma unroll
          for (int j = 0; j < 4; ++j) st.wb[j] = lb + ((j ^ c.tm) * 3072) + wslot(4); }
        SYNC(6);
        STAMP(2);
        issue_w<7>(c);
        { const int lane = opaque(c.lane), h = lane >> 5, r32 = lane & 31;
          const unsigned o = (unsigned)(WS_KV + ((size_t)c.cg * 64 + T) * 4096) + lane * 16 + G * 2048;
          f32x16 y;
          if constexpr (G == 0) {
#pragma unroll
              for (int q = 0; q < 4; ++q) { f32x4 cs = ldsf4((lcp)c.lds + L_PART + (8 * q + 4 * h) * 4);
#pragma unroll
                  for (int t = 1; t < 4; ++t) cs += ldsf4((lcp)c.lds + L_PART + t * 128 + (8 * q + 4 * h) * 4);
#pragma unroll
                  for (int i = 0; i < 4; ++i) y[4 * q + i] = ln_a * __builtin_fmaf(-ln_m, cs[i], st.a0[4 * q + i]); }
          } else {
              const lcp pp = (lcp)c.lds + L_PART + 512 + r32 * 4;
              const float cso = (*(const LAS float*)pp + *(const LAS float*)(pp + 128)) + (*(const LAS float*)(pp + 256) + *(const LAS float*)(pp + 384));
#pragma unroll
              for (int q = 0; q < 4; ++q) { const f32x4 mm = ldsf4((lcp)c.lds + L_CSW + c.tm * 256 + (8 * q + 4 * h) * 4), aa = ldsf4((lcp)c.lds + L_CSW + c.tm * 256 + 128 + (8 * q + 4 * h) * 4);
#pragma unroll
                  for (int i = 0; i < 4; ++i) y[4 * q + i] = aa[i] * __builtin_fmaf(-mm[i], cso, st.a0[4 * q + i]); }
          }
          const u32x4 k0 = pack8(y, 0), k1 = pack8(y, 8);
          __builtin_amdgcn_raw_buffer_store_b128(k0, wsr, o, 0, 16); __builtin_amdgcn_raw_buffer_store_b128(k1, wsr, o + 1024, 0, 16);
          if (w < 3) { const int rho = lane >> 4, col = 8 * (lane & 3) + 4 * (rho >> 1) + 2 * (rho & 1);
            if ((lane & 12) == 0) __builtin_amdgcn_raw_buffer_store_b128((u32x4){__float_as_uint(av[0]), tagLo, __float_as_uint(av[1]), tagLo}, wsr, (unsigned)(WS_CS + (((c.cg * 9 + ptile) * 8 + (c.rb >> 1)) * 32 + col) * 8), 0, 16); }
          if (w == 0 && lane == 0) store_flag(ws, WS_SF + (size_t)blockIdx.x * 8, tag);
          *(LAS u32x4*)(c.lds + L_KVR + c.tm * 4096 + G * 2048 + lane * 16) = k0; *(LAS u32x4*)(c.lds + L_KVR + c.tm * 4096 + G * 2048 + 1024 + lane * 16) = k1;
          SB();
          f32x4 c0 = ldsf4((lcp)c.lds + L_PART + 1024 + (16 * G + 4 * h) * 4), c1 = ldsf4((lcp)c.lds + L_PART + 1024 + (16 * G + 8 + 4 * h) * 4);
#pragma unroll
          for (int t = 1; t < 8; ++t) { c0 += ldsf4((lcp)c.lds + L_PART + 1024 + t * 128 + (16 * G + 4 * h) * 4); c1 += ldsf4((lcp)c.lds + L_PART + 1024 + t * 128 + (16 * G + 8 + 4 * h) * 4); }
          const lcp xp = (lcp)c.lds + (G == 0 ? 16 : 9) * GR + c.tm * 2048 + lane * 16;
          const f32x4 p0 = ldsf4(xp), p1 = ldsf4(xp + 1024);
          const float sc = ln_a * (0.25f * 1.4426950408889634f);
#pragma unroll
          for (int i = 0; i < 4; ++i) { y[i] = sc * __builtin_fmaf(-ln_m, c0[i], st.b0[8 * G + i] + p0[i]); y[4 + i] = sc * __builtin_fmaf(-ln_m, c1[i], st.b0[8 * G + 4 + i] + p1[i]); }
          qf = __builtin_bit_cast(bf16x8, pack8(y, 0)); PIN(qf);
          st.a0 = f32x16{}; st.b0 = f32x16{}; }
        if (EXIT_AT == 1) return;
        stage<G, 4, true, false>(st, [&](int) {}); SYNC(5);
        stage<G, 5, true, true>(st, [&](int f_) { if (f_ >= 1 && f_ < 4) issue_w1<8>(c, f_ - 1); else if (f_ >= 4 && f_ < 7) issue_w1<9>(c, f_ - 4); }); SYNCV(8);
        { const int ln = opaque(lane); c.vd = (ln >> 3) * 1024 + (((ln & 7) ^ (((ln >> 4) & 1) << 2)) * 16); }
        stage<G, 6, false, true>(st, [&](int f_) { if (f_ == 1) issue_d(c, 0);
            if (f_ == 8) { WAITV(7); if (lane == 0) store_flag(ws, WS_KVF + ((size_t)(c.cg * 64 + T) * 2 + G) * 8, tag); } }); SYNCV(5);
        stage<G, 7, false, true>(st, [&](int f_) { if (f_ >= 1 && f_ < 4) issue_w1<10>(c, f_ - 1); }); SYNCV(5);
        STAMP(3);
        u64 kf_;
        stage<G, 8, false, true>(st, [&](int f_) { if (f_ >= 1 && f_ < 4) issue_w1<11>(c, f_ - 1); if (f_ == 7) GATHER_ISSUE(gt2, false);
            if (f_ == 8) { const int tl = 4 * c.rb - 8 + w; aload_flag(kf_, P.ws, (unsigned)(WS_KVF + ((c.cg * 64 + (tl < 0 ? 0 : tl)) * 2 + (opaque(lane) & 1)) * 8)); } }); SYNCV(6);
        stage<G, 9, false, true>(st, [&](int f_) { if (f_ >= 1 && f_ < 4) issue_d(c, f_);
            if (f_ == 6) { WAITV(3); GATHER_CHECK(gt2, false); GATHER_RED(gt2); }
            if (f_ == 8) { asm volatile("" : "+v"(kf_)); const int tl = 4 * c.rb - 8 + w;
                if (tl >= 0) {
                    if (!__all(kf_ == tag)) poll_flags((const u64*)(ws + WS_KVF) + (c.cg * 64 + tl) * 2, 2, tag, lane, ws);
                    const char* base = ws + WS_KV + ((size_t)c.cg * 64 + tl) * 4096; const unsigned dst = c.lds0 + L_KV + w * 4096;
                    dma16_sc1(c.vlin, base, dst); dma16_sc1(c.vlin, base + 1024, dst + 1024); dma16_sc1(c.vlin, base + 2048, dst + 2048); dma16_sc1(c.vlin, base + 3072, dst + 3072); } } }); SYNCV(63);
        stage<G, 10, false, true>(st, [&](int f_) { if (f_ >= 1 && f_ < 4) issue_d(c, 3 + f_); }); SYNCV(63);
        STAMP(4);
        stage<G, 11, false, false>(st, [&](int f_) { if (f_ >= 1 && f_ < 3) issue_d(c, 6 + f_); });
        {
          lp xp = c.lds + 2 * GR + w * 4096 + c.vlin;
          SYNCV(63);
          *(LAS f32x4*)(xp) = F4(st.b0, 8 - 8 * G); *(LAS f32x4*)(xp + 1024) = F4(st.b0, 12 - 8 * G); *(LAS f32x4*)(xp + 2048) = F4(st.b1, 8 - 8 * G); *(LAS f32x4*)(xp + 3072) = F4(st.b1, 12 - 8 * G); }
        SYNC(5);
        STAMP(5);
        {
          const int lane = opaque(c.lane), h = lane >> 5;
          const float a2 = ln_a * ln_a;
          { u32x4 y0, y1;
#pragma unroll
            for (int q = 0; q < 4; ++q) {
                const f32x4 cg_ = ldsf4((lcp)c.lds + L_CS + (3 + 4 * G) * 128 + (8 * q + 4 * h) * 4), cu_ = ldsf4((lcp)c.lds + L_CS + (4 + 4 * G) * 128 + (8 * q + 4 * h) * 4);
                float y[4];
#pragma unroll
                for (int i = 0; i < 4; ++i) { const float g_ = fmaxf(__builtin_fmaf(-ln_m, cg_[i], st.a0[4 * q + i]), 0.f), u_ = __builtin_fmaf(-ln_m, cu_[i], st.a1[4 * q + i]); y[i] = (g_ * a2) * u_; }
                const unsigned lo = pk2(y[0], y[1]), hi = pk2(y[2], y[3]);
                if (q < 2) { y0[2 * q] = lo; y0[2 * q + 1] = hi; } else { y1[2 * q - 4] = lo; y1[2 * q - 3] = hi; }
            }
            if (G == 0 ? v0 : v2) { const unsigned o_ = (unsigned)(WS_C + ((size_t)T * NKS + 2 * (fs + 2 * G)) * 1024) + lane * 16;
                __builtin_amdgcn_raw_buffer_store_b128(y0, wsr, o_, 0, 16); __builtin_amdgcn_raw_buffer_store_b128(y1, wsr, o_ + 1024, 0, 16); } }
          { const lcp xp = (lcp)c.lds + 2 * GR + (w ^ 4) * 4096 + lane * 16;
            u32x4 y0;
#pragma unroll
            for (int q = 0; q < 2; ++q) {
                const f32x4 pg = ldsf4(xp + q * 1024), pu = ldsf4(xp + 2048 + q * 1024);
                const f32x4 cg_ = ldsf4((lcp)c.lds + L_CS + 5 * 128 + (16 * G + 8 * q + 4 * h) * 4), cu_ = ldsf4((lcp)c.lds + L_CS + 6 * 128 + (16 * G + 8 * q + 4 * h) * 4);
                float y[4];
#pragma unroll
                for (int i = 0; i < 4; ++i) { const float g_ = fmaxf(__builtin_fmaf(-ln_m, cg_[i], st.b0[8 * G + 4 * q + i] + pg[i]), 0.f), u_ = __builtin_fmaf(-ln_m, cu_[i], st.b1[8 * G + 4 * q + i] + pu[i]); y[i] = (g_ * a2) * u_; }
                y0[2 * q] = pk2(y[0], y[1]); y0[2 * q + 1] = pk2(y[2], y[3]);
            }
            __builtin_amdgcn_raw_buffer_store_b128(y0, wsr, (unsigned)(WS_C + ((size_t)T * NKS + 2 * (fs + 1) + G) * 1024) + lane * 16, 0, 16); } }
        SYNC(63);
        if constexpr (G == 1) __builtin_amdgcn_s_setprio(1);
    };
    if (c.g == 0) gemm1(std::integral_constant<int, 0>{}); else gemm1(std::integral_constant<int, 1>{});
    STAMP(6);
    if (EXIT_AT <= 2) goto done;
    {
    const int lane2 = opaque(lane);
    const unsigned cbase = (unsigned)(WS_C + ((size_t)T2 * NKS + kq) * 1024) + lane2 * 16;
    bf16x8 hf[22];
    u64 hfl;
#define LDH(i) do { const bool ok_ = ((i) < 21) || (kq < 2); hf[i] = __builtin_bit_cast(bf16x8, __builtin_amdgcn_raw_buffer_load_b128(wsr, ok_ ? cbase + (i) * 4096 : cbase, 0, 16)); } while (0)
    {
        const int lane = lane2, h = lane >> 5, r32 = lane & 31;
        const int i0 = T >= 8 ? 0 : 8 - T;
        const unsigned kb = c.lds0 + L_KV + c.tm * 4096 + c.g * 1024 + lane * 16;
        const unsigned lanebit = r32 < 16 ? 1u : 0u;
        const unsigned vb = c.lds0 + (r32 < 16 ? (unsigned)(((r32 & 15) + 16 * c.g + 32 * h) * 16) : (unsigned)L_ONES);
        auto koff = [&](int i) -> unsigned { return (unsigned)((i >= i0 ? i : 7) * 4096); };
        auto rdk = [&](int i) -> bf16x8 { return ldsf((lcp)(uintptr_t)(kb + koff(i))); };
        auto vaddr = [&](int i) -> unsigned { const bool valid = i >= i0; const unsigned d = valid ? 0u : (unsigned)(L_ZERO - L_ONES), A = valid ? (unsigned)(L_KV + 2048 + (c.tm + i) * 4096) : (unsigned)L_ZERO;
                                              return lanebit * (A - d) + (vb + d); };
        auto dmask = [&](int r) -> bool { u64 mk = 0;
#pragma unroll
            for (int l = 0; l < 64; ++l) { const int kl = (r & 3) + 8 * (r >> 2) + 4 * (l >> 5); if (kl <= (l & 31)) mk |= 1ull << l; }
            return __builtin_amdgcn_inverse_ballot_w64(mk); };
        const f32x16 zero = f32x16{};
        float m0 = -1e30f, m1 = -1e30f;
#define MAXT(S) do { _Pragma("unroll") for (int r = 0; r < 16; r += 4) { m0 = fmaxf(fmaxf(m0, S[r]), S[r + 1]); m1 = fmaxf(fmaxf(m1, S[r + 2]), S[r + 3]); } PIN(m0); PIN(m1); } while (0)
#define EXPT(S) do { _Pragma("unroll") for (int r = 0; r < 16; ++r) S[r] = __builtin_amdgcn_exp2f(S[r]); } while (0)
#define H_PUBLISH() do { STAMP(17); WAITV(4); STAMP(18); if (lane == 0) store_flag(ws, WS_HF + ((size_t)T * 32 + 2 * c.cg + c.g) * 8, tag); } while (0)
#define H_FLAGS() aload_flag(hfl, P.ws, (unsigned)(WS_HF + (T2 * 32 + (lane & 31)) * 8))
        f32x16 o = f32x16{};
        float m;
        if (T == 0) {
            f32x16 s = mfma(ldsf((lcp)(uintptr_t)(kb + 8 * 4096)), qf, zero);
#pragma unroll
            for (int r = 0; r < 16; ++r) s[r] = dmask(r) ? s[r] : -1e30f;
            MAXT(s);
            m = fmaxf(m0, m1);
            { auto rr = __builtin_amdgcn_permlane32_swap(__float_as_uint(m), __float_as_uint(m), false, false); m = fmaxf(__uint_as_float(rr[0]), __uint_as_float(rr[1])); }
#pragma unroll
            for (int r = 0; r < 16; ++r) s[r] = __builtin_amdgcn_exp2f(s[r] - m);
            const unsigned va = vaddr(8);
            o = mfma(ldsf((lcp)(uintptr_t)va), __builtin_bit_cast(bf16x8, pack8(s, 0)), o);
            o = mfma(ldsf((lcp)(uintptr_t)(va + 1024)), __builtin_bit_cast(bf16x8, pack8(s, 8)), o);
            issue_d(c, 9); issue_d(c, 10); issue_d(c, 11); issue_d(c, 12);
            H_PUBLISH(); H_FLAGS();
        } else {
            bf16x8 k0 = rdk(0), k8 = rdk(8), k1 = rdk(1), k2 = rdk(2), k3 = rdk(3);
            f32x16 sA, sB, sC;
            sA = mfma_pin0(k0, qf); sB = mfma_pin0(k8, qf); SB();
            k0 = rdk(4); k8 = rdk(5); sC = mfma_pin0(k1, qf); k1 = rdk(6); SB();
            asm volatile("s_nop 7" ::: );
#pragma unroll
            for (int r = 0; r < 16; ++r) sA[r] = dmask(r) ? sB[r] : sA[r];
            PIN(sA); SB();
            sB = mfma_pin0(k2, qf); k2 = rdk(7); SB();
            MAXT(sA); SB();
            sA = mfma_pin0(k3, qf); issue_d(c, 9); SB(); MAXT(sC); SB();
            sC = mfma_pin0(k0, qf); issue_d(c, 10); SB(); MAXT(sB); SB();
            sB = mfma_pin0(k8, qf); issue_d(c, 11); SB(); MAXT(sA); SB();
            sA = mfma_pin0(k1, qf); issue_d(c, 12); SB(); MAXT(sC); SB();
            sC = mfma_pin0(k2, qf); k0 = rdk(0); k8 = rdk(8); k1 = rdk(1); SB(); MAXT(sB); SB();
            MAXT(sA); SB(); MAXT(sC);
            m = fmaxf(m0, m1);
            { auto rr = __builtin_amdgcn_permlane32_swap(__float_as_uint(m), __float_as_uint(m), false, false); m = fmaxf(__uint_as_float(rr[0]), __uint_as_float(rr[1])); }
            STAMP(9);
            f32x16 cm;
#pragma unroll
            for (int r = 0; r < 16; ++r) cm[r] = -m;
            PIN(cm);
            asm volatile("s_nop 1" ::: ); sA = mfma_pin(k0, qf, cm); sB = mfma_pin(k8, qf, cm); sC = mfma_pin(k1, qf, cm); SB();
            bf16x8 va0, va1, vb0, vb1;
            { const unsigned a0 = vaddr(0), a8 = vaddr(8); va0 = ldsf((lcp)(uintptr_t)a0); va1 = ldsf((lcp)(uintptr_t)(a0 + 1024)); vb0 = ldsf((lcp)(uintptr_t)a8); vb1 = ldsf((lcp)(uintptr_t)(a8 + 1024)); }
            k0 = rdk(2);
            unsigned mp[8];
#pragma unroll
            for (int j = 0; j < 8; ++j) mp[j] = (dmask(2 * j) ? 0xffffu : 0u) | (dmask(2 * j + 1) ? 0xffff0000u : 0u);
            SB();
#pragma unroll
            for (int r = 0; r < 16; ++r) sA[r] = dmask(r) ? sB[r] : sA[r];
            EXPT(sA);
            { const u32x4 p0 = pack8(sA, 0), p1 = pack8(sA, 8); u32x4 d0, d1, o0, o1;
#pragma unroll
              for (int j = 0; j < 4; ++j) { d0[j] = p0[j] & mp[j]; d1[j] = p1[j] & mp[4 + j]; o0[j] = p0[j] & ~mp[j]; o1[j] = p1[j] & ~mp[4 + j]; }
              o = mfma(va0, __builtin_bit_cast(bf16x8, o0), o); o = mfma(va1, __builtin_bit_cast(bf16x8, o1), o);
              o = mfma(vb0, __builtin_bit_cast(bf16x8, d0), o); o = mfma(vb1, __builtin_bit_cast(bf16x8, d1), o); }
            { const unsigned a1 = vaddr(1), a2 = vaddr(2); va0 = ldsf((lcp)(uintptr_t)a1); va1 = ldsf((lcp)(uintptr_t)(a1 + 1024)); vb0 = ldsf((lcp)(uintptr_t)a2); vb1 = ldsf((lcp)(uintptr_t)(a2 + 1024)); }
            SB();
#define EXP8(S, b) do { _Pragma("unroll") for (int r = (b); r < (b) + 8; ++r) S[r] = __builtin_amdgcn_exp2f(S[r]); } while (0)
#define PK4(S, b) (u32x4){pk2(S[b], S[(b) + 1]), pk2(S[(b) + 2], S[(b) + 3]), pk2(S[(b) + 4], S[(b) + 5]), pk2(S[(b) + 6], S[(b) + 7])}
#define TILE2(i, S, V0, V1, SN) do { \
                if ((i) + 1 <= 7) { SN = mfma_pin(k0, qf, cm); SB(); if ((i) + 2 <= 7) k0 = rdk((i) + 2); } \
                EXP8(S, 0); { u32x4 p_ = PK4(S, 0); PIN(p_); SB(); o = mfma(V0, __builtin_bit_cast(bf16x8, p_), o); SB(); } \
                EXP8(S, 8); { u32x4 p_ = PK4(S, 8); PIN(p_); SB(); o = mfma(V1, __builtin_bit_cast(bf16x8, p_), o); SB(); } \
                if ((i) + 2 <= 7) { const unsigned a_ = vaddr((i) + 2); V0 = ldsf((lcp)(uintptr_t)a_); V1 = ldsf((lcp)(uintptr_t)(a_ + 1024)); } SB(); } while (0)
            H_PUBLISH();
            TILE2(1, sC, va0, va1, sB); TILE2(2, sB, vb0, vb1, sC); TILE2(3, sC, va0, va1, sB); TILE2(4, sB, vb0, vb1, sC);
            TILE2(5, sC, va0, va1, sB);
            H_FLAGS();
            TILE2(6, sB, vb0, vb1, sC); TILE2(7, sC, va0, va1, sB);
        }
        const float inv = __builtin_amdgcn_rcpf(o[8]);
        f32x16 y;
#pragma unroll
        for (int r = 0; r < 8; ++r) y[r] = o[r] * inv;
        const unsigned oo = (unsigned)(WS_C + ((size_t)T * NKS + 86 + 2 * c.cg + c.g) * 1024) + lane * 16;
        const u32x4 ov = pack8(y, 0);
        STAMP(28);
        if (EXIT_AT == 3) { __builtin_amdgcn_raw_buffer_store_b128(ov, wsr, oo, 0, 16); goto done; }
        __builtin_amdgcn_raw_buffer_store_b128(ov, wsr, oo, 0, 16);
        WAITV(1); asm volatile("" : "+v"(hfl));
        STAMP(15);
        if (!__all(hfl == tag)) poll_flags((const u64*)(ws + WS_HF) + T2 * 32, 32, tag, lane, ws);
        STAMP(16);
#pragma unroll
        for (int i = 0; i < 22; ++i) LDH(i);
        __builtin_amdgcn_s_setprio(0);
    }
    STAMP(10);
    {
        const int lane = opaque(lane2), h = lane >> 5, r32 = lane & 31, q4 = (lane & 15) >> 2, p4 = lane & 3, b16 = (lane >> 4) & 1;
        const int tok = 64 * c.tb + 32 * th + r32;
        f32x16 acc0 = f32x16{}, acc1 = f32x16{};
        const unsigned wl = c.lds0 + kq * 2048 + (4 * h + q4) * 128 + 32 * b16 + 8 * p4, swz = ((q4 >> 1) & 1) << 6;
        const unsigned wl0 = wl + (0u ^ swz), wl1 = wl + (64u ^ swz);
        bf16x8 wa[3], wb_[3];
#define RDW(u) do { wa[(u) % 3] = trfrag<1024>((lcp)(uintptr_t)(wl0 + dloc(u))); wb_[(u) % 3] = trfrag<1024>((lcp)(uintptr_t)(wl1 + dloc(u))); } while (0)
#define G2STEP(u, frag, nxt) do { acc0 = mfma(wa[(u) % 3], frag, acc0); acc1 = mfma(wb_[(u) % 3], frag, acc1); SB(); if ((nxt) >= 0) RDW((nxt) < 0 ? 0 : (nxt)); SB(); } while (0)
        SYNC(63);
        STAMP(11);
        if (EXIT_AT == 4) goto done;
        const bool ew = blockIdx.x == 0 && w == 7; u32x4 sf0 = {0u, 0u, 0u, 0u}, sf1 = sf0; bool edone = false;
        if (ew) { aload16_sc1(sf0, P.ws + WS_SF, (unsigned)(lane * 32)); aload16_sc1(sf1, P.ws + WS_SF + 16, (unsigned)(lane * 32)); }
        RDW(0); RDW(1); RDW(2);
        G2STEP(0, hf[0], 3);
        if (lane == 0) store_flag(ws, WS_OF + ((size_t)T * 32 + 2 * c.cg + c.g) * 8, tag);
        issue_d(c, 13); G2STEP(1, hf[1], 4); issue_d(c, 14); G2STEP(2, hf[2], 5); issue_d(c, 15); G2STEP(3, hf[3], 6); issue_d(c, 16); G2STEP(4, hf[4], 7); issue_d(c, 17); G2STEP(5, hf[5], 8); issue_d(c, 18);
        SYNCV(63);
        G2STEP(6, hf[6], 9); issue_d(c, 19); G2STEP(7, hf[7], 10); issue_d(c, 20); G2STEP(8, hf[8], 11); issue_d(c, 21); G2STEP(9, hf[9], 12); issue_d(c, 22);
        G2STEP(10, hf[10], -1); issue_d(c, 23); G2STEP(11, hf[11], -1); issue_d(c, 24); G2STEP(12, hf[12], -1);
        STAMP(24);
        SYNCV(6);
        STAMP(25);
        if (ew) { asm volatile("" : "+v"(sf0), "+v"(sf1));
            edone = __all(sf0[0] == tagLo && sf0[1] == tagHi && sf0[2] == tagLo && sf0[3] == tagHi && sf1[0] == tagLo && sf1[1] == tagHi && sf1[2] == tagLo && sf1[3] == tagHi);
            if (edone && lane == 0) store_flag(ws, WS_EPOCH, tag); }
        edone_ = edone;
        RDW(13); RDW(14); RDW(15);
        uint2 xa, xb2;
        const int nt = (w >> 1) & 1, rh = w >> 2;
        const int n0 = 64 * c.nb + 32 * nt + 16 * rh + 4 * h;
        u64 ofl; aload_flag(ofl, P.ws, (unsigned)(WS_OF + (T2 * 32 + (lane & 31)) * 8));
        issue_d(c, 25); G2STEP(13, hf[13], 16); issue_d(c, 26); G2STEP(14, hf[14], 17); issue_d(c, 27); G2STEP(15, hf[15], 18); issue_d(c, 28); G2STEP(16, hf[16], -1); issue_d(c, 29);
        WAITV(5); asm volatile("" : "+v"(ofl));
        if (!__all(ofl == tag)) poll_flags((const u64*)(ws + WS_OF) + T2 * 32, 32, tag, lane, ws);
        STAMP(26);
        bf16x8 of[8];
#pragma unroll
        for (int i = 0; i < 8; ++i) of[i] = __builtin_bit_cast(bf16x8, __builtin_amdgcn_raw_buffer_load_b128(wsr, cbase + (86 + 4 * i) * 1024, 0, 16));
        { const u16* xr = P.x + (size_t)tok * 512 + n0; xa = *(const uint2*)xr; xb2 = *(const uint2*)(xr + 8); }
        G2STEP(17, hf[17], -1); G2STEP(18, hf[18], -1);
        STAMP(12);
        SYNCV(63);
        RDW(19); RDW(20); RDW(21);
        G2STEP(19, hf[19], 22); G2STEP(20, hf[20], 23);
        if (kq < 2) { acc0 = mfma(wa[21 % 3], hf[21], acc0); acc1 = mfma(wb_[21 % 3], hf[21], acc1); }
        SB(); RDW(24); SB();
        STAMP(13);
        G2STEP(22, of[0], -1); G2STEP(23, of[1], -1); G2STEP(24, of[2], -1);
        SYNCV(63);
        RDW(25); RDW(26); RDW(27);
        G2STEP(25, of[3], 28); G2STEP(26, of[4], 29); G2STEP(27, of[5], -1); G2STEP(28, of[6], -1); G2STEP(29, of[7], -1);
        STAMP(27);
        { lp rbp = c.lds + 4 * GR + (kq * 2 + th) * 8192 + lane * 16;
          SYNCV(63);
#pragma unroll
          for (int q = 0; q < 4; ++q) { *(LAS f32x4*)(rbp + q * 1024) = F4(acc0, 4 * q); *(LAS f32x4*)(rbp + 4096 + q * 1024) = F4(acc1, 4 * q); } }
        SYNC(63);
        { f32x4 s0 = {0.f, 0.f, 0.f, 0.f}, s1 = s0;
#pragma unroll
          for (int k = 0; k < 4; ++k) { const lcp bp = (lcp)c.lds + 4 * GR + (k * 2 + th) * 8192 + nt * 4096 + 2 * rh * 1024 + lane * 16; s0 += ldsf4(bp); s1 += ldsf4(bp + 1024); }
          uint2 oa, ob;
          oa.x = pk2(s0[0] + blo(xa.x), s0[1] + bhi(xa.x)); oa.y = pk2(s0[2] + blo(xa.y), s0[3] + bhi(xa.y));
          ob.x = pk2(s1[0] + blo(xb2.x), s1[1] + bhi(xb2.x)); ob.y = pk2(s1[2] + blo(xb2.y), s1[3] + bhi(xb2.y));
          u16* orow = P.out + (size_t)tok * 512 + n0;
          *(uint2*)orow = oa; *(uint2*)(orow + 8) = ob; }
    }
    STAMP(14);
    }
done:
    if (blockIdx.x == 0 && w == 7 && !edone_) {
        const GAS u64* pp = (const GAS u64*)(ws + WS_SF) + lane * 4;
        for (unsigned spins = 0;; ++spins) { bool ok = true;
#pragma unroll
            for (int j = 0; j < 4; ++j) ok &= __hip_atomic_load(pp + j, __ATOMIC_RELAXED, __HIP_MEMORY_SCOPE_AGENT) == tag;
            if (__all(ok)) break; if (spins > SPIN_CAP) { set_timeout(ws, lane); break; } __builtin_amdgcn_s_sleep(2); }
        if (lane == 0) store_flag(ws, WS_EPOCH, tag);
    }
}

extern "C" void kernel_launch(void* const* d_in, const int* in_sizes, int n_in, void* d_out, int out_size, void* d_ws, size_t ws_size, hipStream_t stream) {
    Params p;
    p.x = (const u16*)d_in[0]; p.wqkv = (const u16*)d_in[1]; p.wo = (const u16*)d_in[2];
    p.wg = (const u16*)d_in[4]; p.wu = (const u16*)d_in[5]; p.wd = (const u16*)d_in[6];
    p.out = (u16*)d_out; p.ws = (char*)d_ws;
    (void)hipFuncSetAttribute((const void*)fused_kernel, hipFuncAttributeMaxDynamicSharedMemorySize, LDS_BYTES);
    fused_kernel<<<256, 512, LDS_BYTES, stream>>>(p);
}
```

```cpp
#include <hip/hip_runtime.h>
#include <stdint.h>
#include <type_traits>

#define FI __device__ __forceinline__
#define LAS __attribute__((address_space(3)))
#define GAS __attribute__((address_space(1)))
typedef unsigned short u16;
typedef unsigned long long u64;
typedef short bf16x8 __attribute__((ext_vector_type(8)));
typedef short s16x4 __attribute__((ext_vector_type(4)));
typedef float f32x16 __attribute__((ext_vector_type(16)));
typedef float f32x4 __attribute__((ext_vector_type(4)));
typedef float f32x2 __attribute__((ext_vector_type(2)));
typedef unsigned u32x4 __attribute__((ext_vector_type(4)));
typedef __bf16 bf16x2_t __attribute__((ext_vector_type(2)));
typedef LAS const char* lcp;
typedef LAS char* lp;

constexpr int GR = 8192;
constexpr int L_KV = 13 * GR;
constexpr int L_KVR = 17 * GR;
constexpr int L_MISC = 19 * GR;
constexpr int L_CS = L_MISC;
constexpr int L_CSW = L_MISC + 2048;
constexpr int L_ZERO = L_MISC + 4096;
constexpr int L_PART = 6 * GR;
constexpr int L_ONES = L_MISC + 6144;
constexpr int L_CNT = L_ONES + 64;
constexpr int L_FINE = L_MISC + 6400;
constexpr int LDS_BYTES = 163840;

constexpr size_t WS_EPOCH = 0, WS_TMO = 64;
constexpr size_t WS_SF = 4096;
constexpr size_t WS_KVF = 8192;
constexpr size_t WS_HF = 24576;
constexpr size_t WS_OF = 40960;
constexpr size_t WS_ST = 65536;
constexpr size_t WS_CS = 131072;
constexpr size_t WS_SCR = 1u << 19;
constexpr size_t WS_KV = 1u << 20;
constexpr size_t WS_C = 8u << 20;
constexpr size_t WS_DBG = 32u << 20;
constexpr int NKS = 118;
constexpr unsigned CODE_PF_BYTES = 43008;

FI f32x16 mfma(bf16x8 a, bf16x8 b, f32x16 c) { return __builtin_amdgcn_mfma_f32_32x32x16_bf16(a, b, c, 0, 0, 0); }
FI f32x16 mfma_pin(bf16x8 a, bf16x8 b, const f32x16& c) { f32x16 d; asm volatile("v_mfma_f32_32x32x16_bf16 %0, %1, %2, %3" : "=&v"(d) : "v"(a), "v"(b), "v"(c)); return d; }
FI f32x16 mfma_pin0(bf16x8 a, bf16x8 b) { f32x16 d; asm volatile("v_mfma_f32_32x32x16_bf16 %0, %1, %2, 0" : "=&v"(d) : "v"(a), "v"(b)); return d; }
FI unsigned pk2(float lo, float hi) { f32x2 v = {lo, hi}; bf16x2_t r = __builtin_convertvector(v, bf16x2_t); return __builtin_bit_cast(unsigned, r); }
FI u32x4 pack8(const f32x16& y, int b) { u32x4 r; r[0] = pk2(y[b], y[b + 1]); r[1] = pk2(y[b + 2], y[b + 3]); r[2] = pk2(y[b + 4], y[b + 5]); r[3] = pk2(y[b + 6], y[b + 7]); return r; }
typedef short v4i16_t __attribute__((ext_vector_type(4)));
FI s16x4 trd(lcp p) { return __builtin_bit_cast(s16x4, __builtin_amdgcn_ds_read_tr16_b64_v4i16((LAS v4i16_t*)p)); }
template <int D2> FI bf16x8 trfrag(lcp p) { s16x4 lo = trd(p), hi = trd(p + D2); return __builtin_shufflevector(lo, hi, 0, 1, 2, 3, 4, 5, 6, 7); }
FI bf16x8 ldsf(lcp p) { return *(const LAS bf16x8*)p; }
FI f32x4 ldsf4(lcp p) { return *(const LAS f32x4*)p; }

FI void dma16(unsigned voff, const void* sbase, unsigned dst) {
    asm volatile("s_mov_b32 m0, %2\n\ts_nop 0\n\tglobal_load_lds_dwordx4 %0, %1" :: "v"(voff), "s"(sbase), "s"(__builtin_amdgcn_readfirstlane(dst)) : "memory", "m0");
}
FI void dma16_sc1(unsigned voff, const void* sbase, unsigned dst) {
    asm volatile("s_mov_b32 m0, %2\n\ts_nop 0\n\tglobal_load_lds_dwordx4 %0, %1 sc1" :: "v"(voff), "s"(sbase), "s"(__builtin_amdgcn_readfirstlane(dst)) : "memory", "m0");
}
FI void aload16(u32x4& v, const void* base, unsigned voff) { asm volatile("global_load_dwordx4 %0, %1, %2" : "=v"(v) : "v"(voff), "s"(base) : "memory"); }
FI void aload16_sc1(u32x4& v, const void* base, unsigned voff) { asm volatile("global_load_dwordx4 %0, %1, %2 sc1" : "=v"(v) : "v"(voff), "s"(base) : "memory"); }
FI void aload_flag(u64& v, const char* base, unsigned voff) { asm volatile("global_load_dwordx2 %0, %1, %2 sc1" : "=v"(v) : "v"(voff), "s"(base) : "memory"); }

FI void aprefetch(unsigned& d, const char* p) { asm volatile("global_load_dword %0, %1, off" : "+v"(d) : "v"(p) : "memory"); }
#ifndef EXIT_AT
#define EXIT_AT 99
#endif
#define SB() __builtin_amdgcn_sched_barrier(0)
#define PIN(x) asm volatile("" : "+v"(x))
FI int opaque(int v) { asm volatile("" : "+v"(v)); return v; }
#define FST() do {} while (0)
#define SYNC(N) do { SB(); FST(); asm volatile("s_waitcnt vmcnt(" #N ") lgkmcnt(0)\n\ts_barrier" ::: "memory"); FST(); SB(); } while (0)
#define SYNCV(N) do { SB(); FST(); asm volatile("s_waitcnt vmcnt(" #N ")\n\ts_barrier" ::: "memory"); FST(); SB(); } while (0)
#define WAITV(N) do { SB(); asm volatile("s_waitcnt vmcnt(" #N ")" ::: "memory"); SB(); } while (0)

#define STAMP(k) do {} while (0)

struct Params {
    const u16* x; const u16* wqkv; const u16* wo; const u16* wg; const u16* wu; const u16* wd; u16* out; char* ws;
};

constexpr int wslot(int s) { return (s == 0 ? 4 : s == 1 ? 11 : s == 2 ? 0 : s == 3 ? 7 : s == 4 ? 10 : s == 5 ? 13 : s == 6 ? 0 : s == 7 ? 3 : s == 8 ? 6 : s == 9 ? 9 : s == 10 ? 0 : 3) * GR; }
constexpr int xslot(int c) { return (c == 0 ? 0 : c == 1 ? 2 : c == 2 ? 7 : c == 3 ? 9 : c == 4 ? 14 : c == 5 ? 17 : c == 6 ? 3 : 5) * GR; }
FI constexpr int dloc0(int u) { return (u == 0 ? 12 : u < 4 ? 6 + (u - 1) : u < 7 ? 9 + (u - 4) : u < 9 ? u - 7 : 2 + (u - 9)) * GR; }
FI constexpr int dloc(int u) { return u < 13 ? dloc0(u) : u < 19 ? L_KV + (u - 13) * GR : dloc0(u - 19); }

struct Ctx {
    const Params* p;
    unsigned lds0;
    lp lds;
    int lane, w, tm, g, rb, cg, tb, nb;
    unsigned vq, vf0, vf1, vf2, vx, vlin, vd;
    const char *px, *pq, *pf, *pwd, *pwo;
};

FI void issue_x1(const Ctx& c, int chunk, int k) {
    dma16(k ? c.vx ^ 64u : c.vx, c.px + chunk * 128 + k * 8 * 1024, c.lds0 + xslot(chunk) + 16 * c.w * 128 + k * 8 * 128);
}
FI void issue_x(const Ctx& c, int chunk) { issue_x1(c, chunk, 0); issue_x1(c, chunk, 1); }
template <int S> FI void issue_w1(const Ctx& c, int k) {
    if constexpr (S >= 4) {
        const char* base = c.pf + (size_t)(64 * (S - 4)) * 2752;
        dma16(k == 0 ? c.vf0 : k == 1 ? c.vf1 : c.vf2, base, c.lds0 + wslot(S) + c.g * 12288 + c.tm * 3072 + k * 1024);
    } else {
        const char* base = c.pq + (size_t)(128 * S) * 3072;
        dma16(c.vq, base + (k == 0 ? 1024 : k == 1 ? 2048 : 0), c.lds0 + wslot(S) + c.w * 1024 + k * 8192);
    }
}
template <int S> FI void issue_w(const Ctx& c) { issue_w1<S>(c, 0); issue_w1<S>(c, 1); issue_w1<S>(c, 2); }
FI void issue_d(const Ctx& c, int u) {
    const char* base;
    if (u < 21) base = c.pwd + (size_t)u * 65536;
    else if (u == 21) base = c.pwd + (size_t)21 * 65536 - (c.w >= 4 ? 32768 : 0);
    else base = c.pwo + (size_t)(u - 22) * 65536;
    dma16(c.vd, base, c.lds0 + dloc(u) + c.w * 1024);
}

FI float blo(unsigned d) { return __uint_as_float(d << 16); }
FI float bhi(unsigned d) { return __uint_as_float(d & 0xffff0000u); }
template <int CTRL> FI float dppadd(float x) { return x + __builtin_bit_cast(float, __builtin_amdgcn_update_dpp(0, __builtin_bit_cast(int, x), CTRL, 0xf, 0xf, true)); }
FI void swap32add(float& u, float v) { auto rr = __builtin_amdgcn_permlane32_swap(__float_as_uint(u), __float_as_uint(v), false, false); u = __uint_as_float(rr[0]) + __uint_as_float(rr[1]); }
FI void swap16add(float& u, float v) { auto rr = __builtin_amdgcn_permlane16_swap(__float_as_uint(u), __float_as_uint(v), false, false); u = __uint_as_float(rr[0]) + __uint_as_float(rr[1]); }

FI f32x4 m4(s16x4 a, s16x4 b, f32x4 c) { return __builtin_amdgcn_mfma_f32_4x4x4bf16_1k(a, b, c, 0, 0, 0); }
FI s16x4 lo4(const bf16x8& f) { return __builtin_shufflevector(f, f, 0, 1, 2, 3); }
FI s16x4 hi4(const bf16x8& f) { return __builtin_shufflevector(f, f, 4, 5, 6, 7); }
constexpr s16x4 ONES4 = {0x3f80, 0x3f80, 0x3f80, 0x3f80};
FI void stat4(const bf16x8& f, f32x4& s, f32x4& q) { s = m4(ONES4, lo4(f), s); q = m4(lo4(f), lo4(f), q); s = m4(ONES4, hi4(f), s); q = m4(hi4(f), hi4(f), q); }
FI void sum4(const bf16x8& f, f32x4& s) { s = m4(ONES4, lo4(f), s); s = m4(ONES4, hi4(f), s); }
FI float halfsum(float v) { auto rr = __builtin_amdgcn_permlane32_swap(__float_as_uint(v), __float_as_uint(v), false, false); return __uint_as_float(rr[0]) + __uint_as_float(rr[1]); }
struct G1State {
    bf16x8 xf[32];
    bf16x8 wq[4];
    f32x16 a0, a1, b0, b1;
    unsigned wb[4];
    unsigned xb[4];
    f32x4 cs0, cs1;
};
constexpr int fkind(int G, int f) { return G == 0 ? (f < 8 ? (f & 3) : (f & 1)) : (f < 4 ? (f & 1) : f < 6 ? (f & 1) : f < 8 ? 2 + (f & 1) : f < 10 ? (f & 1) : 2 + (f & 1)); }
constexpr int fpos(int G, int f) { return G == 0 ? (f < 8 ? (f >> 2) : 2 + ((f - 8) >> 1)) : (f < 4 ? (f >> 1) : f < 8 ? 2 : 3); }
constexpr int qkind(int G, int f) { return G == 0 ? ((f == 1 || f == 3 || f == 7 || f == 9) ? 1 : 0) : ((f == 3 || f == 5 || f == 9 || f == 11) ? 1 : 0); }
constexpr int qpos(int G, int f) {
    return G == 0 ? (f == 0 ? 0 : f == 1 ? 0 : f == 2 ? 1 : f == 3 ? 1 : f == 4 ? 2 : f == 5 ? 3 : f == 6 ? 4 : f == 7 ? 4 : f == 8 ? 5 : f == 9 ? 5 : f == 10 ? 6 : 7)
                  : (f == 0 ? 0 : f == 1 ? 1 : f == 2 ? 2 : f == 3 ? 2 : f == 4 ? 3 : f == 5 ? 3 : f == 6 ? 4 : f == 7 ? 5 : f == 8 ? 6 : f == 9 ? 6 : f == 10 ? 7 : 7);
}
template <int G, int S> FI bf16x8 rdfrag(const G1State& st, int f) {
    if constexpr (S >= 4) { const int k = fkind(G, f); return trfrag<768>((lcp)(uintptr_t)(st.wb[fpos(G, f)] + (k & 1) * 12288 + (k >= 2 ? 64 : (G == 0 ? 0 : 128)))); }
    else { const int e = qpos(G, f); return trfrag<256>((lcp)(uintptr_t)(st.wb[e & 3] + (e >> 2) * 4096 + (qkind(G, f) ? 16384 : G * 8192))); }
}
template <int C> FI void rdx(G1State& st) {
#pragma unroll
    for (int e = 0; e < 4; ++e) { st.xf[4 * C + e] = ldsf((lcp)(uintptr_t)st.xb[e]); if (C < 7) st.xb[e] = (unsigned)opaque((int)(st.xb[e] + (xslot(C + 1) - xslot(C)))); }
}
template <int G, int S, bool FRESH, bool PF, class F>
FI void stage(G1State& st, F&& hook) {
    if constexpr (FRESH) {
        if constexpr (S < 4) rdx<(S < 4 ? 2 * S : 0)>(st);
#pragma unroll
        for (int f = 0; f < 4; ++f) st.wq[f] = rdfrag<G, S>(st, f);
        if constexpr (S < 4) rdx<(S < 4 ? 2 * S + 1 : 0)>(st);
        SB();
    }
#pragma unroll
    for (int f = 0; f < 12; ++f) {
        const bf16x8 w = st.wq[f & 3];
        if constexpr (S >= 4) {
            const int k = fkind(G, f); const bf16x8 x = st.xf[4 * (S - 4) + fpos(G, f)];
            if (k == 0) st.a0 = mfma(w, x, st.a0); else if (k == 1) st.a1 = mfma(w, x, st.a1); else if (k == 2) st.b0 = mfma(w, x, st.b0); else st.b1 = mfma(w, x, st.b1);
        } else {
            const bf16x8 x = st.xf[8 * S + qpos(G, f)];
            if (qkind(G, f)) st.b0 = mfma(w, x, st.b0); else if (G == 0) st.a0 = mfma(w, x, st.a0); else st.a0 = mfma(x, w, st.a0);
        }
        SB();
        if (f < 8) st.wq[f & 3] = rdfrag<G, S>(st, f + 4);
        else {
            if (f == 8 && S != 3 && S != 11) { _Pragma("unroll") for (int j = 0; j < 4; ++j) st.wb[j] = (unsigned)opaque((int)(st.wb[j] + (wslot(S + 1) - wslot(S)))); }
            if (PF) st.wq[f & 3] = rdfrag<G, (PF ? S + 1 : S)>(st, f - 8);
        }
        if constexpr (S < 4) {
            if (qkind(G, f) == 0 && (qpos(G, f) & 3) == 0) sum4(w, st.cs0);
            if (qkind(G, f) == 1 && qpos(G, f) == (G == 0 ? 0 : 6)) sum4(w, st.cs1);
        }
        hook(f);
        SB();
    }
}

FI u64 mix64(u64 z) { z += 0x9E3779B97F4A7C15ull; z = (z ^ (z >> 30)) * 0xBF58476D1CE4E5B9ull; z = (z ^ (z >> 27)) * 0x94D049BB133111EBull; return z ^ (z >> 31); }

constexpr unsigned SPIN_CAP = 1u << 18;
FI void set_timeout(char* ws, int lane) { if (lane == 0) __hip_atomic_store((GAS unsigned*)(ws + WS_TMO), 1u, __ATOMIC_RELAXED, __HIP_MEMORY_SCOPE_AGENT); }
FI void poll_flags(const u64* flags, int n, u64 tag, int lane, char* ws) {
    const GAS u64* pp = (const GAS u64*)flags + (lane < n ? lane : 0);
    for (unsigned spins = 0;; ++spins) {
        const u64 v = __hip_atomic_load(pp, __ATOMIC_RELAXED, __HIP_MEMORY_SCOPE_AGENT);
        if (__all(v == tag)) return;
        if (spins > SPIN_CAP) { set_timeout(ws, lane); return; }
        __builtin_amdgcn_s_sleep(2);
    }
}
FI void store_flag(char* ws, size_t off, u64 tag) { __hip_atomic_store((GAS u64*)(ws + off), tag, __ATOMIC_RELAXED, __HIP_MEMORY_SCOPE_AGENT); }

__global__ __launch_bounds__(512, 2) void fused_kernel(Params P) {
    extern __shared__ __attribute__((aligned(16))) char smem[];
    Ctx c; c.p = &P;
    const int tid = threadIdx.x; c.lane = tid & 63;
    unsigned pfd = 0;
    { const char* pc = (const char*)__builtin_amdgcn_s_getpc(); unsigned off = ((unsigned)(blockIdx.x >> 3) * 16u + (tid & 15)) * 128u; if (off > CODE_PF_BYTES) off = CODE_PF_BYTES; if (tid < 16) aprefetch(pfd, pc + off); }
    const int w = __builtin_amdgcn_readfirstlane(tid >> 6);
    c.w = w; c.tm = w & 3; c.g = w >> 2;
    c.lds = (lp)smem; c.lds0 = (unsigned)(uintptr_t)c.lds;
    const int xcd = blockIdx.x & 7, idx = blockIdx.x >> 3;
    c.rb = 4 * (xcd >> 1) + (idx >> 3); c.cg = 8 * (xcd & 1) + (idx & 7);
    c.tb = 4 * xcd + (idx >> 3); c.nb = idx & 7;
    const int fs = c.cg < 11 ? 3 * c.cg : c.cg < 15 ? 33 + 2 * (c.cg - 11) : 40;
    const bool v0 = c.cg != 15, v2 = c.cg < 11 || c.cg == 15;
    const int lane = c.lane, h = lane >> 5, r32 = lane & 31;
    char* ws = P.ws;
    STAMP(0);
    c.px = (const char*)P.x + (size_t)(128 * c.rb + 16 * w) * 1024;
    c.pf = (const char*)(c.g ? P.wu : P.wg) + (size_t)(16 * c.tm) * 2752 + fs * 64;
    u64 tag = 0; unsigned tagLo = 0, tagHi = 0; float ln_a = 0.f, ln_m = 0.f;
    const int T = 4 * c.rb + c.tm;
    const __amdgpu_buffer_rsrc_t wsr = __builtin_amdgcn_make_buffer_rsrc(ws, 0, 32u << 20, 0x00020000);
    c.pq = (const char*)P.wqkv + ((size_t)(16 * w) * 1536 + 32 * c.cg) * 2;
    c.pwd = (const char*)P.wd + (size_t)(8 * w) * 1024 + c.nb * 128;
    c.pwo = (const char*)P.wo + (size_t)(8 * w) * 1024 + c.nb * 128;
    u32x4 g0a, g0b;
#define GATHER_OFFS(tile) const int gl_ = opaque(lane); const unsigned go0 = (unsigned)(WS_CS + (c.cg * 9 + (tile)) * 2048 + (gl_ >> 3) * 256 + (gl_ & 7) * 32)
#define GATHER_ISSUE(tile, ST) do { GATHER_OFFS(tile); aload16_sc1(g0a, P.ws, go0); aload16_sc1(g0b, P.ws + 16, go0); } while (0)
#define GATHER_OK(ST) (g0a[1] == tagLo && g0a[3] == tagLo && g0b[1] == tagLo && g0b[3] == tagLo)
#define GATHER_RED(tile) do { const int gl_ = opaque(lane); float v0_ = __uint_as_float(g0a[0]), v1_ = __uint_as_float(g0a[2]), v2_ = __uint_as_float(g0b[0]), v3_ = __uint_as_float(g0b[2]); \
        swap32add(v0_, v2_); swap32add(v1_, v3_); swap16add(v0_, v1_); v0_ = dppadd<0x128>(v0_); \
        if ((gl_ & 8) == 0) *(LAS float*)(c.lds + L_CS + (tile) * 128 + (4 * (gl_ & 7) + 2 * (gl_ >> 5) + ((gl_ >> 4) & 1)) * 4) = v0_; } while (0)
#define GATHER_CHECK(tile, ST) do { asm volatile("" : "+v"(g0a), "+v"(g0b)); \
        if (!__all(GATHER_OK(ST))) { GATHER_OFFS(tile); \
            for (unsigned spins = 0;; ++spins) { __builtin_amdgcn_s_sleep(2); \
                g0a = __builtin_amdgcn_raw_buffer_load_b128(wsr, go0, 0, 16); g0b = __builtin_amdgcn_raw_buffer_load_b128(wsr, go0 + 16, 0, 16); __builtin_amdgcn_s_waitcnt(0x0F70); \
                if (__all(GATHER_OK(ST))) break; if (spins > SPIN_CAP) { set_timeout(ws, lane); break; } } } \
        } while (0)
#define RD_CS(dst, tile) do { _Pragma("unroll") for (int q_ = 0; q_ < 4; ++q_) { const f32x4 t_ = ldsf4((lcp)c.lds + L_CS + (tile) * 128 + (8 * q_ + 4 * h) * 4); \
            dst[4 * q_] = t_[0]; dst[4 * q_ + 1] = t_[1]; dst[4 * q_ + 2] = t_[2]; dst[4 * q_ + 3] = t_[3]; } } while (0)
#define SPREAD(dst, v, row) do { if (h == 0) *(LAS float*)(c.lds + L_CSW + c.tm * 256 + (row) * 128 + r32 * 4) = (v); \
        _Pragma("unroll") for (int q_ = 0; q_ < 4; ++q_) { const f32x4 t_ = ldsf4((lcp)c.lds + L_CSW + c.tm * 256 + (row) * 128 + (8 * q_ + 4 * h) * 4); \
            dst[4 * q_] = t_[0]; dst[4 * q_ + 1] = t_[1]; dst[4 * q_ + 2] = t_[2]; dst[4 * q_ + 3] = t_[3]; } } while (0)
#define F4(v, b) (f32x4){v[b], v[(b) + 1], v[(b) + 2], v[(b) + 3]}

    const int th = w & 1, kq = w >> 1;
    const int T2 = 2 * c.tb + th;
    bf16x8 qf; bool edone_ = false;
    auto gemm1 = [&](auto Gc) {
        constexpr int G = decltype(Gc)::value;
        const int lane = opaque(c.lane), h = lane >> 5, r32 = lane & 31, tid = w * 64 + lane;
        c.vq = (lane >> 2) * 3072 + (lane & 3) * 16;
        c.vx = (lane >> 3) * 1024 + (((lane & 7) ^ (lane >> 4)) * 16); c.vlin = lane * 16;
        G1State st;
        issue_x(c, 0); issue_x(c, 1); issue_w<0>(c);
        u64 ep; aload_flag(ep, ws + WS_EPOCH, 0u);
        u32x4 ax; aload16(ax, (const char*)P.x + (size_t)(128 * c.rb + 8 * c.cg + w) * 1024, c.vlin);
        u32x4 aw0, aw1, aw2, aw3;
        const int ptile = 3 + 2 * (w < 3 ? w : 0) + (c.rb & 1);
        { const char* tb = (const char*)((c.rb & 1) ? P.wu : P.wg) + 64 * (fs + (w < 3 ? w : 0)) + (size_t)(64 * (c.rb >> 1)) * 2752; const unsigned vo = (lane >> 2) * 2752 + (lane & 3) * 16;
          aw0 = aw1 = aw2 = aw3 = (u32x4){0u, 0u, 0u, 0u};
          if (w < 3) { aload16(aw0, tb, vo); aload16(aw1, tb + 16 * 2752, vo); aload16(aw2, tb + 32 * 2752, vo); aload16(aw3, tb + 48 * 2752, vo); } }
        issue_x(c, 2); issue_x(c, 3); issue_w<1>(c); issue_x(c, 4); issue_x(c, 5);
        unsigned pfw = 0;
        { const int l3 = lane & 7; size_t off;
          if (l3 == 0) off = WS_KVF + ((size_t)(c.cg * 64 + T) * 2 + G) * 8;
          else if (l3 == 1) off = WS_HF + ((size_t)T * 32 + 2 * c.cg + G) * 8;
          else if (l3 == 2) off = WS_OF + ((size_t)T * 32 + 2 * c.cg + G) * 8;
          else if (l3 == 3) off = WS_ST + (size_t)(128 * c.rb + 8 * c.cg + w) * 16;
          else if (l3 == 4) off = WS_SF + (size_t)blockIdx.x * 8;
          else if (l3 == 5) off = WS_EPOCH;
          else off = WS_CS + (size_t)(((c.cg * 9 + ptile) * 8 + (c.rb >> 1)) * 256 + (l3 & 1) * 128);
          aprefetch(pfw, ws + off); }
        *(LAS unsigned*)(c.lds + L_ZERO + tid * 4) = 0u;
        if (tid < 4) { *(LAS unsigned*)(c.lds + L_ONES + tid * 4) = 0x3f803f80u; *(LAS unsigned*)(c.lds + L_ONES + 1024 + tid * 4) = 0x3f803f80u; }
        if (tid >= 64 && tid < 72) *(LAS unsigned*)(c.lds + L_CNT + (tid - 64) * 4) = 0u;
        c.vf0 = (lane / 12) * 2752 + (lane % 12) * 16; c.vf1 = ((lane + 64) / 12) * 2752 + ((lane + 64) % 12) * 16; c.vf2 = ((lane + 128) / 12) * 2752 + ((lane + 128) % 12) * 16;
        { const unsigned lb = c.lds0 + (8 * h + ((lane & 15) >> 2)) * 64 + 32 * ((lane >> 4) & 1) + 8 * (lane & 3);
#pragma unroll
          for (int j = 0; j < 4; ++j) st.wb[j] = lb + ((j ^ c.tm) * 1024) + wslot(0); }
        { const int row = 32 * c.tm + r32;
#pragma unroll
          for (int j = 0; j < 4; ++j) st.xb[j] = c.lds0 + xslot(0) + row * 128 + (((2 * (j ^ c.tm) + h) ^ ((row >> 1) & 7)) * 16); }
        st.a0 = f32x16{}; st.a1 = f32x16{}; st.b0 = f32x16{}; st.b1 = f32x16{}; st.cs0 = f32x4{}; st.cs1 = f32x4{};

        SYNC(12);
        PIN(pfd);
        if (EXIT_AT == 0) { tag = mix64(((u64)(unsigned)__builtin_amdgcn_readfirstlane((int)(unsigned)(ep >> 32)) << 32) | (unsigned)__builtin_amdgcn_readfirstlane((int)(unsigned)ep)); if (w == 0 && lane == 0) store_flag(ws, WS_SF + (size_t)blockIdx.x * 8, tag); return; }
        asm volatile("" : "+v"(ep), "+v"(ax), "+v"(aw0), "+v"(aw1), "+v"(aw2), "+v"(aw3));
        tag = mix64(((u64)(unsigned)__builtin_amdgcn_readfirstlane((int)(unsigned)(ep >> 32)) << 32) | (unsigned)__builtin_amdgcn_readfirstlane((int)(unsigned)ep));
        tagLo = (unsigned)tag; tagHi = (unsigned)(tag >> 32);
        STAMP(1);

        float as_ = 0.f, aq_ = 0.f, av[8];
        auto aux0 = [&](int f) {
            if (f < 4) { const float a = blo(ax[f]), b = bhi(ax[f]); as_ += a + b; aq_ = __builtin_fmaf(a, a, aq_); aq_ = __builtin_fmaf(b, b, aq_); PIN(as_); PIN(aq_); }
            if (f == 4) { as_ = dppadd<0x121>(as_); aq_ = dppadd<0x121>(aq_); as_ = dppadd<0x122>(as_); aq_ = dppadd<0x122>(aq_); PIN(as_); PIN(aq_); }
            if (f == 5) { as_ = dppadd<0x124>(as_); aq_ = dppadd<0x124>(aq_); as_ = dppadd<0x128>(as_); aq_ = dppadd<0x128>(aq_); PIN(as_); PIN(aq_); }
            if (f == 6) { swap16add(as_, as_); swap16add(aq_, aq_); swap32add(as_, as_); swap32add(aq_, aq_); PIN(as_); PIN(aq_); }
            if (f == 7) { const float mean = as_ * (1.f / 512.f), var = aq_ * (1.f / 512.f) - mean * mean, rstd = __builtin_amdgcn_rsqf(fmaxf(var, 0.f) + 1e-5f);
                if (lane == 0) __builtin_amdgcn_raw_buffer_store_b128((u32x4){__float_as_uint(mean), tagLo, __float_as_uint(rstd), tagHi}, wsr, (unsigned)(WS_ST + (128 * c.rb + 8 * c.cg + w) * 16), 0, 16); }
            if (f >= 8) { const int i = f - 8; av[2 * i] = (blo(aw0[i]) + blo(aw1[i])) + (blo(aw2[i]) + blo(aw3[i])); av[2 * i + 1] = (bhi(aw0[i]) + bhi(aw1[i])) + (bhi(aw2[i]) + bhi(aw3[i])); PIN(av[2 * i]); PIN(av[2 * i + 1]); }
        };
        auto aux1 = [&](int f) {
            if (f == 8) { swap32add(av[0], av[4]); swap32add(av[1], av[5]); swap32add(av[2], av[6]); swap32add(av[3], av[7]); PIN(av[0]); PIN(av[1]); PIN(av[2]); PIN(av[3]); }
            if (f == 9) { swap16add(av[0], av[2]); swap16add(av[1], av[3]); av[0] = dppadd<0x124>(av[0]); av[1] = dppadd<0x124>(av[1]); av[0] = dppadd<0x128>(av[0]); av[1] = dppadd<0x128>(av[1]); PIN(av[0]); PIN(av[1]); }
        };
        u32x4 gs;
        const int gt2 = w < 6 ? 3 + w : w - 3;
        stage<G, 0, true, false>(st, [&](int f_) { aux0(f_); }); SYNCV(6);
        stage<G, 1, true, false>(st, [&](int f_) { if (f_ < 3) issue_w1<2>(c, f_); else if (f_ < 5) issue_x1(c, 6, f_ - 3); else if (f_ < 7) issue_x1(c, 7, f_ - 5); aux1(f_); }); SYNCV(4);
        PIN(pfw);
        stage<G, 2, true, false>(st, [&](int f_) { if (f_ < 3) issue_w1<3>(c, f_); else if (f_ < 6) issue_w1<4>(c, f_ - 3);
            if (f_ == 9) aload16_sc1(gs, P.ws, (unsigned)(WS_ST + (128 * c.rb + 32 * c.tm + (opaque(lane) & 31)) * 16)); }); SYNCV(4);
        stage<G, 3, true, false>(st, [&](int f_) { if (f_ < 3) issue_w1<5>(c, f_); else if (f_ < 6) issue_w1<6>(c, f_ - 3);
            if (f_ == 10) { WAITV(6); PIN(gs);
                if (!__all(gs[1] == tagLo && gs[3] == tagHi)) { const unsigned gso = (unsigned)(WS_ST + (128 * c.rb + 32 * c.tm + (opaque(lane) & 31)) * 16);
                    for (unsigned spins = 0;; ++spins) { __builtin_amdgcn_s_sleep(2); gs = __builtin_amdgcn_raw_buffer_load_b128(wsr, gso, 0, 16); __builtin_amdgcn_s_waitcnt(0x0F70);
                        if (__all(gs[1] == tagLo && gs[3] == tagHi)) break; if (spins > SPIN_CAP) { set_timeout(ws, lane); break; } } }
                ln_m = __uint_as_float(gs[0]); ln_a = __uint_as_float(gs[2]); PIN(ln_m); PIN(ln_a); } });
        SYNCV(63);
        { lp xp = c.lds + (G == 0 ? 9 : 16) * GR + c.tm * 2048 + c.vlin; *(LAS f32x4*)(xp) = F4(st.b0, 8 - 8 * G); *(LAS f32x4*)(xp + 1024) = F4(st.b0, 12 - 8 * G);
          const float s0 = halfsum(st.cs0[0]), s1 = halfsum(st.cs1[0]);
          if (h == 0) { *(LAS float*)(c.lds + L_PART + (G * 4 + c.tm) * 128 + r32 * 4) = s0; *(LAS float*)(c.lds + L_PART + 1024 + (G * 4 + c.tm) * 128 + r32 * 4) = s1;
                        if constexpr (G == 1) { *(LAS float*)(c.lds + L_CSW + c.tm * 256 + r32 * 4) = ln_m; *(LAS float*)(c.lds + L_CSW + c.tm * 256 + 128 + r32 * 4) = ln_a; } } }
        { const int ln = opaque(lane); const unsigned lb = c.lds0 + (8 * (ln >> 5) + ((ln & 15) >> 2)) * 192 + 32 * ((ln >> 4) & 1) + 8 * (ln & 3);
#pragma unroll
          for (int j = 0; j < 4; ++j) st.wb[j] = lb + ((j ^ c.tm) * 3072) + wslot(4); }
        SYNC(6);
        STAMP(2);
        issue_w<7>(c);
        { const int lane = opaque(c.lane), h = lane >> 5, r32 = lane & 31;
          const unsigned o = (unsigned)(WS_KV + ((size_t)c.cg * 64 + T) * 4096) + lane * 16 + G * 2048;
          f32x16 y;
          if constexpr (G == 0) {
#pragma unroll
              for (int q = 0; q < 4; ++q) { f32x4 cs = ldsf4((lcp)c.lds + L_PART + (8 * q + 4 * h) * 4);
#pragma unroll
                  for (int t = 1; t < 4; ++t) cs += ldsf4((lcp)c.lds + L_PART + t * 128 + (8 * q + 4 * h) * 4);
#pragma unroll
                  for (int i = 0; i < 4; ++i) y[4 * q + i] = ln_a * __builtin_fmaf(-ln_m, cs[i], st.a0[4 * q + i]); }
          } else {
              const lcp pp = (lcp)c.lds + L_PART + 512 + r32 * 4;
              const float cso = (*(const LAS float*)pp + *(const LAS float*)(pp + 128)) + (*(const LAS float*)(pp + 256) + *(const LAS float*)(pp + 384));
#pragma unroll
              for (int q = 0; q < 4; ++q) { const f32x4 mm = ldsf4((lcp)c.lds + L_CSW + c.tm * 256 + (8 * q + 4 * h) * 4), aa = ldsf4((lcp)c.lds + L_CSW + c.tm * 256 + 128 + (8 * q + 4 * h) * 4);
#pragma unroll
                  for (int i = 0; i < 4; ++i) y[4 * q + i] = aa[i] * __builtin_fmaf(-mm[i], cso, st.a0[4 * q + i]); }
          }
          const u32x4 k0 = pack8(y, 0), k1 = pack8(y, 8);
          __builtin_amdgcn_raw_buffer_store_b128(k0, wsr, o, 0, 16); __builtin_amdgcn_raw_buffer_store_b128(k1, wsr, o + 1024, 0, 16);
          if (w < 3) { const int rho = lane >> 4, col = 8 * (lane & 3) + 4 * (rho >> 1) + 2 * (rho & 1);
            if ((lane & 12) == 0) __builtin_amdgcn_raw_buffer_store_b128((u32x4){__float_as_uint(av[0]), tagLo, __float_as_uint(av[1]), tagLo}, wsr, (unsigned)(WS_CS + (((c.cg * 9 + ptile) * 8 + (c.rb >> 1)) * 32 + col) * 8), 0, 16); }
          if (w == 0 && lane == 0) store_flag(ws, WS_SF + (size_t)blockIdx.x * 8, tag);
          *(LAS u32x4*)(c.lds + L_KVR + c.tm * 4096 + G * 2048 + lane * 16) = k0; *(LAS u32x4*)(c.lds + L_KVR + c.tm * 4096 + G * 2048 + 1024 + lane * 16) = k1;
          SB();
          f32x4 c0 = ldsf4((lcp)c.lds + L_PART + 1024 + (16 * G + 4 * h) * 4), c1 = ldsf4((lcp)c.lds + L_PART + 1024 + (16 * G + 8 + 4 * h) * 4);
#pragma unroll
          for (int t = 1; t < 8; ++t) { c0 += ldsf4((lcp)c.lds + L_PART + 1024 + t * 128 + (16 * G + 4 * h) * 4); c1 += ldsf4((lcp)c.lds + L_PART + 1024 + t * 128 + (16 * G + 8 + 4 * h) * 4); }
          const lcp xp = (lcp)c.lds + (G == 0 ? 16 : 9) * GR + c.tm * 2048 + lane * 16;
          const f32x4 p0 = ldsf4(xp), p1 = ldsf4(xp + 1024);
          const float sc = ln_a * (0.25f * 1.4426950408889634f);
#pragma unroll
          for (int i = 0; i < 4; ++i) { y[i] = sc * __builtin_fmaf(-ln_m, c0[i], st.b0[8 * G + i] + p0[i]); y[4 + i] = sc * __builtin_fmaf(-ln_m, c1[i], st.b0[8 * G + 4 + i] + p1[i]); }
          qf = __builtin_bit_cast(bf16x8, pack8(y, 0)); PIN(qf);
          st.a0 = f32x16{}; st.b0 = f32x16{}; }
        if (EXIT_AT == 1) return;
        stage<G, 4, true, false>(st, [&](int) {}); SYNC(5);
        stage<G, 5, true, true>(st, [&](int f_) { if (f_ >= 1 && f_ < 4) issue_w1<8>(c, f_ - 1); else if (f_ >= 4 && f_ < 7) issue_w1<9>(c, f_ - 4); }); SYNCV(8);
        { const int ln = opaque(lane); c.vd = (ln >> 3) * 1024 + (((ln & 7) ^ (((ln >> 4) & 1) << 2)) * 16); }
        stage<G, 6, false, true>(st, [&](int f_) { if (f_ == 1) issue_d(c, 0);
            if (f_ == 8) { WAITV(7); if (lane == 0) store_flag(ws, WS_KVF + ((size_t)(c.cg * 64 + T) * 2 + G) * 8, tag); } }); SYNCV(5);
        stage<G, 7, false, true>(st, [&](int f_) { if (f_ >= 1 && f_ < 4) issue_w1<10>(c, f_ - 1); }); SYNCV(5);
        STAMP(3);
        u64 kf_;
        stage<G, 8, false, true>(st, [&](int f_) { if (f_ >= 1 && f_ < 4) issue_w1<11>(c, f_ - 1); if (f_ == 7) GATHER_ISSUE(gt2, false);
            if (f_ == 8) { const int tl = 4 * c.rb - 8 + w; aload_flag(kf_, P.ws, (unsigned)(WS_KVF + ((c.cg * 64 + (tl < 0 ? 0 : tl)) * 2 + (opaque(lane) & 1)) * 8)); } }); SYNCV(6);
        stage<G, 9, false, true>(st, [&](int f_) { if (f_ >= 1 && f_ < 4) issue_d(c, f_);
            if (f_ == 6) { WAITV(3); GATHER_CHECK(gt2, false); GATHER_RED(gt2); }
            if (f_ == 8) { asm volatile("" : "+v"(kf_)); const int tl = 4 * c.rb - 8 + w;
                if (tl >= 0) {
                    if (!__all(kf_ == tag)) poll_flags((const u64*)(ws + WS_KVF) + (c.cg * 64 + tl) * 2, 2, tag, lane, ws);
                    const char* base = ws + WS_KV + ((size_t)c.cg * 64 + tl) * 4096; const unsigned dst = c.lds0 + L_KV + w * 4096;
                    dma16_sc1(c.vlin, base, dst); dma16_sc1(c.vlin, base + 1024, dst + 1024); dma16_sc1(c.vlin, base + 2048, dst + 2048); dma16_sc1(c.vlin, base + 3072, dst + 3072); } } }); SYNCV(63);
        stage<G, 10, false, true>(st, [&](int f_) { if (f_ >= 1 && f_ < 4) issue_d(c, 3 + f_); }); SYNCV(63);
        STAMP(4);
        stage<G, 11, false, false>(st, [&](int f_) { if (f_ >= 1 && f_ < 3) issue_d(c, 6 + f_); });
        {
          lp xp = c.lds + 2 * GR + w * 4096 + c.vlin;
          SYNCV(63);
          *(LAS f32x4*)(xp) = F4(st.b0, 8 - 8 * G); *(LAS f32x4*)(xp + 1024) = F4(st.b0, 12 - 8 * G); *(LAS f32x4*)(xp + 2048) = F4(st.b1, 8 - 8 * G); *(LAS f32x4*)(xp + 3072) = F4(st.b1, 12 - 8 * G); }
        SYNC(5);
        STAMP(5);
        {
          const int lane = opaque(c.lane), h = lane >> 5;
          const float a2 = ln_a * ln_a;
          { u32x4 y0, y1;
#pragma unroll
            for (int q = 0; q < 4; ++q) {
                const f32x4 cg_ = ldsf4((lcp)c.lds + L_CS + (3 + 4 * G) * 128 + (8 * q + 4 * h) * 4), cu_ = ldsf4((lcp)c.lds + L_CS + (4 + 4 * G) * 128 + (8 * q + 4 * h) * 4);
                float y[4];
#pragma unroll
                for (int i = 0; i < 4; ++i) { const float g_ = fmaxf(__builtin_fmaf(-ln_m, cg_[i], st.a0[4 * q + i]), 0.f), u_ = __builtin_fmaf(-ln_m, cu_[i], st.a1[4 * q + i]); y[i] = (g_ * a2) * u_; }
                const unsigned lo = pk2(y[0], y[1]), hi = pk2(y[2], y[3]);
                if (q < 2) { y0[2 * q] = lo; y0[2 * q + 1] = hi; } else { y1[2 * q - 4] = lo; y1[2 * q - 3] = hi; }
            }
            if (G == 0 ? v0 : v2) { const unsigned o_ = (unsigned)(WS_C + ((size_t)T * NKS + 2 * (fs + 2 * G)) * 1024) + lane * 16;
                __builtin_amdgcn_raw_buffer_store_b128(y0, wsr, o_, 0, 16); __builtin_amdgcn_raw_buffer_store_b128(y1, wsr, o_ + 1024, 0, 16); } }
          { const lcp xp = (lcp)c.lds + 2 * GR + (w ^ 4) * 4096 + lane * 16;
            u32x4 y0;
#pragma unroll
            for (int q = 0; q < 2; ++q) {
                const f32x4 pg = ldsf4(xp + q * 1024), pu = ldsf4(xp + 2048 + q * 1024);
                const f32x4 cg_ = ldsf4((lcp)c.lds + L_CS + 5 * 128 + (16 * G + 8 * q + 4 * h) * 4), cu_ = ldsf4((lcp)c.lds + L_CS + 6 * 128 + (16 * G + 8 * q + 4 * h) * 4);
                float y[4];
#pragma unroll
                for (int i = 0; i < 4; ++i) { const float g_ = fmaxf(__builtin_fmaf(-ln_m, cg_[i], st.b0[8 * G + 4 * q + i] + pg[i]), 0.f), u_ = __builtin_fmaf(-ln_m, cu_[i], st.b1[8 * G + 4 * q + i] + pu[i]); y[i] = (g_ * a2) * u_; }
                y0[2 * q] = pk2(y[0], y[1]); y0[2 * q + 1] = pk2(y[2], y[3]);
            }
            __builtin_amdgcn_raw_buffer_store_b128(y0, wsr, (unsigned)(WS_C + ((size_t)T * NKS + 2 * (fs + 1) + G) * 1024) + lane * 16, 0, 16); } }
        SYNC(63);
        if constexpr (G == 1) __builtin_amdgcn_s_setprio(1);
    };
    if (c.g == 0) gemm1(std::integral_constant<int, 0>{}); else gemm1(std::integral_constant<int, 1>{});
    STAMP(6);
    if (EXIT_AT <= 2) goto done;
    {
    const int lane2 = opaque(lane);
    const unsigned cbase = (unsigned)(WS_C + ((size_t)T2 * NKS + kq) * 1024) + lane2 * 16;
    bf16x8 hf[22];
    u64 hfl;
#define LDH(i) do { const bool ok_ = ((i) < 21) || (kq < 2); hf[i] = __builtin_bit_cast(bf16x8, __builtin_amdgcn_raw_buffer_load_b128(wsr, ok_ ? cbase + (i) * 4096 : cbase, 0, 16)); } while (0)
    {
        const int lane = lane2, h = lane >> 5, r32 = lane & 31;
        const int i0 = T >= 8 ? 0 : 8 - T;
        const unsigned kb = c.lds0 + L_KV + c.tm * 4096 + c.g * 1024 + lane * 16;
        const unsigned lanebit = r32 < 16 ? 1u : 0u;
        const unsigned vb = c.lds0 + (r32 < 16 ? (unsigned)(((r32 & 15) + 16 * c.g + 32 * h) * 16) : (unsigned)L_ONES);
        auto koff = [&](int i) -> unsigned { return (unsigned)((i >= i0 ? i : 7) * 4096); };
        auto rdk = [&](int i) -> bf16x8 { return ldsf((lcp)(uintptr_t)(kb + koff(i))); };
        auto vaddr = [&](int i) -> unsigned { const bool valid = i >= i0; const unsigned d = valid ? 0u : (unsigned)(L_ZERO - L_ONES), A = valid ? (unsigned)(L_KV + 2048 + (c.tm + i) * 4096) : (unsigned)L_ZERO;
                                              return lanebit * (A - d) + (vb + d); };
        auto dmask = [&](int r) -> bool { u64 mk = 0;
#pragma unroll
            for (int l = 0; l < 64; ++l) { const int kl = (r & 3) + 8 * (r >> 2) + 4 * (l >> 5); if (kl <= (l & 31)) mk |= 1ull << l; }
            return __builtin_amdgcn_inverse_ballot_w64(mk); };
        const f32x16 zero = f32x16{};
        float m0 = -1e30f, m1 = -1e30f;
#define MAXT(S) do { _Pragma("unroll") for (int r = 0; r < 16; r += 4) { m0 = fmaxf(fmaxf(m0, S[r]), S[r + 1]); m1 = fmaxf(fmaxf(m1, S[r + 2]), S[r + 3]); } PIN(m0); PIN(m1); } while (0)
#define EXPT(S) do { _Pragma("unroll") for (int r = 0; r < 16; ++r) S[r] = __builtin_amdgcn_exp2f(S[r]); } while (0)
#define H_PUBLISH() do { STAMP(17); WAITV(4); STAMP(18); if (lane == 0) store_flag(ws, WS_HF + ((size_t)T * 32 + 2 * c.cg + c.g) * 8, tag); } while (0)
#define H_FLAGS() aload_flag(hfl, P.ws, (unsigned)(WS_HF + (T2 * 32 + (lane & 31)) * 8))
        f32x16 o = f32x16{};
        float m;
        if (T == 0) {
            f32x16 s = mfma(ldsf((lcp)(uintptr_t)(kb + 8 * 4096)), qf, zero);
#pragma unroll
            for (int r = 0; r < 16; ++r) s[r] = dmask(r) ? s[r] : -1e30f;
            MAXT(s);
            m = fmaxf(m0, m1);
            { auto rr = __builtin_amdgcn_permlane32_swap(__float_as_uint(m), __float_as_uint(m), false, false); m = fmaxf(__uint_as_float(rr[0]), __uint_as_float(rr[1])); }
#pragma unroll
            for (int r = 0; r < 16; ++r) s[r] = __builtin_amdgcn_exp2f(s[r] - m);
            const unsigned va = vaddr(8);
            o = mfma(ldsf((lcp)(uintptr_t)va), __builtin_bit_cast(bf16x8, pack8(s, 0)), o);
            o = mfma(ldsf((lcp)(uintptr_t)(va + 1024)), __builtin_bit_cast(bf16x8, pack8(s, 8)), o);
            issue_d(c, 9); issue_d(c, 10); issue_d(c, 11); issue_d(c, 12);
            H_PUBLISH(); H_FLAGS();
        } else {
            bf16x8 k0 = rdk(0), k8 = rdk(8), k1 = rdk(1), k2 = rdk(2), k3 = rdk(3);
            f32x16 sA, sB, sC;
            sA = mfma_pin0(k0, qf); sB = mfma_pin0(k8, qf); SB();
            k0 = rdk(4); k8 = rdk(5); sC = mfma_pin0(k1, qf); k1 = rdk(6); SB();
            asm volatile("s_nop 7" ::: );
#pragma unroll
            for (int r = 0; r < 16; ++r) sA[r] = dmask(r) ? sB[r] : sA[r];
            PIN(sA); SB();
            sB = mfma_pin0(k2, qf); k2 = rdk(7); SB();
            MAXT(sA); SB();
            sA = mfma_pin0(k3, qf); issue_d(c, 9); SB(); MAXT(sC); SB();
            sC = mfma_pin0(k0, qf); issue_d(c, 10); SB(); MAXT(sB); SB();
            sB = mfma_pin0(k8, qf); issue_d(c, 11); SB(); MAXT(sA); SB();
            sA = mfma_pin0(k1, qf); issue_d(c, 12); SB(); MAXT(sC); SB();
            sC = mfma_pin0(k2, qf); k0 = rdk(0); k8 = rdk(8); k1 = rdk(1); SB(); MAXT(sB); SB();
            MAXT(sA); SB(); MAXT(sC);
            m = fmaxf(m0, m1);
            { auto rr = __builtin_amdgcn_permlane32_swap(__float_as_uint(m), __float_as_uint(m), false, false); m = fmaxf(__uint_as_float(rr[0]), __uint_as_float(rr[1])); }
            STAMP(9);
            f32x16 cm;
#pragma unroll
            for (int r = 0; r < 16; ++r) cm[r] = -m;
            PIN(cm);
            asm volatile("s_nop 1" ::: ); sA = mfma_pin(k0, qf, cm); sB = mfma_pin(k8, qf, cm); sC = mfma_pin(k1, qf, cm); SB();
            bf16x8 va0, va1, vb0, vb1;
            { const unsigned a0 = vaddr(0), a8 = vaddr(8); va0 = ldsf((lcp)(uintptr_t)a0); va1 = ldsf((lcp)(uintptr_t)(a0 + 1024)); vb0 = ldsf((lcp)(uintptr_t)a8); vb1 = ldsf((lcp)(uintptr_t)(a8 + 1024)); }
            k0 = rdk(2);
            unsigned mp[8];
#pragma unroll
            for (int j = 0; j < 8; ++j) mp[j] = (dmask(2 * j) ? 0xffffu : 0u) | (dmask(2 * j + 1) ? 0xffff0000u : 0u);
            SB();
#pragma unroll
            for (int r = 0; r < 16; ++r) sA[r] = dmask(r) ? sB[r] : sA[r];
            EXPT(sA);
            { const u32x4 p0 = pack8(sA, 0), p1 = pack8(sA, 8); u32x4 d0, d1, o0, o1;
#pragma unroll
              for (int j = 0; j < 4; ++j) { d0[j] = p0[j] & mp[j]; d1[j] = p1[j] & mp[4 + j]; o0[j] = p0[j] & ~mp[j]; o1[j] = p1[j] & ~mp[4 + j]; }
              o = mfma(va0, __builtin_bit_cast(bf16x8, o0), o); o = mfma(va1, __builtin_bit_cast(bf16x8, o1), o);
              o = mfma(vb0, __builtin_bit_cast(bf16x8, d0), o); o = mfma(vb1, __builtin_bit_cast(bf16x8, d1), o); }
            { const unsigned a1 = vaddr(1), a2 = vaddr(2); va0 = ldsf((lcp)(uintptr_t)a1); va1 = ldsf((lcp)(uintptr_t)(a1 + 1024)); vb0 = ldsf((lcp)(uintptr_t)a2); vb1 = ldsf((lcp)(uintptr_t)(a2 + 1024)); }
            SB();
#define EXP8(S, b) do { _Pragma("unroll") for (int r = (b); r < (b) + 8; ++r) S[r] = __builtin_amdgcn_exp2f(S[r]); } while (0)
#define PK4(S, b) (u32x4){pk2(S[b], S[(b) + 1]), pk2(S[(b) + 2], S[(b) + 3]), pk2(S[(b) + 4], S[(b) + 5]), pk2(S[(b) + 6], S[(b) + 7])}
#define TILE2(i, S, V0, V1, SN) do { \
                if ((i) + 1 <= 7) { SN = mfma_pin(k0, qf, cm); SB(); if ((i) + 2 <= 7) k0 = rdk((i) + 2); } \
                EXP8(S, 0); { u32x4 p_ = PK4(S, 0); PIN(p_); SB(); o = mfma(V0, __builtin_bit_cast(bf16x8, p_), o); SB(); } \
                EXP8(S, 8); { u32x4 p_ = PK4(S, 8); PIN(p_); SB(); o = mfma(V1, __builtin_bit_cast(bf16x8, p_), o); SB(); } \
                if ((i) + 2 <= 7) { const unsigned a_ = vaddr((i) + 2); V0 = ldsf((lcp)(uintptr_t)a_); V1 = ldsf((lcp)(uintptr_t)(a_ + 1024)); } SB(); } while (0)
            H_PUBLISH();
            TILE2(1, sC, va0, va1, sB); TILE2(2, sB, vb0, vb1, sC); TILE2(3, sC, va0, va1, sB); TILE2(4, sB, vb0, vb1, sC);
            TILE2(5, sC, va0, va1, sB);
            H_FLAGS();
            TILE2(6, sB, vb0, vb1, sC); TILE2(7, sC, va0, va1, sB);
        }
        const float inv = __builtin_amdgcn_rcpf(o[8]);
        f32x16 y;
#pragma unroll
        for (int r = 0; r < 8; ++r) y[r] = o[r] * inv;
        const unsigned oo = (unsigned)(WS_C + ((size_t)T * NKS + 86 + 2 * c.cg + c.g) * 1024) + lane * 16;
        const u32x4 ov = pack8(y, 0);
        STAMP(28);
        if (EXIT_AT == 3) { __builtin_amdgcn_raw_buffer_store_b128(ov, wsr, oo, 0, 16); goto done; }
        __builtin_amdgcn_raw_buffer_store_b128(ov, wsr, oo, 0, 16);
        WAITV(1); asm volatile("" : "+v"(hfl));
        STAMP(15);
        if (!__all(hfl == tag)) poll_flags((const u64*)(ws + WS_HF) + T2 * 32, 32, tag, lane, ws);
        STAMP(16);
#pragma unroll
        for (int i = 0; i < 22; ++i) LDH(i);
        __builtin_amdgcn_s_setprio(0);
    }
    STAMP(10);
    {
        const int lane = opaque(lane2), h = lane >> 5, r32 = lane & 31, q4 = (lane & 15) >> 2, p4 = lane & 3, b16 = (lane >> 4) & 1;
        const int tok = 64 * c.tb + 32 * th + r32;
        f32x16 acc0 = f32x16{}, acc1 = f32x16{};
        const unsigned wl = c.lds0 + kq * 2048 + (4 * h + q4) * 128 + 32 * b16 + 8 * p4, swz = ((q4 >> 1) & 1) << 6;
        const unsigned wl0 = wl + (0u ^ swz), wl1 = wl + (64u ^ swz);
        bf16x8 wa[3], wb_[3];
#define RDW(u) do { wa[(u) % 3] = trfrag<1024>((lcp)(uintptr_t)(wl0 + dloc(u))); wb_[(u) % 3] = trfrag<1024>((lcp)(uintptr_t)(wl1 + dloc(u))); } while (0)
#define G2STEP(u, frag, nxt) do { acc0 = mfma(wa[(u) % 3], frag, acc0); acc1 = mfma(wb_[(u) % 3], frag, acc1); SB(); if ((nxt) >= 0) RDW((nxt) < 0 ? 0 : (nxt)); SB(); } while (0)
        SYNC(63);
        STAMP(11);
        if (EXIT_AT == 4) goto done;
        const bool ew = blockIdx.x == 0 && w == 7; u32x4 sf0 = {0u, 0u, 0u, 0u}, sf1 = sf0; bool edone = false;
        if (ew) { aload16_sc1(sf0, P.ws + WS_SF, (unsigned)(lane * 32)); aload16_sc1(sf1, P.ws + WS_SF + 16, (unsigned)(lane * 32)); }
        RDW(0); RDW(1); RDW(2);
        G2STEP(0, hf[0], 3);
        if (lane == 0) store_flag(ws, WS_OF + ((size_t)T * 32 + 2 * c.cg + c.g) * 8, tag);
        issue_d(c, 13); G2STEP(1, hf[1], 4); issue_d(c, 14); G2STEP(2, hf[2], 5); issue_d(c, 15); G2STEP(3, hf[3], 6); issue_d(c, 16); G2STEP(4, hf[4], 7); issue_d(c, 17); G2STEP(5, hf[5], 8); issue_d(c, 18);
        SYNCV(63);
        G2STEP(6, hf[6], 9); issue_d(c, 19); G2STEP(7, hf[7], 10); issue_d(c, 20); G2STEP(8, hf[8], 11); issue_d(c, 21); G2STEP(9, hf[9], 12); issue_d(c, 22);
        G2STEP(10, hf[10], -1); issue_d(c, 23); G2STEP(11, hf[11], -1); issue_d(c, 24); G2STEP(12, hf[12], -1);
        STAMP(24);
        SYNCV(6);
        STAMP(25);
        if (ew) { asm volatile("" : "+v"(sf0), "+v"(sf1));
            edone = __all(sf0[0] == tagLo && sf0[1] == tagHi && sf0[2] == tagLo && sf0[3] == tagHi && sf1[0] == tagLo && sf1[1] == tagHi && sf1[2] == tagLo && sf1[3] == tagHi);
            if (edone && lane == 0) store_flag(ws, WS_EPOCH, tag); }
        edone_ = edone;
        RDW(13); RDW(14); RDW(15);
        uint2 xa, xb2;
        const int nt = (w >> 1) & 1, rh = w >> 2;
        const int n0 = 64 * c.nb + 32 * nt + 16 * rh + 4 * h;
        u64 ofl; aload_flag(ofl, P.ws, (unsigned)(WS_OF + (T2 * 32 + (lane & 31)) * 8));
        issue_d(c, 25); G2STEP(13, hf[13], 16); issue_d(c, 26); G2STEP(14, hf[14], 17); issue_d(c, 27); G2STEP(15, hf[15], 18); issue_d(c, 28); G2STEP(16, hf[16], -1); issue_d(c, 29);
        WAITV(5); asm volatile("" : "+v"(ofl));
        if (!__all(ofl == tag)) poll_flags((const u64*)(ws + WS_OF) + T2 * 32, 32, tag, lane, ws);
        STAMP(26);
        bf16x8 of[8];
#pragma unroll
        for (int i = 0; i < 8; ++i) of[i] = __builtin_bit_cast(bf16x8, __builtin_amdgcn_raw_buffer_load_b128(wsr, cbase + (86 + 4 * i) * 1024, 0, 16));
        { const u16* xr = P.x + (size_t)tok * 512 + n0; xa = *(const uint2*)xr; xb2 = *(const uint2*)(xr + 8); }
        G2STEP(17, hf[17], -1); G2STEP(18, hf[18], -1);
        STAMP(12);
        SYNCV(63);
        RDW(19); RDW(20); RDW(21);
        G2STEP(19, hf[19], 22); G2STEP(20, hf[20], 23);
        if (kq < 2) { acc0 = mfma(wa[21 % 3], hf[21], acc0); acc1 = mfma(wb_[21 % 3], hf[21], acc1); }
        SB(); RDW(24); SB();
        STAMP(13);
        G2STEP(22, of[0], -1); G2STEP(23, of[1], -1); G2STEP(24, of[2], -1);
        SYNCV(63);
        RDW(25); RDW(26); RDW(27);
        G2STEP(25, of[3], 28); G2STEP(26, of[4], 29); G2STEP(27, of[5], -1); G2STEP(28, of[6], -1); G2STEP(29, of[7], -1);
        STAMP(27);
        { lp rbp = c.lds + 4 * GR + (kq * 2 + th) * 8192 + lane * 16;
          SYNCV(63);
#pragma unroll
          for (int q = 0; q < 4; ++q) { *(LAS f32x4*)(rbp + q * 1024) = F4(acc0, 4 * q); *(LAS f32x4*)(rbp + 4096 + q * 1024) = F4(acc1, 4 * q); } }
        SYNC(63);
        { f32x4 s0 = {0.f, 0.f, 0.f, 0.f}, s1 = s0;
#pragma unroll
          for (int k = 0; k < 4; ++k) { const lcp bp = (lcp)c.lds + 4 * GR + (k * 2 + th) * 8192 + nt * 4096 + 2 * rh * 1024 + lane * 16; s0 += ldsf4(bp); s1 += ldsf4(bp + 1024); }
          uint2 oa, ob;
          oa.x = pk2(s0[0] + blo(xa.x), s0[1] + bhi(xa.x)); oa.y = pk2(s0[2] + blo(xa.y), s0[3] + bhi(xa.y));
          ob.x = pk2(s1[0] + blo(xb2.x), s1[1] + bhi(xb2.x)); ob.y = pk2(s1[2] + blo(xb2.y), s1[3] + bhi(xb2.y));
          u16* orow = P.out + (size_t)tok * 512 + n0;
          *(uint2*)orow = oa; *(uint2*)(orow + 8) = ob; }
    }
    STAMP(14);
    }
done:
    if (blockIdx.x == 0 && w == 7 && !edone_) {
        const GAS u64* pp = (const GAS u64*)(ws + WS_SF) + lane * 4;
        for (unsigned spins = 0;; ++spins) { bool ok = true;
#pragma unroll
            for (int j = 0; j < 4; ++j) ok &= __hip_atomic_load(pp + j, __ATOMIC_RELAXED, __HIP_MEMORY_SCOPE_AGENT) == tag;
            if (__all(ok)) break; if (spins > SPIN_CAP) { set_timeout(ws, lane); break; } __builtin_amdgcn_s_sleep(2); }
        if (lane == 0) store_flag(ws, WS_EPOCH, tag);
    }
}

extern "C" void kernel_launch(void* const* d_in, const int* in_sizes, int n_in, void* d_out, int out_size, void* d_ws, size_t ws_size, hipStream_t stream) {
    Params p;
    p.x = (const u16*)d_in[0]; p.wqkv = (const u16*)d_in[1]; p.wo = (const u16*)d_in[2];
    p.wg = (const u16*)d_in[4]; p.wu = (const u16*)d_in[5]; p.wd = (const u16*)d_in[6];
    p.out = (u16*)d_out; p.ws = (char*)d_ws;
    (void)hipFuncSetAttribute((const void*)fused_kernel, hipFuncAttributeMaxDynamicSharedMemorySize, LDS_BYTES);
    fused_kernel<<<256, 512, LDS_BYTES, stream>>>(p);
}
```

```cpp
#include <hip/hip_runtime.h>
#include <stdint.h>
#include <type_traits>

#define FI __device__ __forceinline__
#define LAS __attribute__((address_space(3)))
#define GAS __attribute__((address_space(1)))
typedef unsigned short u16;
typedef unsigned long long u64;
typedef short bf16x8 __attribute__((ext_vector_type(8)));
typedef short s16x4 __attribute__((ext_vector_type(4)));
typedef float f32x16 __attribute__((ext_vector_type(16)));
typedef float f32x4 __attribute__((ext_vector_type(4)));
typedef float f32x2 __attribute__((ext_vector_type(2)));
typedef unsigned u32x4 __attribute__((ext_vector_type(4)));
typedef __bf16 bf16x2_t __attribute__((ext_vector_type(2)));
typedef LAS const char* lcp;
typedef LAS char* lp;

constexpr int GR = 8192;
constexpr int L_KV = 13 * GR;
constexpr int L_KVR = 17 * GR;
constexpr int L_MISC = 19 * GR;
constexpr int L_CS = L_MISC;
constexpr int L_CSW = L_MISC + 2048;
constexpr int L_ZERO = L_MISC + 4096;
constexpr int L_PART = 15 * GR;
constexpr int L_ONES = L_MISC + 6144;
constexpr int L_CNT = L_ONES + 64;
constexpr int L_FINE = L_MISC + 6400;
constexpr int LDS_BYTES = 163840;

constexpr size_t WS_EPOCH = 0, WS_TMO = 64;
constexpr size_t WS_SF = 4096;
constexpr size_t WS_KVF = 8192;
constexpr size_t WS_HF = 24576;
constexpr size_t WS_OF = 40960;
constexpr size_t WS_ST = 65536;
constexpr size_t WS_CS = 131072;
constexpr size_t WS_SCR = 768u << 10;
constexpr size_t WS_KV = 2u << 20;
constexpr size_t WS_C = 8u << 20;
constexpr size_t WS_DBG = 32u << 20;
constexpr int NKS = 118;
constexpr unsigned CODE_PF_BYTES = 43008;

FI f32x16 mfma(bf16x8 a, bf16x8 b, f32x16 c) { return __builtin_amdgcn_mfma_f32_32x32x16_bf16(a, b, c, 0, 0, 0); }
FI f32x16 mfma_pin(bf16x8 a, bf16x8 b, const f32x16& c) { f32x16 d; asm volatile("v_mfma_f32_32x32x16_bf16 %0, %1, %2, %3" : "=&v"(d) : "v"(a), "v"(b), "v"(c)); return d; }
FI f32x16 mfma_pin0(bf16x8 a, bf16x8 b) { f32x16 d; asm volatile("v_mfma_f32_32x32x16_bf16 %0, %1, %2, 0" : "=&v"(d) : "v"(a), "v"(b)); return d; }
FI unsigned pk2(float lo, float hi) { f32x2 v = {lo, hi}; bf16x2_t r = __builtin_convertvector(v, bf16x2_t); return __builtin_bit_cast(unsigned, r); }
FI u32x4 pack8(const f32x16& y, int b) { u32x4 r; r[0] = pk2(y[b], y[b + 1]); r[1] = pk2(y[b + 2], y[b + 3]); r[2] = pk2(y[b + 4], y[b + 5]); r[3] = pk2(y[b + 6], y[b + 7]); return r; }
typedef short v4i16_t __attribute__((ext_vector_type(4)));
FI s16x4 trd(lcp p) { return __builtin_bit_cast(s16x4, __builtin_amdgcn_ds_read_tr16_b64_v4i16((LAS v4i16_t*)p)); }
template <int D2> FI bf16x8 trfrag(lcp p) { s16x4 lo = trd(p), hi = trd(p + D2); return __builtin_shufflevector(lo, hi, 0, 1, 2, 3, 4, 5, 6, 7); }
FI bf16x8 ldsf(lcp p) { return *(const LAS bf16x8*)p; }
FI f32x4 ldsf4(lcp p) { return *(const LAS f32x4*)p; }

FI void dma16(unsigned voff, const void* sbase, unsigned dst) {
    asm volatile("s_mov_b32 m0, %2\n\ts_nop 0\n\tglobal_load_lds_dwordx4 %0, %1" :: "v"(voff), "s"(sbase), "s"(__builtin_amdgcn_readfirstlane(dst)) : "memory", "m0");
}
FI void dma16_sc1(unsigned voff, const void* sbase, unsigned dst) {
    asm volatile("s_mov_b32 m0, %2\n\ts_nop 0\n\tglobal_load_lds_dwordx4 %0, %1 sc1" :: "v"(voff), "s"(sbase), "s"(__builtin_amdgcn_readfirstlane(dst)) : "memory", "m0");
}
FI void aload16(u32x4& v, const void* base, unsigned voff) { asm volatile("global_load_dwordx4 %0, %1, %2" : "=v"(v) : "v"(voff), "s"(base) : "memory"); }
FI void aload16_sc1(u32x4& v, const void* base, unsigned voff) { asm volatile("global_load_dwordx4 %0, %1, %2 sc1" : "=v"(v) : "v"(voff), "s"(base) : "memory"); }
FI void aload_flag(u64& v, const char* base, unsigned voff) { asm volatile("global_load_dwordx2 %0, %1, %2 sc1" : "=v"(v) : "v"(voff), "s"(base) : "memory"); }

FI void aprefetch(unsigned& d, const char* p) { asm volatile("global_load_dword %0, %1, off" : "+v"(d) : "v"(p) : "memory"); }
#ifndef EXIT_AT
#define EXIT_AT 99
#endif
#define SB() __builtin_amdgcn_sched_barrier(0)
#define PIN(x) asm volatile("" : "+v"(x))
FI int opaque(int v) { asm volatile("" : "+v"(v)); return v; }
#define FST() do {} while (0)
#define SYNC(N) do { SB(); FST(); asm volatile("s_waitcnt vmcnt(" #N ") lgkmcnt(0)\n\ts_barrier" ::: "memory"); FST(); SB(); } while (0)
#define SYNCV(N) do { SB(); FST(); asm volatile("s_waitcnt vmcnt(" #N ")\n\ts_barrier" ::: "memory"); FST(); SB(); } while (0)
#define WAITV(N) do { SB(); asm volatile("s_waitcnt vmcnt(" #N ")" ::: "memory"); SB(); } while (0)

#define STAMP(k) do {} while (0)

struct Params {
    const u16* x; const u16* wqkv; const u16* wo; const u16* wg; const u16* wu; const u16* wd; u16* out; char* ws;
};

constexpr int wslot(int s) { return (s == 0 ? 4 : s == 1 ? 14 : s == 2 ? 7 : s == 3 ? 0 : s == 4 ? 7 : s == 5 ? 0 : s == 6 ? 10 : s == 7 ? 3 : s == 8 ? 6 : s == 9 ? 9 : s == 10 ? 0 : 3) * GR; }
constexpr int xslot(int c) { return (c == 0 ? 0 : c == 1 ? 2 : c == 2 ? 10 : c == 3 ? 12 : c == 4 ? 3 : c == 5 ? 5 : c == 6 ? 13 : 15) * GR; }
FI constexpr int dloc0(int u) { return (u == 0 ? 12 : u < 4 ? 6 + (u - 1) : u < 7 ? 9 + (u - 4) : u < 9 ? u - 7 : 2 + (u - 9)) * GR; }
FI constexpr int dloc(int u) { return u < 13 ? dloc0(u) : u < 19 ? L_KV + (u - 13) * GR : dloc0(u - 19); }

struct Ctx {
    const Params* p;
    unsigned lds0;
    lp lds;
    int lane, w, tm, g, rb, cg, tb, nb;
    unsigned vq, vf0, vf1, vf2, vx, vlin, vd;
    const char *px, *pq, *pf, *pwd, *pwo;
};

FI void issue_x1(const Ctx& c, int chunk, int k) {
    dma16(k ? c.vx ^ 64u : c.vx, c.px + chunk * 128 + k * 8 * 1024, c.lds0 + xslot(chunk) + 16 * c.w * 128 + k * 8 * 128);
}
FI void issue_x(const Ctx& c, int chunk) { issue_x1(c, chunk, 0); issue_x1(c, chunk, 1); }
template <int S> FI void issue_w1(const Ctx& c, int k) {
    if constexpr (S >= 4) {
        const char* base = c.pf + (size_t)(64 * (S - 4)) * 2752;
        dma16(k == 0 ? c.vf0 : k == 1 ? c.vf1 : c.vf2, base, c.lds0 + wslot(S) + c.g * 12288 + c.tm * 3072 + k * 1024);
    } else {
        const char* base = c.pq + (size_t)(128 * S) * 3072;
        dma16(c.vq, base + (k == 0 ? 1024 : k == 1 ? 2048 : 0), c.lds0 + wslot(S) + c.w * 1024 + k * 8192);
    }
}
template <int S> FI void issue_w(const Ctx& c) { issue_w1<S>(c, 0); issue_w1<S>(c, 1); issue_w1<S>(c, 2); }
FI void issue_d(const Ctx& c, int u) {
    const char* base;
    if (u < 21) base = c.pwd + (size_t)u * 65536;
    else if (u == 21) base = c.pwd + (size_t)21 * 65536 - (c.w >= 4 ? 32768 : 0);
    else base = c.pwo + (size_t)(u - 22) * 65536;
    dma16(c.vd, base, c.lds0 + dloc(u) + c.w * 1024);
}

FI float blo(unsigned d) { return __uint_as_float(d << 16); }
FI float bhi(unsigned d) { return __uint_as_float(d & 0xffff0000u); }
template <int CTRL> FI float dppadd(float x) { return x + __builtin_bit_cast(float, __builtin_amdgcn_update_dpp(0, __builtin_bit_cast(int, x), CTRL, 0xf, 0xf, true)); }
FI void swap32add(float& u, float v) { auto rr = __builtin_amdgcn_permlane32_swap(__float_as_uint(u), __float_as_uint(v), false, false); u = __uint_as_float(rr[0]) + __uint_as_float(rr[1]); }
FI void swap16add(float& u, float v) { auto rr = __builtin_amdgcn_permlane16_swap(__float_as_uint(u), __float_as_uint(v), false, false); u = __uint_as_float(rr[0]) + __uint_as_float(rr[1]); }

FI f32x4 m4(s16x4 a, s16x4 b, f32x4 c) { return __builtin_amdgcn_mfma_f32_4x4x4bf16_1k(a, b, c, 0, 0, 0); }
FI s16x4 lo4(const bf16x8& f) { return __builtin_shufflevector(f, f, 0, 1, 2, 3); }
FI s16x4 hi4(const bf16x8& f) { return __builtin_shufflevector(f, f, 4, 5, 6, 7); }
constexpr s16x4 ONES4 = {0x3f80, 0x3f80, 0x3f80, 0x3f80};
FI void stat4(const bf16x8& f, f32x4& s, f32x4& q) { s = m4(ONES4, lo4(f), s); q = m4(lo4(f), lo4(f), q); s = m4(ONES4, hi4(f), s); q = m4(hi4(f), hi4(f), q); }
FI void sum4(const bf16x8& f, f32x4& s) { s = m4(ONES4, lo4(f), s); s = m4(ONES4, hi4(f), s); }
FI float halfsum(float v) { auto rr = __builtin_amdgcn_permlane32_swap(__float_as_uint(v), __float_as_uint(v), false, false); return __uint_as_float(rr[0]) + __uint_as_float(rr[1]); }
struct G1State {
    bf16x8 xf[32];
    bf16x8 wq[4];
    f32x16 a0, a1, b0, b1;
    f32x16 qa, qb;
    unsigned wbf[4], wbq[4];
    unsigned xb[4];
};
constexpr int fkind(int G, int f) { return G == 0 ? (f < 8 ? (f & 3) : (f & 1)) : (f < 4 ? (f & 1) : f < 6 ? (f & 1) : f < 8 ? 2 + (f & 1) : f < 10 ? (f & 1) : 2 + (f & 1)); }
constexpr int fpos(int G, int f) { return G == 0 ? (f < 8 ? (f >> 2) : 2 + ((f - 8) >> 1)) : (f < 4 ? (f >> 1) : f < 8 ? 2 : 3); }
constexpr int qkind(int G, int f) { return G == 0 ? ((f == 1 || f == 3 || f == 7 || f == 9) ? 1 : 0) : ((f == 3 || f == 5 || f == 9 || f == 11) ? 1 : 0); }
constexpr int qpos(int G, int f) {
    return G == 0 ? (f == 0 ? 0 : f == 1 ? 0 : f == 2 ? 1 : f == 3 ? 1 : f == 4 ? 2 : f == 5 ? 3 : f == 6 ? 4 : f == 7 ? 4 : f == 8 ? 5 : f == 9 ? 5 : f == 10 ? 6 : 7)
                  : (f == 0 ? 0 : f == 1 ? 1 : f == 2 ? 2 : f == 3 ? 2 : f == 4 ? 3 : f == 5 ? 3 : f == 6 ? 4 : f == 7 ? 5 : f == 8 ? 6 : f == 9 ? 6 : f == 10 ? 7 : 7);
}
template <int G, int S> FI bf16x8 rdfrag(const G1State& st, int f) {
    if constexpr (S >= 4) { const int k = fkind(G, f); return trfrag<768>((lcp)(uintptr_t)(st.wbf[fpos(G, f)] + (k & 1) * 12288 + (k >= 2 ? 64 : (G == 0 ? 0 : 128)))); }
    else { const int e = qpos(G, f); return trfrag<256>((lcp)(uintptr_t)(st.wbq[e & 3] + (e >> 2) * 4096 + (qkind(G, f) ? 16384 : G * 8192))); }
}
template <int C> FI void rdx(G1State& st) {
#pragma unroll
    for (int e = 0; e < 4; ++e) { st.xf[4 * C + e] = ldsf((lcp)(uintptr_t)st.xb[e]); if (C < 7) st.xb[e] = (unsigned)opaque((int)(st.xb[e] + (xslot(C + 1) - xslot(C)))); }
}
template <int G, int S, bool FRESH, bool XF, int XP, int NX, class F>
FI void stage(G1State& st, F&& hook) {
    if constexpr (XF) rdx<(XF ? 2 * S : 0)>(st);
    if constexpr (FRESH) {
#pragma unroll
        for (int f = 0; f < 4; ++f) st.wq[f] = rdfrag<G, S>(st, f);
    }
    if constexpr (XF) rdx<(XF ? 2 * S + 1 : 0)>(st);
    SB();
#pragma unroll
    for (int f = 0; f < 12; ++f) {
        const bf16x8 w = st.wq[f & 3];
        if constexpr (S >= 4) {
            const int k = fkind(G, f); const bf16x8 x = st.xf[4 * (S - 4) + fpos(G, f)];
            if (k == 0) st.a0 = mfma(w, x, st.a0); else if (k == 1) st.a1 = mfma(w, x, st.a1); else if (k == 2) st.b0 = mfma(w, x, st.b0); else st.b1 = mfma(w, x, st.b1);
        } else {
            const bf16x8 x = st.xf[8 * S + qpos(G, f)];
            if (qkind(G, f)) st.qb = mfma(w, x, st.qb); else if (G == 0) st.qa = mfma(w, x, st.qa); else st.qa = mfma(x, w, st.qa);
        }
        SB();
        if (f < 8) st.wq[f & 3] = rdfrag<G, S>(st, f + 4);
        else {
            if (f == 8) {
                if constexpr (S < 3) { _Pragma("unroll") for (int j = 0; j < 4; ++j) st.wbq[j] = (unsigned)opaque((int)(st.wbq[j] + (wslot(S + 1) - wslot(S)))); }
                if constexpr (S >= 4 && S < 11) { _Pragma("unroll") for (int j = 0; j < 4; ++j) st.wbf[j] = (unsigned)opaque((int)(st.wbf[j] + (wslot(S + 1) - wslot(S)))); }
            }
            if constexpr (NX >= 0) st.wq[f & 3] = rdfrag<G, (NX >= 0 ? NX : 0)>(st, f - 8);
        }
        if constexpr (XP >= 0) { if (f == 2) rdx<(XP >= 0 ? XP : 0)>(st); if (f == 6) rdx<(XP >= 0 ? XP + 1 : 0)>(st); }
        hook(f);
        SB();
    }
}

FI u64 mix64(u64 z) { z += 0x9E3779B97F4A7C15ull; z = (z ^ (z >> 30)) * 0xBF58476D1CE4E5B9ull; z = (z ^ (z >> 27)) * 0x94D049BB133111EBull; return z ^ (z >> 31); }

constexpr unsigned SPIN_CAP = 1u << 18;
FI void set_timeout(char* ws, int lane) { if (lane == 0) __hip_atomic_store((GAS unsigned*)(ws + WS_TMO), 1u, __ATOMIC_RELAXED, __HIP_MEMORY_SCOPE_AGENT); }
FI void poll_flags(const u64* flags, int n, u64 tag, int lane, char* ws) {
    const GAS u64* pp = (const GAS u64*)flags + (lane < n ? lane : 0);
    for (unsigned spins = 0;; ++spins) {
        const u64 v = __hip_atomic_load(pp, __ATOMIC_RELAXED, __HIP_MEMORY_SCOPE_AGENT);
        if (__all(v == tag)) return;
        if (spins > SPIN_CAP) { set_timeout(ws, lane); return; }
        __builtin_amdgcn_s_sleep(2);
    }
}
FI void store_flag(char* ws, size_t off, u64 tag) { __hip_atomic_store((GAS u64*)(ws + off), tag, __ATOMIC_RELAXED, __HIP_MEMORY_SCOPE_AGENT); }

__global__ __launch_bounds__(512, 2) void fused_kernel(Params P) {
    extern __shared__ __attribute__((aligned(16))) char smem[];
    Ctx c; c.p = &P;
    const int tid = threadIdx.x; c.lane = tid & 63;
    unsigned pfd = 0;
    { const char* pc = (const char*)__builtin_amdgcn_s_getpc(); unsigned off = ((unsigned)(blockIdx.x >> 3) * 16u + (tid & 15)) * 128u; if (off > CODE_PF_BYTES) off = CODE_PF_BYTES; if (tid < 16) aprefetch(pfd, pc + off); }
    const int w = __builtin_amdgcn_readfirstlane(tid >> 6);
    c.w = w; c.tm = w & 3; c.g = w >> 2;
    c.lds = (lp)smem; c.lds0 = (unsigned)(uintptr_t)c.lds;
    const int xcd = blockIdx.x & 7, idx = blockIdx.x >> 3;
    c.rb = 4 * (xcd >> 1) + (idx >> 3); c.cg = 8 * (xcd & 1) + (idx & 7);
    c.tb = 4 * xcd + (idx >> 3); c.nb = idx & 7;
    const int fs = c.cg < 11 ? 3 * c.cg : c.cg < 15 ? 33 + 2 * (c.cg - 11) : 40;
    const bool v0 = c.cg != 15, v2 = c.cg < 11 || c.cg == 15;
    const int lane = c.lane, h = lane >> 5, r32 = lane & 31;
    char* ws = P.ws;
    STAMP(0);
    c.px = (const char*)P.x + (size_t)(128 * c.rb + 16 * w) * 1024;
    c.pf = (const char*)(c.g ? P.wu : P.wg) + (size_t)(16 * c.tm) * 2752 + fs * 64;
    u64 tag = 0; unsigned tagLo = 0, tagHi = 0; float ln_a = 0.f, ln_m = 0.f;
    const int T = 4 * c.rb + c.tm;
    const __amdgpu_buffer_rsrc_t wsr = __builtin_amdgcn_make_buffer_rsrc(ws, 0, 32u << 20, 0x00020000);
    c.pq = (const char*)P.wqkv + ((size_t)(16 * w) * 1536 + 32 * c.cg) * 2;
    c.pwd = (const char*)P.wd + (size_t)(8 * w) * 1024 + c.nb * 128;
    c.pwo = (const char*)P.wo + (size_t)(8 * w) * 1024 + c.nb * 128;
    u32x4 g0a, g0b;
#define GATHER_OFFS(tile) const int gl_ = opaque(lane); const unsigned go0 = (unsigned)(WS_CS + (c.cg * 9 + (tile)) * 2048 + (gl_ >> 3) * 256 + (gl_ & 7) * 32)
#define GATHER_ISSUE(tile, ST) do { GATHER_OFFS(tile); aload16_sc1(g0a, P.ws, go0); aload16_sc1(g0b, P.ws + 16, go0); } while (0)
#define GATHER_OK(ST) (g0a[1] == tagLo && g0a[3] == tagLo && g0b[1] == tagLo && g0b[3] == tagLo)
#define GATHER_RED(tile) do { const int gl_ = opaque(lane); float v0_ = __uint_as_float(g0a[0]), v1_ = __uint_as_float(g0a[2]), v2_ = __uint_as_float(g0b[0]), v3_ = __uint_as_float(g0b[2]); \
        swap32add(v0_, v2_); swap32add(v1_, v3_); swap16add(v0_, v1_); v0_ = dppadd<0x128>(v0_); \
        if ((gl_ & 8) == 0) *(LAS float*)(c.lds + L_CS + (tile) * 128 + (4 * (gl_ & 7) + 2 * (gl_ >> 5) + ((gl_ >> 4) & 1)) * 4) = v0_; } while (0)
#define GATHER_CHECK(tile, ST) do { asm volatile("" : "+v"(g0a), "+v"(g0b)); \
        if (!__all(GATHER_OK(ST))) { GATHER_OFFS(tile); \
            for (unsigned spins = 0;; ++spins) { __builtin_amdgcn_s_sleep(2); \
                g0a = __builtin_amdgcn_raw_buffer_load_b128(wsr, go0, 0, 16); g0b = __builtin_amdgcn_raw_buffer_load_b128(wsr, go0 + 16, 0, 16); __builtin_amdgcn_s_waitcnt(0x0F70); \
                if (__all(GATHER_OK(ST))) break; if (spins > SPIN_CAP) { set_timeout(ws, lane); break; } } } \
        } while (0)
#define RD_CS(dst, tile) do { _Pragma("unroll") for (int q_ = 0; q_ < 4; ++q_) { const f32x4 t_ = ldsf4((lcp)c.lds + L_CS + (tile) * 128 + (8 * q_ + 4 * h) * 4); \
            dst[4 * q_] = t_[0]; dst[4 * q_ + 1] = t_[1]; dst[4 * q_ + 2] = t_[2]; dst[4 * q_ + 3] = t_[3]; } } while (0)
#define SPREAD(dst, v, row) do { if (h == 0) *(LAS float*)(c.lds + L_CSW + c.tm * 256 + (row) * 128 + r32 * 4) = (v); \
        _Pragma("unroll") for (int q_ = 0; q_ < 4; ++q_) { const f32x4 t_ = ldsf4((lcp)c.lds + L_CSW + c.tm * 256 + (row) * 128 + (8 * q_ + 4 * h) * 4); \
            dst[4 * q_] = t_[0]; dst[4 * q_ + 1] = t_[1]; dst[4 * q_ + 2] = t_[2]; dst[4 * q_ + 3] = t_[3]; } } while (0)
#define F4(v, b) (f32x4){v[b], v[(b) + 1], v[(b) + 2], v[(b) + 3]}

    const int th = w & 1, kq = w >> 1;
    const int T2 = 2 * c.tb + th;
    bf16x8 qf; bool edone_ = false;
    auto gemm1 = [&](auto Gc) {
        constexpr int G = decltype(Gc)::value;
        const int lane = opaque(c.lane), h = lane >> 5, r32 = lane & 31, tid = w * 64 + lane;
        c.vq = (lane >> 2) * 3072 + (lane & 3) * 16;
        c.vx = (lane >> 3) * 1024 + (((lane & 7) ^ (lane >> 4)) * 16); c.vlin = lane * 16;
        G1State st;
        { unsigned long long dummy = (unsigned long long)c.px; asm volatile("" :: "s"(dummy)); }
        STAMP(19);
        issue_x(c, 0); issue_x(c, 1); issue_w<0>(c);
        STAMP(20);
        u64 ep; aload_flag(ep, ws + WS_EPOCH, 0u);
        u32x4 ax; aload16(ax, (const char*)P.x + (size_t)(128 * c.rb + 8 * c.cg + w) * 1024, c.vlin);
        u32x4 aw0, aw1, aw2, aw3;
        const int ptile = 2 * w + (c.rb & 1); const bool pvalid = ptile <= 8;
        aw0 = aw1 = aw2 = aw3 = (u32x4){0u, 0u, 0u, 0u};
        if (pvalid) {
            const char* tb; unsigned pitch, vo;
            if (ptile < 3) { tb = (const char*)P.wqkv + (ptile == 0 ? 512 : ptile == 1 ? 1024 : 0) * 2 + 64 * c.cg; pitch = 3072; vo = c.vq; }
            else { tb = (const char*)((ptile & 1) ? P.wg : P.wu) + 64 * (fs + ((ptile - 3) >> 1)); pitch = 2752; vo = (lane >> 2) * 2752 + (lane & 3) * 16; }
            tb += (size_t)(64 * (c.rb >> 1)) * pitch;
            aload16(aw0, tb, vo); aload16(aw1, tb + 16 * pitch, vo); aload16(aw2, tb + 32 * pitch, vo); aload16(aw3, tb + 48 * pitch, vo);
        }
        c.vf0 = (lane / 12) * 2752 + (lane % 12) * 16; c.vf1 = ((lane + 64) / 12) * 2752 + ((lane + 64) % 12) * 16; c.vf2 = ((lane + 128) / 12) * 2752 + ((lane + 128) % 12) * 16;
        issue_w<4>(c);
        STAMP(21);
        *(LAS unsigned*)(c.lds + L_ZERO + tid * 4) = 0u;
        if (tid < 4) { *(LAS unsigned*)(c.lds + L_ONES + tid * 4) = 0x3f803f80u; *(LAS unsigned*)(c.lds + L_ONES + 1024 + tid * 4) = 0x3f803f80u; }
        if (tid >= 64 && tid < 72) *(LAS unsigned*)(c.lds + L_CNT + (tid - 64) * 4) = 0u;
        { const unsigned lb = c.lds0 + (8 * h + ((lane & 15) >> 2)) * 64 + 32 * ((lane >> 4) & 1) + 8 * (lane & 3);
#pragma unroll
          for (int j = 0; j < 4; ++j) st.wbq[j] = lb + ((j ^ c.tm) * 1024) + wslot(0); }
        { const unsigned lb = c.lds0 + (8 * h + ((lane & 15) >> 2)) * 192 + 32 * ((lane >> 4) & 1) + 8 * (lane & 3);
#pragma unroll
          for (int j = 0; j < 4; ++j) st.wbf[j] = lb + ((j ^ c.tm) * 3072) + wslot(4); }
        { const int row = 32 * c.tm + r32;
#pragma unroll
          for (int j = 0; j < 4; ++j) st.xb[j] = c.lds0 + xslot(0) + row * 128 + (((2 * (j ^ c.tm) + h) ^ ((row >> 1) & 7)) * 16); }
        st.a0 = f32x16{}; st.a1 = f32x16{}; st.b0 = f32x16{}; st.b1 = f32x16{}; st.qa = f32x16{}; st.qb = f32x16{};

        STAMP(22);
        SYNC(3);
        PIN(pfd);
        asm volatile("" : "+v"(ep), "+v"(ax), "+v"(aw0), "+v"(aw1), "+v"(aw2), "+v"(aw3));
        tag = mix64(((u64)(unsigned)__builtin_amdgcn_readfirstlane((int)(unsigned)(ep >> 32)) << 32) | (unsigned)__builtin_amdgcn_readfirstlane((int)(unsigned)ep));
        tagLo = (unsigned)tag; tagHi = (unsigned)(tag >> 32);
        STAMP(1);

        float as_ = 0.f, aq_ = 0.f, av[8];
        auto aux0 = [&](int f) {
            if (f < 4) { const float a = blo(ax[f]), b = bhi(ax[f]); as_ += a + b; aq_ = __builtin_fmaf(a, a, aq_); aq_ = __builtin_fmaf(b, b, aq_); PIN(as_); PIN(aq_); }
            if (f == 4) { as_ = dppadd<0x121>(as_); aq_ = dppadd<0x121>(aq_); as_ = dppadd<0x122>(as_); aq_ = dppadd<0x122>(aq_); PIN(as_); PIN(aq_); }
            if (f == 5) { as_ = dppadd<0x124>(as_); aq_ = dppadd<0x124>(aq_); as_ = dppadd<0x128>(as_); aq_ = dppadd<0x128>(aq_); PIN(as_); PIN(aq_); }
            if (f == 6) { swap16add(as_, as_); swap16add(aq_, aq_); swap32add(as_, as_); swap32add(aq_, aq_); PIN(as_); PIN(aq_); }
            if (f == 8) { const float mean = as_ * (1.f / 512.f), var = aq_ * (1.f / 512.f) - mean * mean, rstd = __builtin_amdgcn_rsqf(fmaxf(var, 0.f) + 1e-5f);
                if (lane == 0) __builtin_amdgcn_raw_buffer_store_b128((u32x4){__float_as_uint(mean), tagLo, __float_as_uint(rstd), tagHi}, wsr, (unsigned)(WS_ST + (128 * c.rb + 8 * c.cg + w) * 16), 0, 16); }
        };
        auto aux1 = [&](int f) {
            if (f < 4) { const int i = f; av[2 * i] = (blo(aw0[i]) + blo(aw1[i])) + (blo(aw2[i]) + blo(aw3[i])); av[2 * i + 1] = (bhi(aw0[i]) + bhi(aw1[i])) + (bhi(aw2[i]) + bhi(aw3[i])); PIN(av[2 * i]); PIN(av[2 * i + 1]); }
            if (f == 8) { swap32add(av[0], av[4]); swap32add(av[1], av[5]); swap32add(av[2], av[6]); swap32add(av[3], av[7]); PIN(av[0]); PIN(av[1]); PIN(av[2]); PIN(av[3]); }
            if (f == 9) { swap16add(av[0], av[2]); swap16add(av[1], av[3]); av[0] = dppadd<0x124>(av[0]); av[1] = dppadd<0x124>(av[1]); av[0] = dppadd<0x128>(av[0]); av[1] = dppadd<0x128>(av[1]); PIN(av[0]); PIN(av[1]); }
            if (f == 10) { const int rho = lane >> 4, col = 8 * (lane & 3) + 4 * (rho >> 1) + 2 * (rho & 1);
                const unsigned off = pvalid ? (unsigned)(WS_CS + (((c.cg * 9 + ptile) * 8 + (c.rb >> 1)) * 32 + col) * 8) : (unsigned)(WS_SCR + (blockIdx.x * 8 + w) * 256 + col * 8);
                if ((lane & 12) == 0) __builtin_amdgcn_raw_buffer_store_b128((u32x4){__float_as_uint(av[0]), tagLo, __float_as_uint(av[1]), tagLo}, wsr, off, 0, 16); }
        };
        u32x4 gs;
        const int gt1 = w < 3 ? w : w < 6 ? w - 3 : w - 6, gt2 = w < 6 ? 3 + w : w - 3;
        stage<G, 0, true, true, -1, -1>(st, [&](int f_) { if (f_ < 2) issue_x1(c, 2, f_); else if (f_ < 4) issue_x1(c, 3, f_ - 2); else if (f_ < 7) issue_w1<1>(c, f_ - 4); aux0(f_); }); SYNCV(8);
        stage<G, 4, true, false, -1, -1>(st, [&](int f_) { if (f_ < 3) issue_w1<5>(c, f_); else if (f_ < 5) issue_x1(c, 4, f_ - 3); else if (f_ < 7) issue_x1(c, 5, f_ - 5); aux1(f_); }); SYNCV(9);
        stage<G, 1, true, true, -1, -1>(st, [&](int f_) { if (f_ >= 1 && f_ < 4) issue_w1<2>(c, f_ - 1); }); SYNCV(4);
        stage<G, 5, true, false, 4, -1>(st, [&](int f_) { if (f_ < 3) issue_w1<6>(c, f_); else if (f_ < 5) issue_x1(c, 6, f_ - 3); else if (f_ < 7) issue_x1(c, 7, f_ - 5); }); SYNCV(7);
        stage<G, 2, true, false, -1, -1>(st, [&](int f_) { if (f_ >= 1 && f_ < 4) issue_w1<3>(c, f_ - 1);
            if (f_ == 8) { aload16_sc1(gs, P.ws, (unsigned)(WS_ST + (128 * c.rb + 32 * c.tm + (opaque(lane) & 31)) * 16)); GATHER_ISSUE(gt1, false); } }); SYNCV(6);
        stage<G, 6, true, false, 6, -1>(st, [&](int f_) { if (f_ >= 1 && f_ < 4) issue_w1<7>(c, f_ - 1); else if (f_ >= 4 && f_ < 7) issue_w1<8>(c, f_ - 4); }); SYNCV(9);
        stage<G, 3, true, false, -1, -1>(st, [&](int f_) { if (f_ >= 1 && f_ < 4) issue_w1<9>(c, f_ - 1);
            if (f_ == 10) { WAITV(9); PIN(gs);
                if (!__all(gs[1] == tagLo && gs[3] == tagHi)) { const unsigned gso = (unsigned)(WS_ST + (128 * c.rb + 32 * c.tm + (opaque(lane) & 31)) * 16);
                    for (unsigned spins = 0;; ++spins) { __builtin_amdgcn_s_sleep(2); gs = __builtin_amdgcn_raw_buffer_load_b128(wsr, gso, 0, 16); __builtin_amdgcn_s_waitcnt(0x0F70);
                        if (__all(gs[1] == tagLo && gs[3] == tagHi)) break; if (spins > SPIN_CAP) { set_timeout(ws, lane); break; } } }
                ln_m = __uint_as_float(gs[0]); ln_a = __uint_as_float(gs[2]); PIN(ln_m); PIN(ln_a);
                GATHER_CHECK(gt1, false); }
            if (f_ == 11) GATHER_RED(gt1); });
        { lp xp = c.lds + 13 * GR + w * 2048 + c.vlin; *(LAS f32x4*)(xp) = F4(st.qb, 8 - 8 * G); *(LAS f32x4*)(xp + 1024) = F4(st.qb, 12 - 8 * G);
          if constexpr (G == 1) { if (h == 0) { *(LAS float*)(c.lds + L_CSW + c.tm * 256 + r32 * 4) = ln_m; *(LAS float*)(c.lds + L_CSW + c.tm * 256 + 128 + r32 * 4) = ln_a; } } }
        SYNC(3);
        STAMP(2);
        issue_w<10>(c);
        { const int lane = opaque(c.lane), h = lane >> 5, r32 = lane & 31;
          const unsigned o = (unsigned)(WS_KV + ((size_t)c.cg * 64 + T) * 4096) + lane * 16 + G * 2048;
          f32x16 y;
          if constexpr (G == 0) {
#pragma unroll
              for (int q = 0; q < 4; ++q) { const f32x4 cs = ldsf4((lcp)c.lds + L_CS + (8 * q + 4 * h) * 4);
#pragma unroll
                  for (int i = 0; i < 4; ++i) y[4 * q + i] = ln_a * __builtin_fmaf(-ln_m, cs[i], st.qa[4 * q + i]); }
          } else {
              const float cso = *(const LAS float*)(c.lds + L_CS + 128 + r32 * 4);
#pragma unroll
              for (int q = 0; q < 4; ++q) { const f32x4 mm = ldsf4((lcp)c.lds + L_CSW + c.tm * 256 + (8 * q + 4 * h) * 4), aa = ldsf4((lcp)c.lds + L_CSW + c.tm * 256 + 128 + (8 * q + 4 * h) * 4);
#pragma unroll
                  for (int i = 0; i < 4; ++i) y[4 * q + i] = aa[i] * __builtin_fmaf(-mm[i], cso, st.qa[4 * q + i]); }
          }
          const u32x4 k0 = pack8(y, 0), k1 = pack8(y, 8);
          __builtin_amdgcn_raw_buffer_store_b128(k0, wsr, o, 0, 16); __builtin_amdgcn_raw_buffer_store_b128(k1, wsr, o + 1024, 0, 16);
          if (w == 0 && lane == 0) store_flag(ws, WS_SF + (size_t)blockIdx.x * 8, tag);
          *(LAS u32x4*)(c.lds + L_KVR + c.tm * 4096 + G * 2048 + lane * 16) = k0; *(LAS u32x4*)(c.lds + L_KVR + c.tm * 4096 + G * 2048 + 1024 + lane * 16) = k1;
          SB();
          const f32x4 c0 = ldsf4((lcp)c.lds + L_CS + 2 * 128 + (16 * G + 4 * h) * 4), c1 = ldsf4((lcp)c.lds + L_CS + 2 * 128 + (16 * G + 8 + 4 * h) * 4);
          const lcp xp = (lcp)c.lds + 13 * GR + (w ^ 4) * 2048 + lane * 16;
          const f32x4 p0 = ldsf4(xp), p1 = ldsf4(xp + 1024);
          const float sc = ln_a * (0.25f * 1.4426950408889634f);
#pragma unroll
          for (int i = 0; i < 4; ++i) { y[i] = sc * __builtin_fmaf(-ln_m, c0[i], st.qb[8 * G + i] + p0[i]); y[4 + i] = sc * __builtin_fmaf(-ln_m, c1[i], st.qb[8 * G + 4 + i] + p1[i]); }
          qf = __builtin_bit_cast(bf16x8, pack8(y, 0)); PIN(qf); }
        if (EXIT_AT == 1) return;
        stage<G, 7, true, false, -1, 8>(st, [&](int) {}); SYNC(5);
        { const int ln = opaque(lane); c.vd = (ln >> 3) * 1024 + (((ln & 7) ^ (((ln >> 4) & 1) << 2)) * 16); }
        stage<G, 8, false, false, -1, 9>(st, [&](int f_) { if (f_ >= 1 && f_ < 4) issue_w1<11>(c, f_ - 1); if (f_ == 4) issue_d(c, 0); }); SYNCV(6);
        STAMP(3);
        stage<G, 9, false, false, -1, 10>(st, [&](int f_) { if (f_ >= 1 && f_ < 4) issue_d(c, f_);
            if (f_ == 5) { WAITV(7); if (lane == 0) store_flag(ws, WS_KVF + ((size_t)(c.cg * 64 + T) * 2 + G) * 8, tag); }
            if (f_ == 8) GATHER_ISSUE(gt2, false); }); SYNCV(7);
        u64 kf_;
        stage<G, 10, false, false, -1, 11>(st, [&](int f_) { if (f_ >= 1 && f_ < 4) issue_d(c, 3 + f_);
            if (f_ == 6) { WAITV(3); GATHER_CHECK(gt2, false); GATHER_RED(gt2); }
            if (f_ == 10) { const int tl = 4 * c.rb - 8 + w; aload_flag(kf_, P.ws, (unsigned)(WS_KVF + ((c.cg * 64 + (tl < 0 ? 0 : tl)) * 2 + (opaque(lane) & 1)) * 8)); } }); SYNCV(63);
        STAMP(4);
        stage<G, 11, false, false, -1, -1>(st, [&](int f_) { if (f_ >= 1 && f_ < 3) issue_d(c, 6 + f_);
            if (f_ == 6) { WAITV(2); asm volatile("" : "+v"(kf_)); const int tl = 4 * c.rb - 8 + w;
                if (tl >= 0) {
                    if (!__all(kf_ == tag)) poll_flags((const u64*)(ws + WS_KVF) + (c.cg * 64 + tl) * 2, 2, tag, lane, ws);
                    const char* base = ws + WS_KV + ((size_t)c.cg * 64 + tl) * 4096; const unsigned dst = c.lds0 + L_KV + w * 4096;
                    dma16_sc1(c.vlin, base, dst); dma16_sc1(c.vlin, base + 1024, dst + 1024); dma16_sc1(c.vlin, base + 2048, dst + 2048); dma16_sc1(c.vlin, base + 3072, dst + 3072); } } });
        {
          lp xp = c.lds + 2 * GR + w * 4096 + c.vlin;
          SYNCV(63);
          *(LAS f32x4*)(xp) = F4(st.b0, 8 - 8 * G); *(LAS f32x4*)(xp + 1024) = F4(st.b0, 12 - 8 * G); *(LAS f32x4*)(xp + 2048) = F4(st.b1, 8 - 8 * G); *(LAS f32x4*)(xp + 3072) = F4(st.b1, 12 - 8 * G); }
        SYNC(63);
        STAMP(5);
        {
          const int lane = opaque(c.lane), h = lane >> 5;
          const float a2 = ln_a * ln_a;
          { u32x4 y0, y1;
#pragma unroll
            for (int q = 0; q < 4; ++q) {
                const f32x4 cg_ = ldsf4((lcp)c.lds + L_CS + (3 + 4 * G) * 128 + (8 * q + 4 * h) * 4), cu_ = ldsf4((lcp)c.lds + L_CS + (4 + 4 * G) * 128 + (8 * q + 4 * h) * 4);
                float y[4];
#pragma unroll
                for (int i = 0; i < 4; ++i) { const float g_ = fmaxf(__builtin_fmaf(-ln_m, cg_[i], st.a0[4 * q + i]), 0.f), u_ = __builtin_fmaf(-ln_m, cu_[i], st.a1[4 * q + i]); y[i] = (g_ * a2) * u_; }
                const unsigned lo = pk2(y[0], y[1]), hi = pk2(y[2], y[3]);
                if (q < 2) { y0[2 * q] = lo; y0[2 * q + 1] = hi; } else { y1[2 * q - 4] = lo; y1[2 * q - 3] = hi; }
            }
            if (G == 0 ? v0 : v2) { const unsigned o_ = (unsigned)(WS_C + ((size_t)T * NKS + 2 * (fs + 2 * G)) * 1024) + lane * 16;
                __builtin_amdgcn_raw_buffer_store_b128(y0, wsr, o_, 0, 16); __builtin_amdgcn_raw_buffer_store_b128(y1, wsr, o_ + 1024, 0, 16); } }
          { const lcp xp = (lcp)c.lds + 2 * GR + (w ^ 4) * 4096 + lane * 16;
            u32x4 y0;
#pragma unroll
            for (int q = 0; q < 2; ++q) {
                const f32x4 pg = ldsf4(xp + q * 1024), pu = ldsf4(xp + 2048 + q * 1024);
                const f32x4 cg_ = ldsf4((lcp)c.lds + L_CS + 5 * 128 + (16 * G + 8 * q + 4 * h) * 4), cu_ = ldsf4((lcp)c.lds + L_CS + 6 * 128 + (16 * G + 8 * q + 4 * h) * 4);
                float y[4];
#pragma unroll
                for (int i = 0; i < 4; ++i) { const float g_ = fmaxf(__builtin_fmaf(-ln_m, cg_[i], st.b0[8 * G + 4 * q + i] + pg[i]), 0.f), u_ = __builtin_fmaf(-ln_m, cu_[i], st.b1[8 * G + 4 * q + i] + pu[i]); y[i] = (g_ * a2) * u_; }
                y0[2 * q] = pk2(y[0], y[1]); y0[2 * q + 1] = pk2(y[2], y[3]);
            }
            __builtin_amdgcn_raw_buffer_store_b128(y0, wsr, (unsigned)(WS_C + ((size_t)T * NKS + 2 * (fs + 1) + G) * 1024) + lane * 16, 0, 16); } }
        if (G == 0 ? v0 : v2) { SYNC(3); } else { SYNC(1); }
        if constexpr (G == 1) __builtin_amdgcn_s_setprio(1);
    };
    if (c.g == 0) gemm1(std::integral_constant<int, 0>{}); else gemm1(std::integral_constant<int, 1>{});
    STAMP(6);
    if (EXIT_AT <= 2) goto done;
    {
    const int lane2 = opaque(lane);
    const unsigned cbase = (unsigned)(WS_C + ((size_t)T2 * NKS + kq) * 1024) + lane2 * 16;
    bf16x8 hf[22];
    u64 hfl; bool hrdy_ = false;
#define LDH(i) do { const bool ok_ = ((i) < 21) || (kq < 2); hf[i] = __builtin_bit_cast(bf16x8, __builtin_amdgcn_raw_buffer_load_b128(wsr, ok_ ? cbase + (i) * 4096 : cbase, 0, 16)); } while (0)
    {
        const int lane = lane2, h = lane >> 5, r32 = lane & 31;
        const int i0 = T >= 8 ? 0 : 8 - T;
        const unsigned kb = c.lds0 + L_KV + c.tm * 4096 + c.g * 1024 + lane * 16;
        const unsigned lanebit = r32 < 16 ? 1u : 0u;
        const unsigned vb = c.lds0 + (r32 < 16 ? (unsigned)(((r32 & 15) + 16 * c.g + 32 * h) * 16) : (unsigned)L_ONES);
        auto koff = [&](int i) -> unsigned { return (unsigned)((i >= i0 ? i : 7) * 4096); };
        auto rdk = [&](int i) -> bf16x8 { return ldsf((lcp)(uintptr_t)(kb + koff(i))); };
        auto vaddr = [&](int i) -> unsigned { const bool valid = i >= i0; const unsigned d = valid ? 0u : (unsigned)(L_ZERO - L_ONES), A = valid ? (unsigned)(L_KV + 2048 + (c.tm + i) * 4096) : (unsigned)L_ZERO;
                                              return lanebit * (A - d) + (vb + d); };
        auto dmask = [&](int r) -> bool { u64 mk = 0;
#pragma unroll
            for (int l = 0; l < 64; ++l) { const int kl = (r & 3) + 8 * (r >> 2) + 4 * (l >> 5); if (kl <= (l & 31)) mk |= 1ull << l; }
            return __builtin_amdgcn_inverse_ballot_w64(mk); };
        const f32x16 zero = f32x16{};
        float m0 = -1e30f, m1 = -1e30f;
#define MAXT(S) do { _Pragma("unroll") for (int r = 0; r < 16; r += 4) { m0 = fmaxf(fmaxf(m0, S[r]), S[r + 1]); m1 = fmaxf(fmaxf(m1, S[r + 2]), S[r + 3]); } PIN(m0); PIN(m1); } while (0)
#define EXPT(S) do { _Pragma("unroll") for (int r = 0; r < 16; ++r) S[r] = __builtin_amdgcn_exp2f(S[r]); } while (0)
#define H_PUBLISH(N) do { STAMP(17); WAITV(N); STAMP(18); if (lane == 0) store_flag(ws, WS_HF + ((size_t)T * 32 + 2 * c.cg + c.g) * 8, tag); } while (0)
#define H_FLAGS() aload_flag(hfl, P.ws, (unsigned)(WS_HF + (T2 * 32 + (lane & 31)) * 8))
        f32x16 o = f32x16{};
        float m; bool hrdy = false;
        if (T == 0) {
            f32x16 s = mfma(ldsf((lcp)(uintptr_t)(kb + 8 * 4096)), qf, zero);
#pragma unroll
            for (int r = 0; r < 16; ++r) s[r] = dmask(r) ? s[r] : -1e30f;
            MAXT(s);
            m = fmaxf(m0, m1);
            { auto rr = __builtin_amdgcn_permlane32_swap(__float_as_uint(m), __float_as_uint(m), false, false); m = fmaxf(__uint_as_float(rr[0]), __uint_as_float(rr[1])); }
#pragma unroll
            for (int r = 0; r < 16; ++r) s[r] = __builtin_amdgcn_exp2f(s[r] - m);
            const unsigned va = vaddr(8);
            o = mfma(ldsf((lcp)(uintptr_t)va), __builtin_bit_cast(bf16x8, pack8(s, 0)), o);
            o = mfma(ldsf((lcp)(uintptr_t)(va + 1024)), __builtin_bit_cast(bf16x8, pack8(s, 8)), o);
            issue_d(c, 9); issue_d(c, 10); issue_d(c, 11); issue_d(c, 12);
            H_PUBLISH(4);
        } else {
            bf16x8 k0 = rdk(0), k8 = rdk(8), k1 = rdk(1), k2 = rdk(2), k3 = rdk(3);
            f32x16 sA, sB, sC;
            sA = mfma_pin0(k0, qf); sB = mfma_pin0(k8, qf); SB();
            k0 = rdk(4); k8 = rdk(5); sC = mfma_pin0(k1, qf); k1 = rdk(6); SB();
            asm volatile("s_nop 7" ::: );
#pragma unroll
            for (int r = 0; r < 16; ++r) sA[r] = dmask(r) ? sB[r] : sA[r];
            PIN(sA); SB();
            sB = mfma_pin0(k2, qf); k2 = rdk(7); SB();
            MAXT(sA); SB();
            sA = mfma_pin0(k3, qf); issue_d(c, 9); SB(); MAXT(sC); SB();
            sC = mfma_pin0(k0, qf); issue_d(c, 10); SB(); MAXT(sB); SB();
            sB = mfma_pin0(k8, qf); issue_d(c, 11); SB(); MAXT(sA); SB();
            sA = mfma_pin0(k1, qf); issue_d(c, 12); SB(); MAXT(sC); SB();
            H_PUBLISH(4);
            sC = mfma_pin0(k2, qf); k0 = rdk(0); k8 = rdk(8); k1 = rdk(1); SB(); MAXT(sB); SB();
            MAXT(sA); SB(); MAXT(sC);
            m = fmaxf(m0, m1);
            { auto rr = __builtin_amdgcn_permlane32_swap(__float_as_uint(m), __float_as_uint(m), false, false); m = fmaxf(__uint_as_float(rr[0]), __uint_as_float(rr[1])); }
            STAMP(9);
            f32x16 cm;
#pragma unroll
            for (int r = 0; r < 16; ++r) cm[r] = -m;
            PIN(cm);
            asm volatile("s_nop 1" ::: ); sA = mfma_pin(k0, qf, cm); sB = mfma_pin(k8, qf, cm); sC = mfma_pin(k1, qf, cm); SB();
            bf16x8 va0, va1, vb0, vb1;
            { const unsigned a0 = vaddr(0), a8 = vaddr(8); va0 = ldsf((lcp)(uintptr_t)a0); va1 = ldsf((lcp)(uintptr_t)(a0 + 1024)); vb0 = ldsf((lcp)(uintptr_t)a8); vb1 = ldsf((lcp)(uintptr_t)(a8 + 1024)); }
            k0 = rdk(2);
            unsigned mp[8];
#pragma unroll
            for (int j = 0; j < 8; ++j) mp[j] = (dmask(2 * j) ? 0xffffu : 0u) | (dmask(2 * j + 1) ? 0xffff0000u : 0u);
            SB();
#pragma unroll
            for (int r = 0; r < 16; ++r) sA[r] = dmask(r) ? sB[r] : sA[r];
            EXPT(sA);
            { const u32x4 p0 = pack8(sA, 0), p1 = pack8(sA, 8); u32x4 d0, d1, o0, o1;
#pragma unroll
              for (int j = 0; j < 4; ++j) { d0[j] = p0[j] & mp[j]; d1[j] = p1[j] & mp[4 + j]; o0[j] = p0[j] & ~mp[j]; o1[j] = p1[j] & ~mp[4 + j]; }
              o = mfma(va0, __builtin_bit_cast(bf16x8, o0), o); o = mfma(va1, __builtin_bit_cast(bf16x8, o1), o);
              o = mfma(vb0, __builtin_bit_cast(bf16x8, d0), o); o = mfma(vb1, __builtin_bit_cast(bf16x8, d1), o); }
            { const unsigned a1 = vaddr(1), a2 = vaddr(2); va0 = ldsf((lcp)(uintptr_t)a1); va1 = ldsf((lcp)(uintptr_t)(a1 + 1024)); vb0 = ldsf((lcp)(uintptr_t)a2); vb1 = ldsf((lcp)(uintptr_t)(a2 + 1024)); }
            SB();
#define EXP8(S, b) do { _Pragma("unroll") for (int r = (b); r < (b) + 8; ++r) S[r] = __builtin_amdgcn_exp2f(S[r]); } while (0)
#define PK4(S, b) (u32x4){pk2(S[b], S[(b) + 1]), pk2(S[(b) + 2], S[(b) + 3]), pk2(S[(b) + 4], S[(b) + 5]), pk2(S[(b) + 6], S[(b) + 7])}
#define TILE2(i, S, V0, V1, SN) do { \
                if ((i) + 1 <= 7) { SN = mfma_pin(k0, qf, cm); SB(); if ((i) + 2 <= 7) k0 = rdk((i) + 2); } \
                EXP8(S, 0); { u32x4 p_ = PK4(S, 0); PIN(p_); SB(); o = mfma(V0, __builtin_bit_cast(bf16x8, p_), o); SB(); } \
                EXP8(S, 8); { u32x4 p_ = PK4(S, 8); PIN(p_); SB(); o = mfma(V1, __builtin_bit_cast(bf16x8, p_), o); SB(); } \
                if ((i) + 2 <= 7) { const unsigned a_ = vaddr((i) + 2); V0 = ldsf((lcp)(uintptr_t)a_); V1 = ldsf((lcp)(uintptr_t)(a_ + 1024)); } SB(); } while (0)
            TILE2(1, sC, va0, va1, sB);
            H_FLAGS();
            TILE2(2, sB, vb0, vb1, sC); TILE2(3, sC, va0, va1, sB);
            WAITV(0); asm volatile("" : "+v"(hfl));
            hrdy = __all(hfl == tag);
            if (hrdy) { LDH(0); LDH(1); LDH(2); LDH(3); LDH(4); LDH(5); }
            TILE2(4, sB, vb0, vb1, sC);
            if (hrdy) { LDH(6); LDH(7); LDH(8); LDH(9); LDH(10); LDH(11); }
            TILE2(5, sC, va0, va1, sB);
            if (hrdy) { LDH(12); LDH(13); LDH(14); LDH(15); LDH(16); }
            TILE2(6, sB, vb0, vb1, sC);
            if (hrdy) { LDH(17); LDH(18); LDH(19); LDH(20); LDH(21); }
            TILE2(7, sC, va0, va1, sB);
        }
        const float inv = __builtin_amdgcn_rcpf(o[8]);
        f32x16 y;
#pragma unroll
        for (int r = 0; r < 8; ++r) y[r] = o[r] * inv;
        const unsigned oo = (unsigned)(WS_C + ((size_t)T * NKS + 86 + 2 * c.cg + c.g) * 1024) + lane * 16;
        const u32x4 ov = pack8(y, 0);
        STAMP(28);
        if (EXIT_AT == 3) { __builtin_amdgcn_raw_buffer_store_b128(ov, wsr, oo, 0, 16); goto done; }
        __builtin_amdgcn_raw_buffer_store_b128(ov, wsr, oo, 0, 16);
        STAMP(15);
        if (!hrdy) {
            poll_flags((const u64*)(ws + WS_HF) + T2 * 32, 32, tag, lane, ws);
#pragma unroll
            for (int i = 0; i < 22; ++i) LDH(i);
        }
        STAMP(16);
        hrdy_ = hrdy;
        __builtin_amdgcn_s_setprio(0);
    }
    STAMP(10);
    {
        const int lane = opaque(lane2), h = lane >> 5, r32 = lane & 31, q4 = (lane & 15) >> 2, p4 = lane & 3, b16 = (lane >> 4) & 1;
        const int tok = 64 * c.tb + 32 * th + r32;
        f32x16 acc0 = f32x16{}, acc1 = f32x16{};
        const unsigned wl = c.lds0 + kq * 2048 + (4 * h + q4) * 128 + 32 * b16 + 8 * p4, swz = ((q4 >> 1) & 1) << 6;
        const unsigned wl0 = wl + (0u ^ swz), wl1 = wl + (64u ^ swz);
        bf16x8 wa[3], wb_[3];
#define RDW(u) do { wa[(u) % 3] = trfrag<1024>((lcp)(uintptr_t)(wl0 + dloc(u))); wb_[(u) % 3] = trfrag<1024>((lcp)(uintptr_t)(wl1 + dloc(u))); } while (0)
#define G2STEP(u, frag, nxt) do { acc0 = mfma(wa[(u) % 3], frag, acc0); acc1 = mfma(wb_[(u) % 3], frag, acc1); SB(); if ((nxt) >= 0) RDW((nxt) < 0 ? 0 : (nxt)); SB(); } while (0)
        SYNC(63);
        STAMP(11);
        if (EXIT_AT == 4) goto done;
        const bool ew = blockIdx.x == 0 && w == 7; u32x4 sf0 = {0u, 0u, 0u, 0u}, sf1 = sf0; bool edone = false;
        if (ew) { aload16_sc1(sf0, P.ws + WS_SF, (unsigned)(lane * 32)); aload16_sc1(sf1, P.ws + WS_SF + 16, (unsigned)(lane * 32)); }
        RDW(0); RDW(1); RDW(2);
        G2STEP(0, hf[0], 3);
        issue_d(c, 13); G2STEP(1, hf[1], 4); issue_d(c, 14); G2STEP(2, hf[2], 5); issue_d(c, 15); G2STEP(3, hf[3], 6); issue_d(c, 16); G2STEP(4, hf[4], 7); issue_d(c, 17); G2STEP(5, hf[5], 8); issue_d(c, 18);
        if (hrdy_) { WAITV(6); }
        if (lane == 0) store_flag(ws, WS_OF + ((size_t)T * 32 + 2 * c.cg + c.g) * 8, tag);
        SYNCV(63);
        G2STEP(6, hf[6], 9); issue_d(c, 19); G2STEP(7, hf[7], 10); issue_d(c, 20); G2STEP(8, hf[8], 11); issue_d(c, 21); G2STEP(9, hf[9], 12); issue_d(c, 22);
        G2STEP(10, hf[10], -1); issue_d(c, 23); G2STEP(11, hf[11], -1); issue_d(c, 24); G2STEP(12, hf[12], -1);
        STAMP(24);
        SYNCV(7);
        STAMP(25);
        if (ew) { asm volatile("" : "+v"(sf0), "+v"(sf1));
            edone = __all(sf0[0] == tagLo && sf0[1] == tagHi && sf0[2] == tagLo && sf0[3] == tagHi && sf1[0] == tagLo && sf1[1] == tagHi && sf1[2] == tagLo && sf1[3] == tagHi);
            if (edone && lane == 0) store_flag(ws, WS_EPOCH, tag); }
        edone_ = edone;
        RDW(13); RDW(14); RDW(15);
        uint2 xa, xb2;
        const int nt = (w >> 1) & 1, rh = w >> 2;
        const int n0 = 64 * c.nb + 32 * nt + 16 * rh + 4 * h;
        u64 ofl; aload_flag(ofl, P.ws, (unsigned)(WS_OF + (T2 * 32 + (lane & 31)) * 8));
        issue_d(c, 25); G2STEP(13, hf[13], 16); issue_d(c, 26); G2STEP(14, hf[14], 17); issue_d(c, 27); G2STEP(15, hf[15], 18); issue_d(c, 28); G2STEP(16, hf[16], -1); issue_d(c, 29);
        WAITV(5); asm volatile("" : "+v"(ofl));
        if (!__all(ofl == tag)) poll_flags((const u64*)(ws + WS_OF) + T2 * 32, 32, tag, lane, ws);
        STAMP(26);
        bf16x8 of[8];
#pragma unroll
        for (int i = 0; i < 8; ++i) of[i] = __builtin_bit_cast(bf16x8, __builtin_amdgcn_raw_buffer_load_b128(wsr, cbase + (86 + 4 * i) * 1024, 0, 16));
        { const u16* xr = P.x + (size_t)tok * 512 + n0; xa = *(const uint2*)xr; xb2 = *(const uint2*)(xr + 8); }
        G2STEP(17, hf[17], -1); G2STEP(18, hf[18], -1);
        STAMP(12);
        SYNCV(63);
        RDW(19); RDW(20); RDW(21);
        G2STEP(19, hf[19], 22); G2STEP(20, hf[20], 23);
        if (kq < 2) { acc0 = mfma(wa[21 % 3], hf[21], acc0); acc1 = mfma(wb_[21 % 3], hf[21], acc1); }
        SB(); RDW(24); SB();
        STAMP(13);
        G2STEP(22, of[0], -1); G2STEP(23, of[1], -1); G2STEP(24, of[2], -1);
        SYNCV(63);
        RDW(25); RDW(26); RDW(27);
        G2STEP(25, of[3], 28); G2STEP(26, of[4], 29); G2STEP(27, of[5], -1); G2STEP(28, of[6], -1); G2STEP(29, of[7], -1);
        STAMP(27);
        { lp rbp = c.lds + (kq < 2 ? 4 * GR : 13 * GR - 32768) + (kq * 2 + th) * 8192 + lane * 16;
#pragma unroll
          for (int q = 0; q < 4; ++q) { *(LAS f32x4*)(rbp + q * 1024) = F4(acc0, 4 * q); *(LAS f32x4*)(rbp + 4096 + q * 1024) = F4(acc1, 4 * q); } }
        SYNC(63);
        { f32x4 s0 = {0.f, 0.f, 0.f, 0.f}, s1 = s0;
#pragma unroll
          for (int k = 0; k < 4; ++k) { const lcp bp = (lcp)c.lds + (k < 2 ? 4 * GR : 13 * GR - 32768) + (k * 2 + th) * 8192 + nt * 4096 + 2 * rh * 1024 + lane * 16; s0 += ldsf4(bp); s1 += ldsf4(bp + 1024); }
          uint2 oa, ob;
          oa.x = pk2(s0[0] + blo(xa.x), s0[1] + bhi(xa.x)); oa.y = pk2(s0[2] + blo(xa.y), s0[3] + bhi(xa.y));
          ob.x = pk2(s1[0] + blo(xb2.x), s1[1] + bhi(xb2.x)); ob.y = pk2(s1[2] + blo(xb2.y), s1[3] + bhi(xb2.y));
          u16* orow = P.out + (size_t)tok * 512 + n0;
          *(uint2*)orow = oa; *(uint2*)(orow + 8) = ob; }
    }
    STAMP(14);
    }
done:
    if (blockIdx.x == 0 && w == 7 && !edone_) {
        const GAS u64* pp = (const GAS u64*)(ws + WS_SF) + lane * 4;
        for (unsigned spins = 0;; ++spins) { bool ok = true;
#pragma unroll
            for (int j = 0; j < 4; ++j) ok &= __hip_atomic_load(pp + j, __ATOMIC_RELAXED, __HIP_MEMORY_SCOPE_AGENT) == tag;
            if (__all(ok)) break; if (spins > SPIN_CAP) { set_timeout(ws, lane); break; } __builtin_amdgcn_s_sleep(2); }
        if (lane == 0) store_flag(ws, WS_EPOCH, tag);
    }
}

extern "C" void kernel_launch(void* const* d_in, const int* in_sizes, int n_in, void* d_out, int out_size, void* d_ws, size_t ws_size, hipStream_t stream) {
    Params p;
    p.x = (const u16*)d_in[0]; p.wqkv = (const u16*)d_in[1]; p.wo = (const u16*)d_in[2];
    p.wg = (const u16*)d_in[4]; p.wu = (const u16*)d_in[5]; p.wd = (const u16*)d_in[6];
    p.out = (u16*)d_out; p.ws = (char*)d_ws;
    (void)hipFuncSetAttribute((const void*)fused_kernel, hipFuncAttributeMaxDynamicSharedMemorySize, LDS_BYTES);
    fused_kernel<<<256, 512, LDS_BYTES, stream>>>(p);
}
```

```cpp
#include <hip/hip_runtime.h>
#include <stdint.h>
#include <type_traits>

#define FI __device__ __forceinline__
#define LAS __attribute__((address_space(3)))
#define GAS __attribute__((address_space(1)))
typedef unsigned short u16;
typedef unsigned long long u64;
typedef short bf16x8 __attribute__((ext_vector_type(8)));
typedef short s16x4 __attribute__((ext_vector_type(4)));
typedef float f32x16 __attribute__((ext_vector_type(16)));
typedef float f32x4 __attribute__((ext_vector_type(4)));
typedef float f32x2 __attribute__((ext_vector_type(2)));
typedef unsigned u32x4 __attribute__((ext_vector_type(4)));
typedef __bf16 bf16x2_t __attribute__((ext_vector_type(2)));
typedef LAS const char* lcp;
typedef LAS char* lp;

constexpr int GR = 8192;
constexpr int L_KV = 13 * GR;
constexpr int L_KVR = 17 * GR;
constexpr int L_MISC = 19 * GR;
constexpr int L_CS = L_MISC;
constexpr int L_CSW = L_MISC + 2048;
constexpr int L_ZERO = L_MISC + 4096;
constexpr int L_ONES = L_MISC + 6144;
constexpr int LDS_BYTES = 163840;

constexpr size_t WS_EPOCH = 0, WS_TMO = 64;
constexpr size_t WS_SF = 4096;
constexpr size_t WS_KVF = 8192;
constexpr size_t WS_HF = 24576;
constexpr size_t WS_OF = 40960;
constexpr size_t WS_ST = 65536;
constexpr size_t WS_CS = 131072;
constexpr size_t WS_SCR = 768u << 10;
constexpr size_t WS_KV = 2u << 20;
constexpr size_t WS_C = 8u << 20;
constexpr int NKS = 118;

FI f32x16 mfma(bf16x8 a, bf16x8 b, f32x16 c) { return __builtin_amdgcn_mfma_f32_32x32x16_bf16(a, b, c, 0, 0, 0); }
FI f32x16 mfma_pin(bf16x8 a, bf16x8 b, const f32x16& c) { f32x16 d; asm volatile("v_mfma_f32_32x32x16_bf16 %0, %1, %2, %3" : "=&v"(d) : "v"(a), "v"(b), "v"(c)); return d; }
FI f32x16 mfma_pin0(bf16x8 a, bf16x8 b) { f32x16 d; asm volatile("v_mfma_f32_32x32x16_bf16 %0, %1, %2, 0" : "=&v"(d) : "v"(a), "v"(b)); return d; }
FI unsigned pk2(float lo, float hi) { f32x2 v = {lo, hi}; bf16x2_t r = __builtin_convertvector(v, bf16x2_t); return __builtin_bit_cast(unsigned, r); }
FI u32x4 pack8(const f32x16& y, int b) { u32x4 r; r[0] = pk2(y[b], y[b + 1]); r[1] = pk2(y[b + 2], y[b + 3]); r[2] = pk2(y[b + 4], y[b + 5]); r[3] = pk2(y[b + 6], y[b + 7]); return r; }
typedef short v4i16_t __attribute__((ext_vector_type(4)));
FI s16x4 trd(lcp p) { return __builtin_bit_cast(s16x4, __builtin_amdgcn_ds_read_tr16_b64_v4i16((LAS v4i16_t*)p)); }
template <int D2> FI bf16x8 trfrag(lcp p) { s16x4 lo = trd(p), hi = trd(p + D2); return __builtin_shufflevector(lo, hi, 0, 1, 2, 3, 4, 5, 6, 7); }
FI bf16x8 ldsf(lcp p) { return *(const LAS bf16x8*)p; }
FI f32x4 ldsf4(lcp p) { return *(const LAS f32x4*)p; }

FI void dma16(unsigned voff, const void* sbase, unsigned dst) {
    asm volatile("s_mov_b32 m0, %2\n\ts_nop 0\n\tglobal_load_lds_dwordx4 %0, %1" :: "v"(voff), "s"(sbase), "s"(__builtin_amdgcn_readfirstlane(dst)) : "memory", "m0");
}
FI void dma16_sc1(unsigned voff, const void* sbase, unsigned dst) {
    asm volatile("s_mov_b32 m0, %2\n\ts_nop 0\n\tglobal_load_lds_dwordx4 %0, %1 sc1" :: "v"(voff), "s"(sbase), "s"(__builtin_amdgcn_readfirstlane(dst)) : "memory", "m0");
}
FI void aload16(u32x4& v, const void* base, unsigned voff) { asm volatile("global_load_dwordx4 %0, %1, %2" : "=v"(v) : "v"(voff), "s"(base) : "memory"); }
FI void aload16_sc1(u32x4& v, const void* base, unsigned voff) { asm volatile("global_load_dwordx4 %0, %1, %2 sc1" : "=v"(v) : "v"(voff), "s"(base) : "memory"); }
FI void aload_flag(u64& v, const char* base, unsigned voff) { asm volatile("global_load_dwordx2 %0, %1, %2 sc1" : "=v"(v) : "v"(voff), "s"(base) : "memory"); }

#define SB() __builtin_amdgcn_sched_barrier(0)
#define PIN(x) asm volatile("" : "+v"(x))
FI int opaque(int v) { asm volatile("" : "+v"(v)); return v; }
#define SYNC(N) do { SB(); asm volatile("s_waitcnt vmcnt(" #N ") lgkmcnt(0)\n\ts_barrier" ::: "memory"); SB(); } while (0)
#define SYNCV(N) do { SB(); asm volatile("s_waitcnt vmcnt(" #N ")\n\ts_barrier" ::: "memory"); SB(); } while (0)
#define WAITV(N) do { SB(); asm volatile("s_waitcnt vmcnt(" #N ")" ::: "memory"); SB(); } while (0)

struct Params {
    const u16* x; const u16* wqkv; const u16* wo; const u16* wg; const u16* wu; const u16* wd; u16* out; char* ws;
};

constexpr int wslot(int s) { return (s == 0 ? 4 : s == 1 ? 14 : s == 2 ? 7 : s == 3 ? 0 : s == 4 ? 7 : s == 5 ? 0 : s == 6 ? 10 : s == 7 ? 3 : s == 8 ? 6 : s == 9 ? 9 : s == 10 ? 0 : 3) * GR; }
constexpr int xslot(int c) { return (c == 0 ? 0 : c == 1 ? 2 : c == 2 ? 10 : c == 3 ? 12 : c == 4 ? 3 : c == 5 ? 5 : c == 6 ? 13 : 15) * GR; }
FI constexpr int dloc0(int u) { return (u == 0 ? 12 : u < 4 ? 6 + (u - 1) : u < 7 ? 9 + (u - 4) : u < 9 ? u - 7 : 2 + (u - 9)) * GR; }
FI constexpr int dloc(int u) { return u < 13 ? dloc0(u) : u < 19 ? L_KV + (u - 13) * GR : dloc0(u - 19); }

struct Ctx {
    const Params* p;
    unsigned lds0;
    lp lds;
    int lane, w, tm, g, rb, cg, tb, nb;
    unsigned vq, vf0, vf1, vf2, vx, vlin, vd;
    const char *px, *pq, *pf, *pwd, *pwo;
};

FI void issue_x1(const Ctx& c, int chunk, int k) {
    dma16(k ? c.vx ^ 64u : c.vx, c.px + chunk * 128 + k * 8 * 1024, c.lds0 + xslot(chunk) + 16 * c.w * 128 + k * 8 * 128);
}
FI void issue_x(const Ctx& c, int chunk) { issue_x1(c, chunk, 0); issue_x1(c, chunk, 1); }
template <int S> FI void issue_w1(const Ctx& c, int k) {
    if constexpr (S >= 4) {
        const char* base = c.pf + (size_t)(S - 4) * (64 * 2752);
        dma16(k == 0 ? c.vf0 : k == 1 ? c.vf1 : c.vf2, base, c.lds0 + wslot(S) + c.g * 12288 + c.tm * 3072 + k * 1024);
    } else {
        const char* base = c.pq + (size_t)S * (128 * 3072);
        dma16(c.vq, base + (k == 0 ? 1024 : k == 1 ? 2048 : 0), c.lds0 + wslot(S) + c.w * 1024 + k * 8192);
    }
}
template <int S> FI void issue_w(const Ctx& c) { issue_w1<S>(c, 0); issue_w1<S>(c, 1); issue_w1<S>(c, 2); }
FI void issue_d(const Ctx& c, int u) {
    const char* base;
    if (u < 21) base = c.pwd + (size_t)u * 65536;
    else if (u == 21) base = c.pwd + (size_t)21 * 65536 - (c.w >= 4 ? 32768 : 0);
    else base = c.pwo + (size_t)(u - 22) * 65536;
    dma16(c.vd, base, c.lds0 + dloc(u) + c.w * 1024);
}

FI float blo(unsigned d) { return __uint_as_float(d << 16); }
FI float bhi(unsigned d) { return __uint_as_float(d & 0xffff0000u); }
template <int CTRL> FI float dppadd(float x) { return x + __builtin_bit_cast(float, __builtin_amdgcn_update_dpp(0, __builtin_bit_cast(int, x), CTRL, 0xf, 0xf, true)); }
FI void swap32add(float& u, float v) { auto rr = __builtin_amdgcn_permlane32_swap(__float_as_uint(u), __float_as_uint(v), false, false); u = __uint_as_float(rr[0]) + __uint_as_float(rr[1]); }
FI void swap16add(float& u, float v) { auto rr = __builtin_amdgcn_permlane16_swap(__float_as_uint(u), __float_as_uint(v), false, false); u = __uint_as_float(rr[0]) + __uint_as_float(rr[1]); }

struct G1State {
    bf16x8 xf[32];
    bf16x8 wq[4];
    f32x16 a0, a1, b0, b1;
    f32x16 qa, qb;
    unsigned wbf[4], wbq[4];
    unsigned xb[4];
};
constexpr int fkind(int G, int f) { return G == 0 ? (f < 8 ? (f & 3) : (f & 1)) : (f < 4 ? (f & 1) : f < 6 ? (f & 1) : f < 8 ? 2 + (f & 1) : f < 10 ? (f & 1) : 2 + (f & 1)); }
constexpr int fpos(int G, int f) { return G == 0 ? (f < 8 ? (f >> 2) : 2 + ((f - 8) >> 1)) : (f < 4 ? (f >> 1) : f < 8 ? 2 : 3); }
constexpr int qkind(int G, int f) { return G == 0 ? ((f == 1 || f == 3 || f == 7 || f == 9) ? 1 : 0) : ((f == 3 || f == 5 || f == 9 || f == 11) ? 1 : 0); }
constexpr int qpos(int G, int f) {
    return G == 0 ? (f == 0 ? 0 : f == 1 ? 0 : f == 2 ? 1 : f == 3 ? 1 : f == 4 ? 2 : f == 5 ? 3 : f == 6 ? 4 : f == 7 ? 4 : f == 8 ? 5 : f == 9 ? 5 : f == 10 ? 6 : 7)
                  : (f == 0 ? 0 : f == 1 ? 1 : f == 2 ? 2 : f == 3 ? 2 : f == 4 ? 3 : f == 5 ? 3 : f == 6 ? 4 : f == 7 ? 5 : f == 8 ? 6 : f == 9 ? 6 : f == 10 ? 7 : 7);
}
template <int G, int S> FI bf16x8 rdfrag(const G1State& st, int f) {
    if constexpr (S >= 4) { const int k = fkind(G, f); return trfrag<768>((lcp)(uintptr_t)(st.wbf[fpos(G, f)] + (k & 1) * 12288 + (k >= 2 ? 64 : (G == 0 ? 0 : 128)))); }
    else { const int e = qpos(G, f); return trfrag<256>((lcp)(uintptr_t)(st.wbq[e & 3] + (e >> 2) * 4096 + (qkind(G, f) ? 16384 : G * 8192))); }
}
template <int C> FI void rdx(G1State& st) {
#pragma unroll
    for (int e = 0; e < 4; ++e) { st.xf[4 * C + e] = ldsf((lcp)(uintptr_t)st.xb[e]); if (C < 7) st.xb[e] = (unsigned)opaque((int)(st.xb[e] + (xslot(C + 1) - xslot(C)))); }
}
template <int G, int S, bool FRESH, bool XF, int XP, int NX, class F>
FI void stage(G1State& st, F&& hook) {
    if constexpr (XF) rdx<(XF ? 2 * S : 0)>(st);
    if constexpr (FRESH) {
#pragma unroll
        for (int f = 0; f < 4; ++f) st.wq[f] = rdfrag<G, S>(st, f);
    }
    if constexpr (XF) rdx<(XF ? 2 * S + 1 : 0)>(st);
    SB();
#pragma unroll
    for (int f = 0; f < 12; ++f) {
        const bf16x8 w = st.wq[f & 3];
        if constexpr (S >= 4) {
            const int k = fkind(G, f); const bf16x8 x = st.xf[4 * (S - 4) + fpos(G, f)];
            if (k == 0) st.a0 = mfma(w, x, st.a0); else if (k == 1) st.a1 = mfma(w, x, st.a1); else if (k == 2) st.b0 = mfma(w, x, st.b0); else st.b1 = mfma(w, x, st.b1);
        } else {
            const bf16x8 x = st.xf[8 * S + qpos(G, f)];
            if (qkind(G, f)) st.qb = mfma(w, x, st.qb); else if (G == 0) st.qa = mfma(w, x, st.qa); else st.qa = mfma(x, w, st.qa);
        }
        SB();
        if (f < 8) st.wq[f & 3] = rdfrag<G, S>(st, f + 4);
        else {
            if (f == 8) {
                if constexpr (S < 3) { _Pragma("unroll") for (int j = 0; j < 4; ++j) st.wbq[j] = (unsigned)opaque((int)(st.wbq[j] + (wslot(S + 1) - wslot(S)))); }
                if constexpr (S >= 4 && S < 11) { _Pragma("unroll") for (int j = 0; j < 4; ++j) st.wbf[j] = (unsigned)opaque((int)(st.wbf[j] + (wslot(S + 1) - wslot(S)))); }
            }
            if constexpr (NX >= 0) st.wq[f & 3] = rdfrag<G, (NX >= 0 ? NX : 0)>(st, f - 8);
        }
        if constexpr (XP >= 0) { if (f == 2) rdx<(XP >= 0 ? XP : 0)>(st); if (f == 6) rdx<(XP >= 0 ? XP + 1 : 0)>(st); }
        hook(f);
        SB();
    }
}

FI u64 mix64(u64 z) { z += 0x9E3779B97F4A7C15ull; z = (z ^ (z >> 30)) * 0xBF58476D1CE4E5B9ull; z = (z ^ (z >> 27)) * 0x94D049BB133111EBull; return z ^ (z >> 31); }

constexpr unsigned SPIN_CAP = 1u << 18;
FI void set_timeout(char* ws, int lane) { if (lane == 0) __hip_atomic_store((GAS unsigned*)(ws + WS_TMO), 1u, __ATOMIC_RELAXED, __HIP_MEMORY_SCOPE_AGENT); }
FI void poll_flags(const u64* flags, int n, u64 tag, int lane, char* ws) {
    const GAS u64* pp = (const GAS u64*)flags + (lane < n ? lane : 0);
    for (unsigned spins = 0;; ++spins) {
        const u64 v = __hip_atomic_load(pp, __ATOMIC_RELAXED, __HIP_MEMORY_SCOPE_AGENT);
        if (__all(v == tag)) return;
        if (spins > SPIN_CAP) { set_timeout(ws, lane); return; }
        __builtin_amdgcn_s_sleep(2);
    }
}
FI void store_flag(char* ws, size_t off, u64 tag) { __hip_atomic_store((GAS u64*)(ws + off), tag, __ATOMIC_RELAXED, __HIP_MEMORY_SCOPE_AGENT); }

__global__ __launch_bounds__(512, 2) void fused_kernel(Params P) {
    extern __shared__ __attribute__((aligned(16))) char smem[];
    Ctx c; c.p = &P;
    const int tid = threadIdx.x; c.lane = tid & 63;
    const int w = __builtin_amdgcn_readfirstlane(tid >> 6);
    c.w = w; c.tm = w & 3; c.g = w >> 2;
    c.lds = (lp)smem; c.lds0 = (unsigned)(uintptr_t)c.lds;
    const int xcd = blockIdx.x & 7, idx = blockIdx.x >> 3;
    c.rb = 4 * (xcd >> 1) + (idx >> 3); c.cg = 8 * (xcd & 1) + (idx & 7);
    c.tb = 4 * xcd + (idx >> 3); c.nb = idx & 7;
    const int fs = c.cg < 11 ? 3 * c.cg : c.cg < 15 ? 33 + 2 * (c.cg - 11) : 40;
    const bool v0 = c.cg != 15, v2 = c.cg < 11 || c.cg == 15;
    const int lane = c.lane, h = lane >> 5, r32 = lane & 31;
    char* ws = P.ws;
    c.px = (const char*)P.x + (size_t)(128 * c.rb + 16 * w) * 1024;
    c.pf = (const char*)(c.g ? P.wu : P.wg) + (size_t)(16 * c.tm) * 2752 + fs * 64;
    u64 tag = 0; unsigned tagLo = 0, tagHi = 0; float ln_a = 0.f, ln_m = 0.f;
    const int T = 4 * c.rb + c.tm;
    const __amdgpu_buffer_rsrc_t wsr = __builtin_amdgcn_make_buffer_rsrc(ws, 0, 32u << 20, 0x00020000);
    c.pq = (const char*)P.wqkv + ((size_t)(16 * w) * 1536 + 32 * c.cg) * 2;
    c.pwd = (const char*)P.wd + (size_t)(8 * w) * 1024 + c.nb * 128;
    c.pwo = (const char*)P.wo + (size_t)(8 * w) * 1024 + c.nb * 128;
    u32x4 g0a, g0b;
#define GATHER_OFFS(tile) const int gl_ = opaque(lane); const unsigned go0 = (unsigned)(WS_CS + (c.cg * 9 + (tile)) * 2048 + (gl_ >> 3) * 256 + (gl_ & 7) * 32)
#define GATHER_ISSUE(tile) do { GATHER_OFFS(tile); aload16_sc1(g0a, P.ws, go0); aload16_sc1(g0b, P.ws + 16, go0); } while (0)
#define GATHER_OK() (g0a[1] == tagLo && g0a[3] == tagLo && g0b[1] == tagLo && g0b[3] == tagLo)
#define GATHER_RED(tile) do { const int gl_ = opaque(lane); float v0_ = __uint_as_float(g0a[0]), v1_ = __uint_as_float(g0a[2]), v2_ = __uint_as_float(g0b[0]), v3_ = __uint_as_float(g0b[2]); \
        swap32add(v0_, v2_); swap32add(v1_, v3_); swap16add(v0_, v1_); v0_ = dppadd<0x128>(v0_); \
        if ((gl_ & 8) == 0) *(LAS float*)(c.lds + L_CS + (tile) * 128 + (4 * (gl_ & 7) + 2 * (gl_ >> 5) + ((gl_ >> 4) & 1)) * 4) = v0_; } while (0)
#define GATHER_CHECK(tile) do { asm volatile("" : "+v"(g0a), "+v"(g0b)); \
        if (!__all(GATHER_OK())) { GATHER_OFFS(tile); \
            for (unsigned spins = 0;; ++spins) { __builtin_amdgcn_s_sleep(2); \
                g0a = __builtin_amdgcn_raw_buffer_load_b128(wsr, go0, 0, 16); g0b = __builtin_amdgcn_raw_buffer_load_b128(wsr, go0 + 16, 0, 16); __builtin_amdgcn_s_waitcnt(0x0F70); \
                if (__all(GATHER_OK())) break; if (spins > SPIN_CAP) { set_timeout(ws, lane); break; } } } \
        } while (0)
#define F4(v, b) (f32x4){v[b], v[(b) + 1], v[(b) + 2], v[(b) + 3]}

    const int th = w & 1, kq = w >> 1;
    const int T2 = 2 * c.tb + th;
    bf16x8 qf; bool edone_ = false;
    auto gemm1 = [&](auto Gc) {
        constexpr int G = decltype(Gc)::value;
        const int lane = opaque(c.lane), h = lane >> 5, r32 = lane & 31, tid = w * 64 + lane;
        c.vq = (lane >> 2) * 3072 + (lane & 3) * 16;
        c.vx = (lane >> 3) * 1024 + (((lane & 7) ^ (lane >> 4)) * 16); c.vlin = lane * 16;
        G1State st;
        issue_x(c, 0); issue_x(c, 1); issue_w<0>(c);
        u64 ep; aload_flag(ep, ws + WS_EPOCH, 0u);
        u32x4 ax; aload16(ax, (const char*)P.x + (size_t)(128 * c.rb + 8 * c.cg + w) * 1024, c.vlin);
        u32x4 aw0, aw1, aw2, aw3;
        const int ptile = 2 * w + (c.rb & 1); const bool pvalid = ptile <= 8;
        aw0 = aw1 = aw2 = aw3 = (u32x4){0u, 0u, 0u, 0u};
        if (pvalid) {
            const char* tb; unsigned pitch, vo;
            if (ptile < 3) { tb = (const char*)P.wqkv + (ptile == 0 ? 512 : ptile == 1 ? 1024 : 0) * 2 + 64 * c.cg; pitch = 3072; vo = c.vq; }
            else { tb = (const char*)((ptile & 1) ? P.wg : P.wu) + 64 * (fs + ((ptile - 3) >> 1)); pitch = 2752; vo = (lane >> 2) * 2752 + (lane & 3) * 16; }
            tb += (size_t)(64 * (c.rb >> 1)) * pitch;
            aload16(aw0, tb, vo); aload16(aw1, tb + 16 * pitch, vo); aload16(aw2, tb + 32 * pitch, vo); aload16(aw3, tb + 48 * pitch, vo);
        }
        c.vf0 = (lane / 12) * 2752 + (lane % 12) * 16; c.vf1 = ((lane + 64) / 12) * 2752 + ((lane + 64) % 12) * 16; c.vf2 = ((lane + 128) / 12) * 2752 + ((lane + 128) % 12) * 16;
        issue_w<4>(c);
        *(LAS unsigned*)(c.lds + L_ZERO + tid * 4) = 0u;
        if (tid < 4) { *(LAS unsigned*)(c.lds + L_ONES + tid * 4) = 0x3f803f80u; *(LAS unsigned*)(c.lds + L_ONES + 1024 + tid * 4) = 0x3f803f80u; }
        { const unsigned lb = c.lds0 + (8 * h + ((lane & 15) >> 2)) * 64 + 32 * ((lane >> 4) & 1) + 8 * (lane & 3);
#pragma unroll
          for (int j = 0; j < 4; ++j) st.wbq[j] = lb + ((j ^ c.tm) * 1024) + wslot(0); }
        { const unsigned lb = c.lds0 + (8 * h + ((lane & 15) >> 2)) * 192 + 32 * ((lane >> 4) & 1) + 8 * (lane & 3);
#pragma unroll
          for (int j = 0; j < 4; ++j) st.wbf[j] = lb + ((j ^ c.tm) * 3072) + wslot(4); }
        { const int row = 32 * c.tm + r32;
#pragma unroll
          for (int j = 0; j < 4; ++j) st.xb[j] = c.lds0 + xslot(0) + row * 128 + (((2 * (j ^ c.tm) + h) ^ ((row >> 1) & 7)) * 16); }
        st.a0 = f32x16{}; st.a1 = f32x16{}; st.b0 = f32x16{}; st.b1 = f32x16{}; st.qa = f32x16{}; st.qb = f32x16{};

        SYNC(3);
        asm volatile("" : "+v"(ep), "+v"(ax), "+v"(aw0), "+v"(aw1), "+v"(aw2), "+v"(aw3));
        tag = mix64(((u64)(unsigned)__builtin_amdgcn_readfirstlane((int)(unsigned)(ep >> 32)) << 32) | (unsigned)__builtin_amdgcn_readfirstlane((int)(unsigned)ep));
        tagLo = (unsigned)tag; tagHi = (unsigned)(tag >> 32);

        float as_ = 0.f, aq_ = 0.f, av[8];
        auto aux0 = [&](int f) {
            if (f < 4) { const float a = blo(ax[f]), b = bhi(ax[f]); as_ += a + b; aq_ = __builtin_fmaf(a, a, aq_); aq_ = __builtin_fmaf(b, b, aq_); PIN(as_); PIN(aq_); }
            if (f == 4) { as_ = dppadd<0x121>(as_); aq_ = dppadd<0x121>(aq_); as_ = dppadd<0x122>(as_); aq_ = dppadd<0x122>(aq_); PIN(as_); PIN(aq_); }
            if (f == 5) { as_ = dppadd<0x124>(as_); aq_ = dppadd<0x124>(aq_); as_ = dppadd<0x128>(as_); aq_ = dppadd<0x128>(aq_); PIN(as_); PIN(aq_); }
            if (f == 6) { swap16add(as_, as_); swap16add(aq_, aq_); swap32add(as_, as_); swap32add(aq_, aq_); PIN(as_); PIN(aq_); }
            if (f == 8) { const float mean = as_ * (1.f / 512.f), var = aq_ * (1.f / 512.f) - mean * mean, rstd = __builtin_amdgcn_rsqf(fmaxf(var, 0.f) + 1e-5f);
                if (lane == 0) __builtin_amdgcn_raw_buffer_store_b128((u32x4){__float_as_uint(mean), tagLo, __float_as_uint(rstd), tagHi}, wsr, (unsigned)(WS_ST + (128 * c.rb + 8 * c.cg + w) * 16), 0, 16); }
        };
        auto aux1 = [&](int f) {
            if (f < 4) { const int i = f; av[2 * i] = (blo(aw0[i]) + blo(aw1[i])) + (blo(aw2[i]) + blo(aw3[i])); av[2 * i + 1] = (bhi(aw0[i]) + bhi(aw1[i])) + (bhi(aw2[i]) + bhi(aw3[i])); PIN(av[2 * i]); PIN(av[2 * i + 1]); }
            if (f == 8) { swap32add(av[0], av[4]); swap32add(av[1], av[5]); swap32add(av[2], av[6]); swap32add(av[3], av[7]); PIN(av[0]); PIN(av[1]); PIN(av[2]); PIN(av[3]); }
            if (f == 9) { swap16add(av[0], av[2]); swap16add(av[1], av[3]); av[0] = dppadd<0x124>(av[0]); av[1] = dppadd<0x124>(av[1]); av[0] = dppadd<0x128>(av[0]); av[1] = dppadd<0x128>(av[1]); PIN(av[0]); PIN(av[1]); }
            if (f == 10) { const int rho = lane >> 4, col = 8 * (lane & 3) + 4 * (rho >> 1) + 2 * (rho & 1);
                const unsigned off = pvalid ? (unsigned)(WS_CS + (((c.cg * 9 + ptile) * 8 + (c.rb >> 1)) * 32 + col) * 8) : (unsigned)(WS_SCR + (blockIdx.x * 8 + w) * 256 + col * 8);
                if ((lane & 12) == 0) __builtin_amdgcn_raw_buffer_store_b128((u32x4){__float_as_uint(av[0]), tagLo, __float_as_uint(av[1]), tagLo}, wsr, off, 0, 16); }
        };
        u32x4 gs;
        const int gt1 = w < 3 ? w : w < 6 ? w - 3 : w - 6, gt2 = w < 6 ? 3 + w : w - 3;
        stage<G, 0, true, true, -1, -1>(st, [&](int f_) { if (f_ < 2) issue_x1(c, 2, f_); else if (f_ < 4) issue_x1(c, 3, f_ - 2); else if (f_ < 7) issue_w1<1>(c, f_ - 4); aux0(f_); }); SYNCV(8);
        stage<G, 4, true, false, -1, -1>(st, [&](int f_) { if (f_ < 3) issue_w1<5>(c, f_); else if (f_ < 5) issue_x1(c, 4, f_ - 3); else if (f_ < 7) issue_x1(c, 5, f_ - 5); aux1(f_); }); SYNCV(9);
        stage<G, 1, true, true, -1, -1>(st, [&](int f_) { if (f_ >= 1 && f_ < 4) issue_w1<2>(c, f_ - 1); }); SYNCV(4);
        stage<G, 5, true, false, 4, -1>(st, [&](int f_) { if (f_ < 3) issue_w1<6>(c, f_); else if (f_ < 5) issue_x1(c, 6, f_ - 3); else if (f_ < 7) issue_x1(c, 7, f_ - 5); }); SYNCV(7);
        stage<G, 2, true, false, -1, -1>(st, [&](int f_) { if (f_ >= 1 && f_ < 4) issue_w1<3>(c, f_ - 1);
            if (f_ == 8) { aload16_sc1(gs, P.ws, (unsigned)(WS_ST + (128 * c.rb + 32 * c.tm + (opaque(lane) & 31)) * 16)); GATHER_ISSUE(gt1); } }); SYNCV(6);
        stage<G, 6, true, false, 6, -1>(st, [&](int f_) { if (f_ >= 1 && f_ < 4) issue_w1<7>(c, f_ - 1); else if (f_ >= 4 && f_ < 7) issue_w1<8>(c, f_ - 4); }); SYNCV(9);
        stage<G, 3, true, false, -1, -1>(st, [&](int f_) { if (f_ >= 1 && f_ < 4) issue_w1<9>(c, f_ - 1);
            if (f_ == 10) { WAITV(9); PIN(gs);
                if (!__all(gs[1] == tagLo && gs[3] == tagHi)) { const unsigned gso = (unsigned)(WS_ST + (128 * c.rb + 32 * c.tm + (opaque(lane) & 31)) * 16);
                    for (unsigned spins = 0;; ++spins) { __builtin_amdgcn_s_sleep(2); gs = __builtin_amdgcn_raw_buffer_load_b128(wsr, gso, 0, 16); __builtin_amdgcn_s_waitcnt(0x0F70);
                        if (__all(gs[1] == tagLo && gs[3] == tagHi)) break; if (spins > SPIN_CAP) { set_timeout(ws, lane); break; } } }
                ln_m = __uint_as_float(gs[0]); ln_a = __uint_as_float(gs[2]); PIN(ln_m); PIN(ln_a);
                GATHER_CHECK(gt1); }
            if (f_ == 11) GATHER_RED(gt1); });
        { lp xp = c.lds + 13 * GR + w * 2048 + c.vlin; *(LAS f32x4*)(xp) = F4(st.qb, 8 - 8 * G); *(LAS f32x4*)(xp + 1024) = F4(st.qb, 12 - 8 * G);
          if constexpr (G == 1) { if (h == 0) { *(LAS float*)(c.lds + L_CSW + c.tm * 256 + r32 * 4) = ln_m; *(LAS float*)(c.lds + L_CSW + c.tm * 256 + 128 + r32 * 4) = ln_a; } } }
        SYNC(3);
        issue_w<10>(c);
        { const int lane = opaque(c.lane), h = lane >> 5, r32 = lane & 31;
          const unsigned o = (unsigned)(WS_KV + ((size_t)c.cg * 64 + T) * 4096) + lane * 16 + G * 2048;
          f32x16 y;
          if constexpr (G == 0) {
#pragma unroll
              for (int q = 0; q < 4; ++q) { const f32x4 cs = ldsf4((lcp)c.lds + L_CS + (8 * q + 4 * h) * 4);
#pragma unroll
                  for (int i = 0; i < 4; ++i) y[4 * q + i] = ln_a * __builtin_fmaf(-ln_m, cs[i], st.qa[4 * q + i]); }
          } else {
              const float cso = *(const LAS float*)(c.lds + L_CS + 128 + r32 * 4);
#pragma unroll
              for (int q = 0; q < 4; ++q) { const f32x4 mm = ldsf4((lcp)c.lds + L_CSW + c.tm * 256 + (8 * q + 4 * h) * 4), aa = ldsf4((lcp)c.lds + L_CSW + c.tm * 256 + 128 + (8 * q + 4 * h) * 4);
#pragma unroll
                  for (int i = 0; i < 4; ++i) y[4 * q + i] = aa[i] * __builtin_fmaf(-mm[i], cso, st.qa[4 * q + i]); }
          }
          const u32x4 k0 = pack8(y, 0), k1 = pack8(y, 8);
          __builtin_amdgcn_raw_buffer_store_b128(k0, wsr, o, 0, 16); __builtin_amdgcn_raw_buffer_store_b128(k1, wsr, o + 1024, 0, 16);
          if (w == 0 && lane == 0) store_flag(ws, WS_SF + (size_t)blockIdx.x * 8, tag);
          *(LAS u32x4*)(c.lds + L_KVR + c.tm * 4096 + G * 2048 + lane * 16) = k0; *(LAS u32x4*)(c.lds + L_KVR + c.tm * 4096 + G * 2048 + 1024 + lane * 16) = k1;
          SB();
          const f32x4 c0 = ldsf4((lcp)c.lds + L_CS + 2 * 128 + (16 * G + 4 * h) * 4), c1 = ldsf4((lcp)c.lds + L_CS + 2 * 128 + (16 * G + 8 + 4 * h) * 4);
          const lcp xp = (lcp)c.lds + 13 * GR + (w ^ 4) * 2048 + lane * 16;
          const f32x4 p0 = ldsf4(xp), p1 = ldsf4(xp + 1024);
          const float sc = ln_a * (0.25f * 1.4426950408889634f);
#pragma unroll
          for (int i = 0; i < 4; ++i) { y[i] = sc * __builtin_fmaf(-ln_m, c0[i], st.qb[8 * G + i] + p0[i]); y[4 + i] = sc * __builtin_fmaf(-ln_m, c1[i], st.qb[8 * G + 4 + i] + p1[i]); }
          qf = __builtin_bit_cast(bf16x8, pack8(y, 0)); PIN(qf); }
        stage<G, 7, true, false, -1, 8>(st, [&](int) {}); SYNC(5);
        { const int ln = opaque(lane); c.vd = (ln >> 3) * 1024 + (((ln & 7) ^ (((ln >> 4) & 1) << 2)) * 16); }
        stage<G, 8, false, false, -1, 9>(st, [&](int f_) { if (f_ >= 1 && f_ < 4) issue_w1<11>(c, f_ - 1); if (f_ == 4) issue_d(c, 0); }); SYNCV(6);
        stage<G, 9, false, false, -1, 10>(st, [&](int f_) { if (f_ >= 1 && f_ < 4) issue_d(c, f_);
            if (f_ == 5) { WAITV(7); if (lane == 0) store_flag(ws, WS_KVF + ((size_t)(c.cg * 64 + T) * 2 + G) * 8, tag); }
            if (f_ == 8) GATHER_ISSUE(gt2); }); SYNCV(7);
        u64 kf_;
        stage<G, 10, false, false, -1, 11>(st, [&](int f_) { if (f_ >= 1 && f_ < 4) issue_d(c, 3 + f_);
            if (f_ == 6) { WAITV(3); GATHER_CHECK(gt2); GATHER_RED(gt2); }
            if (f_ == 10) { const int tl = 4 * c.rb - 8 + w; aload_flag(kf_, P.ws, (unsigned)(WS_KVF + ((c.cg * 64 + (tl < 0 ? 0 : tl)) * 2 + (opaque(lane) & 1)) * 8)); } }); SYNCV(63);
        stage<G, 11, false, false, -1, -1>(st, [&](int f_) { if (f_ >= 1 && f_ < 3) issue_d(c, 6 + f_);
            if (f_ == 6) { WAITV(2); asm volatile("" : "+v"(kf_)); const int tl = 4 * c.rb - 8 + w;
                if (tl >= 0) {
                    if (!__all(kf_ == tag)) poll_flags((const u64*)(ws + WS_KVF) + (c.cg * 64 + tl) * 2, 2, tag, lane, ws);
                    const char* base = ws + WS_KV + ((size_t)c.cg * 64 + tl) * 4096; const unsigned dst = c.lds0 + L_KV + w * 4096;
                    dma16_sc1(c.vlin, base, dst); dma16_sc1(c.vlin, base + 1024, dst + 1024); dma16_sc1(c.vlin, base + 2048, dst + 2048); dma16_sc1(c.vlin, base + 3072, dst + 3072); } } });
        {
          lp xp = c.lds + 2 * GR + w * 4096 + c.vlin;
          SYNCV(63);
          *(LAS f32x4*)(xp) = F4(st.b0, 8 - 8 * G); *(LAS f32x4*)(xp + 1024) = F4(st.b0, 12 - 8 * G); *(LAS f32x4*)(xp + 2048) = F4(st.b1, 8 - 8 * G); *(LAS f32x4*)(xp + 3072) = F4(st.b1, 12 - 8 * G); }
        SYNC(63);
        {
          const int lane = opaque(c.lane), h = lane >> 5;
          const float a2 = ln_a * ln_a;
          { u32x4 y0, y1;
#pragma unroll
            for (int q = 0; q < 4; ++q) {
                const f32x4 cg_ = ldsf4((lcp)c.lds + L_CS + (3 + 4 * G) * 128 + (8 * q + 4 * h) * 4), cu_ = ldsf4((lcp)c.lds + L_CS + (4 + 4 * G) * 128 + (8 * q + 4 * h) * 4);
                float y[4];
#pragma unroll
                for (int i = 0; i < 4; ++i) { const float g_ = fmaxf(__builtin_fmaf(-ln_m, cg_[i], st.a0[4 * q + i]), 0.f), u_ = __builtin_fmaf(-ln_m, cu_[i], st.a1[4 * q + i]); y[i] = (g_ * a2) * u_; }
                const unsigned lo = pk2(y[0], y[1]), hi = pk2(y[2], y[3]);
                if (q < 2) { y0[2 * q] = lo; y0[2 * q + 1] = hi; } else { y1[2 * q - 4] = lo; y1[2 * q - 3] = hi; }
            }
            if (G == 0 ? v0 : v2) { const unsigned o_ = (unsigned)(WS_C + ((size_t)T * NKS + 2 * (fs + 2 * G)) * 1024) + lane * 16;
                __builtin_amdgcn_raw_buffer_store_b128(y0, wsr, o_, 0, 16); __builtin_amdgcn_raw_buffer_store_b128(y1, wsr, o_ + 1024, 0, 16); } }
          { const lcp xp = (lcp)c.lds + 2 * GR + (w ^ 4) * 4096 + lane * 16;
            u32x4 y0;
#pragma unroll
            for (int q = 0; q < 2; ++q) {
                const f32x4 pg = ldsf4(xp + q * 1024), pu = ldsf4(xp + 2048 + q * 1024);
                const f32x4 cg_ = ldsf4((lcp)c.lds + L_CS + 5 * 128 + (16 * G + 8 * q + 4 * h) * 4), cu_ = ldsf4((lcp)c.lds + L_CS + 6 * 128 + (16 * G + 8 * q + 4 * h) * 4);
                float y[4];
#pragma unroll
                for (int i = 0; i < 4; ++i) { const float g_ = fmaxf(__builtin_fmaf(-ln_m, cg_[i], st.b0[8 * G + 4 * q + i] + pg[i]), 0.f), u_ = __builtin_fmaf(-ln_m, cu_[i], st.b1[8 * G + 4 * q + i] + pu[i]); y[i] = (g_ * a2) * u_; }
                y0[2 * q] = pk2(y[0], y[1]); y0[2 * q + 1] = pk2(y[2], y[3]);
            }
            __builtin_amdgcn_raw_buffer_store_b128(y0, wsr, (unsigned)(WS_C + ((size_t)T * NKS + 2 * (fs + 1) + G) * 1024) + lane * 16, 0, 16); } }
        if (G == 0 ? v0 : v2) { SYNC(3); } else { SYNC(1); }
        if constexpr (G == 1) __builtin_amdgcn_s_setprio(1);
    };
    if (c.g == 0) gemm1(std::integral_constant<int, 0>{}); else gemm1(std::integral_constant<int, 1>{});
    const int lane2 = opaque(lane);
    const unsigned cbase = (unsigned)(WS_C + ((size_t)T2 * NKS + kq) * 1024) + lane2 * 16;
    bf16x8 hf[22];
    u64 hfl; bool hrdy_ = false;
#define LDH(i) do { const bool ok_ = ((i) < 21) || (kq < 2); hf[i] = __builtin_bit_cast(bf16x8, __builtin_amdgcn_raw_buffer_load_b128(wsr, ok_ ? cbase + (i) * 4096 : cbase, 0, 16)); } while (0)
    {
        const int lane = lane2, h = lane >> 5, r32 = lane & 31;
        const int i0 = T >= 8 ? 0 : 8 - T;
        const unsigned kb = c.lds0 + L_KV + c.tm * 4096 + c.g * 1024 + lane * 16;
        const unsigned lanebit = r32 < 16 ? 1u : 0u;
        const unsigned vb = c.lds0 + (r32 < 16 ? (unsigned)(((r32 & 15) + 16 * c.g + 32 * h) * 16) : (unsigned)L_ONES);
        auto koff = [&](int i) -> unsigned { return (unsigned)((i >= i0 ? i : 7) * 4096); };
        auto rdk = [&](int i) -> bf16x8 { return ldsf((lcp)(uintptr_t)(kb + koff(i))); };
        auto vaddr = [&](int i) -> unsigned { const bool valid = i >= i0; const unsigned d = valid ? 0u : (unsigned)(L_ZERO - L_ONES), A = valid ? (unsigned)(L_KV + 2048 + (c.tm + i) * 4096) : (unsigned)L_ZERO;
                                              return lanebit * (A - d) + (vb + d); };
        auto dmask = [&](int r) -> bool { u64 mk = 0;
#pragma unroll
            for (int l = 0; l < 64; ++l) { const int kl = (r & 3) + 8 * (r >> 2) + 4 * (l >> 5); if (kl <= (l & 31)) mk |= 1ull << l; }
            return __builtin_amdgcn_inverse_ballot_w64(mk); };
        const f32x16 zero = f32x16{};
        float m0 = -1e30f, m1 = -1e30f;
#define MAXT(S) do { _Pragma("unroll") for (int r = 0; r < 16; r += 4) { m0 = fmaxf(fmaxf(m0, S[r]), S[r + 1]); m1 = fmaxf(fmaxf(m1, S[r + 2]), S[r + 3]); } PIN(m0); PIN(m1); } while (0)
#define EXPT(S) do { _Pragma("unroll") for (int r = 0; r < 16; ++r) S[r] = __builtin_amdgcn_exp2f(S[r]); } while (0)
#define H_PUBLISH(N) do { WAITV(N); if (lane == 0) store_flag(ws, WS_HF + ((size_t)T * 32 + 2 * c.cg + c.g) * 8, tag); } while (0)
#define H_FLAGS() aload_flag(hfl, P.ws, (unsigned)(WS_HF + (T2 * 32 + (lane & 31)) * 8))
        f32x16 o = f32x16{};
        float m; bool hrdy = false;
        if (T == 0) {
            f32x16 s = mfma(ldsf((lcp)(uintptr_t)(kb + 8 * 4096)), qf, zero);
#pragma unroll
            for (int r = 0; r < 16; ++r) s[r] = dmask(r) ? s[r] : -1e30f;
            MAXT(s);
            m = fmaxf(m0, m1);
            { auto rr = __builtin_amdgcn_permlane32_swap(__float_as_uint(m), __float_as_uint(m), false, false); m = fmaxf(__uint_as_float(rr[0]), __uint_as_float(rr[1])); }
#pragma unroll
            for (int r = 0; r < 16; ++r) s[r] = __builtin_amdgcn_exp2f(s[r] - m);
            const unsigned va = vaddr(8);
            o = mfma(ldsf((lcp)(uintptr_t)va), __builtin_bit_cast(bf16x8, pack8(s, 0)), o);
            o = mfma(ldsf((lcp)(uintptr_t)(va + 1024)), __builtin_bit_cast(bf16x8, pack8(s, 8)), o);
            issue_d(c, 9); issue_d(c, 10); issue_d(c, 11); issue_d(c, 12);
            H_PUBLISH(4);
        } else {
            bf16x8 k0 = rdk(0), k8 = rdk(8), k1 = rdk(1), k2 = rdk(2), k3 = rdk(3);
            f32x16 sA, sB, sC;
            sA = mfma_pin0(k0, qf); sB = mfma_pin0(k8, qf); SB();
            k0 = rdk(4); k8 = rdk(5); sC = mfma_pin0(k1, qf); k1 = rdk(6); SB();
            asm volatile("s_nop 7\n\ts_nop 3"); SB();
#pragma unroll
            for (int r = 0; r < 16; ++r) sA[r] = dmask(r) ? sB[r] : sA[r];
            PIN(sA); SB();
            sB = mfma_pin0(k2, qf); k2 = rdk(7); SB();
            MAXT(sA); SB();
            H_PUBLISH(0);
            sA = mfma_pin0(k3, qf); issue_d(c, 9); SB(); MAXT(sC); SB();
            sC = mfma_pin0(k0, qf); issue_d(c, 10); SB(); MAXT(sB); SB();
            sB = mfma_pin0(k8, qf); issue_d(c, 11); SB(); MAXT(sA); SB();
            sA = mfma_pin0(k1, qf); issue_d(c, 12); SB(); MAXT(sC); SB();
            sC = mfma_pin0(k2, qf); k0 = rdk(0); k8 = rdk(8); k1 = rdk(1); SB(); MAXT(sB); SB();
            MAXT(sA); SB(); MAXT(sC);
            m = fmaxf(m0, m1);
            { auto rr = __builtin_amdgcn_permlane32_swap(__float_as_uint(m), __float_as_uint(m), false, false); m = fmaxf(__uint_as_float(rr[0]), __uint_as_float(rr[1])); }
            f32x16 cm;
#pragma unroll
            for (int r = 0; r < 16; ++r) cm[r] = -m;
            PIN(cm);
            asm volatile("s_nop 1"); sA = mfma_pin(k0, qf, cm); sB = mfma_pin(k8, qf, cm); sC = mfma_pin(k1, qf, cm); SB();
            bf16x8 va0, va1, vb0, vb1;
            { const unsigned a0 = vaddr(0), a8 = vaddr(8); va0 = ldsf((lcp)(uintptr_t)a0); va1 = ldsf((lcp)(uintptr_t)(a0 + 1024)); vb0 = ldsf((lcp)(uintptr_t)a8); vb1 = ldsf((lcp)(uintptr_t)(a8 + 1024)); }
            k0 = rdk(2);
            unsigned mp[8];
#pragma unroll
            for (int j = 0; j < 8; ++j) mp[j] = (dmask(2 * j) ? 0xffffu : 0u) | (dmask(2 * j + 1) ? 0xffff0000u : 0u);
            SB(); asm volatile("s_nop 7"); SB();
#pragma unroll
            for (int r = 0; r < 16; ++r) sA[r] = dmask(r) ? sB[r] : sA[r];
            EXPT(sA);
            { const u32x4 p0 = pack8(sA, 0), p1 = pack8(sA, 8); u32x4 d0, d1, o0, o1;
#pragma unroll
              for (int j = 0; j < 4; ++j) { d0[j] = p0[j] & mp[j]; d1[j] = p1[j] & mp[4 + j]; o0[j] = p0[j] & ~mp[j]; o1[j] = p1[j] & ~mp[4 + j]; }
              o = mfma(va0, __builtin_bit_cast(bf16x8, o0), o); o = mfma(va1, __builtin_bit_cast(bf16x8, o1), o);
              o = mfma(vb0, __builtin_bit_cast(bf16x8, d0), o); o = mfma(vb1, __builtin_bit_cast(bf16x8, d1), o); }
            { const unsigned a1 = vaddr(1), a2 = vaddr(2); va0 = ldsf((lcp)(uintptr_t)a1); va1 = ldsf((lcp)(uintptr_t)(a1 + 1024)); vb0 = ldsf((lcp)(uintptr_t)a2); vb1 = ldsf((lcp)(uintptr_t)(a2 + 1024)); }
            SB();
#define EXP8(S, b) do { _Pragma("unroll") for (int r = (b); r < (b) + 8; ++r) S[r] = __builtin_amdgcn_exp2f(S[r]); } while (0)
#define PK4(S, b) (u32x4){pk2(S[b], S[(b) + 1]), pk2(S[(b) + 2], S[(b) + 3]), pk2(S[(b) + 4], S[(b) + 5]), pk2(S[(b) + 6], S[(b) + 7])}
#define TILE2(i, S, V0, V1, SN) do { \
                if ((i) + 1 <= 7) { SN = mfma_pin(k0, qf, cm); SB(); if ((i) + 2 <= 7) k0 = rdk((i) + 2); } \
                EXP8(S, 0); { u32x4 p_ = PK4(S, 0); PIN(p_); SB(); o = mfma(V0, __builtin_bit_cast(bf16x8, p_), o); SB(); } \
                EXP8(S, 8); { u32x4 p_ = PK4(S, 8); PIN(p_); SB(); o = mfma(V1, __builtin_bit_cast(bf16x8, p_), o); SB(); } \
                if ((i) + 2 <= 7) { const unsigned a_ = vaddr((i) + 2); V0 = ldsf((lcp)(uintptr_t)a_); V1 = ldsf((lcp)(uintptr_t)(a_ + 1024)); } SB(); } while (0)
            TILE2(1, sC, va0, va1, sB); TILE2(2, sB, vb0, vb1, sC);
            H_FLAGS();
            TILE2(3, sC, va0, va1, sB); TILE2(4, sB, vb0, vb1, sC);
            WAITV(0); asm volatile("" : "+v"(hfl));
            hrdy = __all(hfl == tag);
            if (hrdy) { LDH(0); LDH(1); LDH(2); LDH(3); LDH(4); LDH(5); LDH(6); }
            TILE2(5, sC, va0, va1, sB);
            if (hrdy) { LDH(7); LDH(8); LDH(9); LDH(10); LDH(11); LDH(12); LDH(13); }
            TILE2(6, sB, vb0, vb1, sC);
            if (hrdy) { LDH(14); LDH(15); LDH(16); LDH(17); LDH(18); LDH(19); LDH(20); LDH(21); }
            TILE2(7, sC, va0, va1, sB);
        }
        const float inv = __builtin_amdgcn_rcpf(o[8]);
        f32x16 y;
#pragma unroll
        for (int r = 0; r < 8; ++r) y[r] = o[r] * inv;
        const unsigned oo = (unsigned)(WS_C + ((size_t)T * NKS + 86 + 2 * c.cg + c.g) * 1024) + lane * 16;
        const u32x4 ov = pack8(y, 0);
        __builtin_amdgcn_raw_buffer_store_b128(ov, wsr, oo, 0, 16);
        if (!hrdy) {
            poll_flags((const u64*)(ws + WS_HF) + T2 * 32, 32, tag, lane, ws);
#pragma unroll
            for (int i = 0; i < 22; ++i) LDH(i);
        }
        hrdy_ = hrdy;
        __builtin_amdgcn_s_waitcnt(0x0F71);
        __builtin_amdgcn_s_setprio(0);
    }
    {
        const int lane = opaque(lane2), h = lane >> 5, r32 = lane & 31, q4 = (lane & 15) >> 2, p4 = lane & 3, b16 = (lane >> 4) & 1;
        f32x16 acc0 = f32x16{}, acc1 = f32x16{};
        const unsigned wl = c.lds0 + kq * 2048 + (4 * h + q4) * 128 + 32 * b16 + 8 * p4, swz = ((q4 >> 1) & 1) << 6;
        const unsigned wl0 = wl + (0u ^ swz), wl1 = wl + (64u ^ swz);
        bf16x8 wa[3], wb_[3];
#define RDW(u) do { wa[(u) % 3] = trfrag<1024>((lcp)(uintptr_t)(wl0 + dloc(u))); wb_[(u) % 3] = trfrag<1024>((lcp)(uintptr_t)(wl1 + dloc(u))); } while (0)
#define G2STEP(u, frag, nxt) do { acc0 = mfma(wa[(u) % 3], frag, acc0); acc1 = mfma(wb_[(u) % 3], frag, acc1); SB(); if ((nxt) >= 0) RDW((nxt) < 0 ? 0 : (nxt)); SB(); } while (0)
        SYNC(63);
        const bool ew = blockIdx.x == 0 && w == 7; u32x4 sf0 = {0u, 0u, 0u, 0u}, sf1 = sf0; bool edone = false;
        if (ew) { aload16_sc1(sf0, P.ws + WS_SF, (unsigned)(lane * 32)); aload16_sc1(sf1, P.ws + WS_SF + 16, (unsigned)(lane * 32)); }
        RDW(0); RDW(1); RDW(2);
        G2STEP(0, hf[0], 3);
        issue_d(c, 13); G2STEP(1, hf[1], 4); issue_d(c, 14); G2STEP(2, hf[2], 5); issue_d(c, 15); G2STEP(3, hf[3], 6); issue_d(c, 16); G2STEP(4, hf[4], 7); issue_d(c, 17); G2STEP(5, hf[5], 8); issue_d(c, 18);
        if (hrdy_) { WAITV(6); }
        if (lane == 0) store_flag(ws, WS_OF + ((size_t)T * 32 + 2 * c.cg + c.g) * 8, tag);
        SYNCV(63);
        G2STEP(6, hf[6], 9); issue_d(c, 19); G2STEP(7, hf[7], 10); issue_d(c, 20); G2STEP(8, hf[8], 11); issue_d(c, 21); G2STEP(9, hf[9], 12); issue_d(c, 22);
        G2STEP(10, hf[10], -1); issue_d(c, 23); G2STEP(11, hf[11], -1); issue_d(c, 24); G2STEP(12, hf[12], -1);
        SYNCV(7);
        if (ew) { asm volatile("" : "+v"(sf0), "+v"(sf1));
            edone = __all(sf0[0] == tagLo && sf0[1] == tagHi && sf0[2] == tagLo && sf0[3] == tagHi && sf1[0] == tagLo && sf1[1] == tagHi && sf1[2] == tagLo && sf1[3] == tagHi);
            if (edone && lane == 0) store_flag(ws, WS_EPOCH, tag); }
        edone_ = edone;
        RDW(13); RDW(14); RDW(15);
        u32x4 xres;
        u64 ofl;
        issue_d(c, 25); G2STEP(13, hf[13], 16); issue_d(c, 26); G2STEP(14, hf[14], 17); issue_d(c, 27); G2STEP(15, hf[15], 18); issue_d(c, 28);
        aload_flag(ofl, P.ws, (unsigned)(WS_OF + (T2 * 32 + (lane & 31)) * 8));
        G2STEP(16, hf[16], -1); issue_d(c, 29);
        xres = *(const u32x4*)(P.x + (size_t)(64 * c.tb + 8 * w + (lane >> 3)) * 512 + 64 * c.nb + (lane & 7) * 8);
        G2STEP(17, hf[17], -1); G2STEP(18, hf[18], -1);
        WAITV(2); asm volatile("" : "+v"(ofl));
        if (!__all(ofl == tag)) poll_flags((const u64*)(ws + WS_OF) + T2 * 32, 32, tag, lane, ws);
        bf16x8 of[8];
#pragma unroll
        for (int i = 0; i < 8; ++i) of[i] = __builtin_bit_cast(bf16x8, __builtin_amdgcn_raw_buffer_load_b128(wsr, cbase + (86 + 4 * i) * 1024, 0, 16));
        SYNCV(63);
        RDW(19); RDW(20); RDW(21);
        G2STEP(19, hf[19], 22); G2STEP(20, hf[20], 23);
        if (kq < 2) { acc0 = mfma(wa[21 % 3], hf[21], acc0); acc1 = mfma(wb_[21 % 3], hf[21], acc1); }
        SB(); RDW(24); SB();
        G2STEP(22, of[0], -1); G2STEP(23, of[1], -1); G2STEP(24, of[2], -1);
        SYNCV(63);
        RDW(25); RDW(26); RDW(27);
        G2STEP(25, of[3], 28); G2STEP(26, of[4], 29); G2STEP(27, of[5], -1); G2STEP(28, of[6], -1); G2STEP(29, of[7], -1);
        { lp rbp = c.lds + (kq < 2 ? 4 * GR : 13 * GR - 32768) + (kq * 2 + th) * 8192;
#pragma unroll
          for (int q = 0; q < 4; ++q) { *(LAS f32x4*)(rbp + q * 1024 + ((lane + 2 * q) & 63) * 16) = F4(acc0, 4 * q); *(LAS f32x4*)(rbp + 4096 + q * 1024 + ((lane + 2 * (4 + q)) & 63) * 16) = F4(acc1, 4 * q); } }
        SYNC(63);
        {
          const int t = 8 * w + (lane >> 3), cc = lane & 7;
          const unsigned a0 = (unsigned)((t >> 5) * 8192 + cc * 1024), s0i = (unsigned)(((t & 31) + 2 * cc) & 63) * 16, s1i = (unsigned)(((t & 31) + 32 + 2 * cc) & 63) * 16;
          f32x4 s0 = {0.f, 0.f, 0.f, 0.f}, s1 = s0;
#pragma unroll
          for (int k = 0; k < 4; ++k) { const lcp bp = (lcp)c.lds + (k < 2 ? 4 * GR : 13 * GR - 32768) + k * 16384 + a0; s0 += ldsf4(bp + s0i); s1 += ldsf4(bp + s1i); }
          u32x4 ov;
          ov[0] = pk2(s0[0] + blo(xres[0]), s0[1] + bhi(xres[0])); ov[1] = pk2(s0[2] + blo(xres[1]), s0[3] + bhi(xres[1]));
          ov[2] = pk2(s1[0] + blo(xres[2]), s1[1] + bhi(xres[2])); ov[3] = pk2(s1[2] + blo(xres[3]), s1[3] + bhi(xres[3]));
          *(u32x4*)(P.out + (size_t)(64 * c.tb + t) * 512 + 64 * c.nb + cc * 8) = ov; }
    }
    if (blockIdx.x == 0 && w == 7 && !edone_) {
        const GAS u64* pp = (const GAS u64*)(ws + WS_SF) + lane * 4;
        for (unsigned spins = 0;; ++spins) { bool ok = true;
#pragma unroll
            for (int j = 0; j < 4; ++j) ok &= __hip_atomic_load(pp + j, __ATOMIC_RELAXED, __HIP_MEMORY_SCOPE_AGENT) == tag;
            if (__all(ok)) break; if (spins > SPIN_CAP) { set_timeout(ws, lane); break; } __builtin_amdgcn_s_sleep(2); }
        if (lane == 0) store_flag(ws, WS_EPOCH, tag);
    }
}

extern "C" void kernel_launch(void* const* d_in, const int* in_sizes, int n_in, void* d_out, int out_size, void* d_ws, size_t ws_size, hipStream_t stream) {
    Params p;
    p.x = (const u16*)d_in[0]; p.wqkv = (const u16*)d_in[1]; p.wo = (const u16*)d_in[2];
    p.wg = (const u16*)d_in[4]; p.wu = (const u16*)d_in[5]; p.wd = (const u16*)d_in[6];
    p.out = (u16*)d_out; p.ws = (char*)d_ws;
    (void)hipFuncSetAttribute((const void*)fused_kernel, hipFuncAttributeMaxDynamicSharedMemorySize, LDS_BYTES);
    fused_kernel<<<256, 512, LDS_BYTES, stream>>>(p);
}
```

```cpp
#include <hip/hip_runtime.h>
#include <stdint.h>
#include <type_traits>
#define DSEL 1
#define DTHR 150.f

#define FI __device__ __forceinline__
#define LAS __attribute__((address_space(3)))
#define GAS __attribute__((address_space(1)))
typedef unsigned short u16;
typedef unsigned long long u64;
typedef short bf16x8 __attribute__((ext_vector_type(8)));
typedef short s16x4 __attribute__((ext_vector_type(4)));
typedef float f32x16 __attribute__((ext_vector_type(16)));
typedef float f32x4 __attribute__((ext_vector_type(4)));
typedef float f32x2 __attribute__((ext_vector_type(2)));
typedef unsigned u32x4 __attribute__((ext_vector_type(4)));
typedef __bf16 bf16x2_t __attribute__((ext_vector_type(2)));
typedef LAS const char* lcp;
typedef LAS char* lp;

constexpr int GR = 8192;
constexpr int L_KV = 13 * GR;
constexpr int L_KVR = 17 * GR;
constexpr int L_MISC = 19 * GR;
constexpr int L_CS = L_MISC;
constexpr int L_CSW = L_MISC + 2048;
constexpr int L_ZERO = L_MISC + 4096;
constexpr int L_ONES = L_MISC + 6144;
constexpr int LDS_BYTES = 163840;

constexpr size_t WS_EPOCH = 0, WS_TMO = 64;
constexpr size_t WS_SF = 4096;
constexpr size_t WS_KVF = 8192;
constexpr size_t WS_HF = 24576;
constexpr size_t WS_OF = 40960;
constexpr size_t WS_ST = 65536;
constexpr size_t WS_CS = 131072;
constexpr size_t WS_SCR = 768u << 10;
constexpr size_t WS_KV = 2u << 20;
constexpr size_t WS_C = 8u << 20;
constexpr int NKS = 118;

FI f32x16 mfma(bf16x8 a, bf16x8 b, f32x16 c) { return __builtin_amdgcn_mfma_f32_32x32x16_bf16(a, b, c, 0, 0, 0); }
FI f32x16 mfma_pin(bf16x8 a, bf16x8 b, const f32x16& c) { f32x16 d; asm volatile("v_mfma_f32_32x32x16_bf16 %0, %1, %2, %3" : "=&v"(d) : "v"(a), "v"(b), "v"(c)); return d; }
FI f32x16 mfma_pin0(bf16x8 a, bf16x8 b) { f32x16 d; asm volatile("v_mfma_f32_32x32x16_bf16 %0, %1, %2, 0" : "=&v"(d) : "v"(a), "v"(b)); return d; }
FI unsigned pk2(float lo, float hi) { f32x2 v = {lo, hi}; bf16x2_t r = __builtin_convertvector(v, bf16x2_t); return __builtin_bit_cast(unsigned, r); }
FI u32x4 pack8(const f32x16& y, int b) { u32x4 r; r[0] = pk2(y[b], y[b + 1]); r[1] = pk2(y[b + 2], y[b + 3]); r[2] = pk2(y[b + 4], y[b + 5]); r[3] = pk2(y[b + 6], y[b + 7]); return r; }
typedef short v4i16_t __attribute__((ext_vector_type(4)));
FI s16x4 trd(lcp p) { return __builtin_bit_cast(s16x4, __builtin_amdgcn_ds_read_tr16_b64_v4i16((LAS v4i16_t*)p)); }
template <int D2> FI bf16x8 trfrag(lcp p) { s16x4 lo = trd(p), hi = trd(p + D2); return __builtin_shufflevector(lo, hi, 0, 1, 2, 3, 4, 5, 6, 7); }
FI bf16x8 ldsf(lcp p) { return *(const LAS bf16x8*)p; }
FI f32x4 ldsf4(lcp p) { return *(const LAS f32x4*)p; }

FI void dma16(unsigned voff, const void* sbase, unsigned dst) {
    asm volatile("s_mov_b32 m0, %2\n\ts_nop 0\n\tglobal_load_lds_dwordx4 %0, %1" :: "v"(voff), "s"(sbase), "s"(__builtin_amdgcn_readfirstlane(dst)) : "memory", "m0");
}
FI void dma16_sc1(unsigned voff, const void* sbase, unsigned dst) {
    asm volatile("s_mov_b32 m0, %2\n\ts_nop 0\n\tglobal_load_lds_dwordx4 %0, %1 sc1" :: "v"(voff), "s"(sbase), "s"(__builtin_amdgcn_readfirstlane(dst)) : "memory", "m0");
}
FI void aload16(u32x4& v, const void* base, unsigned voff) { asm volatile("global_load_dwordx4 %0, %1, %2" : "=v"(v) : "v"(voff), "s"(base) : "memory"); }
FI void aload16_sc1(u32x4& v, const void* base, unsigned voff) { asm volatile("global_load_dwordx4 %0, %1, %2 sc1" : "=v"(v) : "v"(voff), "s"(base) : "memory"); }
FI void aload_flag(u64& v, const char* base, unsigned voff) { asm volatile("global_load_dwordx2 %0, %1, %2 sc1" : "=v"(v) : "v"(voff), "s"(base) : "memory"); }

#define SB() __builtin_amdgcn_sched_barrier(0)
#define PIN(x) asm volatile("" : "+v"(x))
FI int opaque(int v) { asm volatile("" : "+v"(v)); return v; }
#define SYNC(N) do { SB(); asm volatile("s_waitcnt vmcnt(" #N ") lgkmcnt(0)\n\ts_barrier" ::: "memory"); SB(); } while (0)
#define SYNCV(N) do { SB(); asm volatile("s_waitcnt vmcnt(" #N ")\n\ts_barrier" ::: "memory"); SB(); } while (0)
#define WAITV(N) do { SB(); asm volatile("s_waitcnt vmcnt(" #N ")" ::: "memory"); SB(); } while (0)

struct Params {
    const u16* x; const u16* wqkv; const u16* wo; const u16* wg; const u16* wu; const u16* wd; u16* out; char* ws;
};

constexpr int wslot(int s) { return (s == 0 ? 4 : s == 1 ? 14 : s == 2 ? 7 : s == 3 ? 0 : s == 4 ? 7 : s == 5 ? 0 : s == 6 ? 10 : s == 7 ? 3 : s == 8 ? 6 : s == 9 ? 9 : s == 10 ? 0 : 3) * GR; }
constexpr int xslot(int c) { return (c == 0 ? 0 : c == 1 ? 2 : c == 2 ? 10 : c == 3 ? 12 : c == 4 ? 3 : c == 5 ? 5 : c == 6 ? 13 : 15) * GR; }
FI constexpr int dloc0(int u) { return (u == 0 ? 12 : u < 4 ? 6 + (u - 1) : u < 7 ? 9 + (u - 4) : u < 9 ? u - 7 : 2 + (u - 9)) * GR; }
FI constexpr int dloc(int u) { return u < 13 ? dloc0(u) : u < 19 ? L_KV + (u - 13) * GR : dloc0(u - 19); }

struct Ctx {
    const Params* p;
    unsigned lds0;
    lp lds;
    int lane, w, tm, g, rb, cg, tb, nb;
    unsigned vq, vf0, vf1, vf2, vx, vlin, vd;
    const char *px, *pq, *pf, *pwd, *pwo;
};

FI void issue_x1(const Ctx& c, int chunk, int k) {
    dma16(k ? c.vx ^ 64u : c.vx, c.px + chunk * 128 + k * 8 * 1024, c.lds0 + xslot(chunk) + 16 * c.w * 128 + k * 8 * 128);
}
FI void issue_x(const Ctx& c, int chunk) { issue_x1(c, chunk, 0); issue_x1(c, chunk, 1); }
template <int S> FI void issue_w1(const Ctx& c, int k) {
    if constexpr (S >= 4) {
        const char* base = c.pf + (size_t)(S - 4) * (64 * 2752);
        dma16(k == 0 ? c.vf0 : k == 1 ? c.vf1 : c.vf2, base, c.lds0 + wslot(S) + c.g * 12288 + c.tm * 3072 + k * 1024);
    } else {
        const char* base = c.pq + (size_t)S * (128 * 3072);
        dma16(c.vq, base + (k == 0 ? 1024 : k == 1 ? 2048 : 0), c.lds0 + wslot(S) + c.w * 1024 + k * 8192);
    }
}
template <int S> FI void issue_w(const Ctx& c) { issue_w1<S>(c, 0); issue_w1<S>(c, 1); issue_w1<S>(c, 2); }
FI void issue_d(const Ctx& c, int u) {
    const char* base;
    if (u < 21) base = c.pwd + (size_t)u * 65536;
    else if (u == 21) base = c.pwd + (size_t)21 * 65536 - (c.w >= 4 ? 32768 : 0);
    else base = c.pwo + (size_t)(u - 22) * 65536;
    dma16(c.vd, base, c.lds0 + dloc(u) + c.w * 1024);
}

FI float blo(unsigned d) { return __uint_as_float(d << 16); }
FI float bhi(unsigned d) { return __uint_as_float(d & 0xffff0000u); }
template <int CTRL> FI float dppadd(float x) { return x + __builtin_bit_cast(float, __builtin_amdgcn_update_dpp(0, __builtin_bit_cast(int, x), CTRL, 0xf, 0xf, true)); }
FI void swap32add(float& u, float v) { auto rr = __builtin_amdgcn_permlane32_swap(__float_as_uint(u), __float_as_uint(v), false, false); u = __uint_as_float(rr[0]) + __uint_as_float(rr[1]); }
FI void swap16add(float& u, float v) { auto rr = __builtin_amdgcn_permlane16_swap(__float_as_uint(u), __float_as_uint(v), false, false); u = __uint_as_float(rr[0]) + __uint_as_float(rr[1]); }

struct G1State {
    bf16x8 xf[32];
    bf16x8 wq[4];
    f32x16 a0, a1, b0, b1;
    f32x16 qa, qb;
    unsigned wbf[4], wbq[4];
    unsigned xb[4];
};
constexpr int fkind(int G, int f) { return G == 0 ? (f < 8 ? (f & 3) : (f & 1)) : (f < 4 ? (f & 1) : f < 6 ? (f & 1) : f < 8 ? 2 + (f & 1) : f < 10 ? (f & 1) : 2 + (f & 1)); }
constexpr int fpos(int G, int f) { return G == 0 ? (f < 8 ? (f >> 2) : 2 + ((f - 8) >> 1)) : (f < 4 ? (f >> 1) : f < 8 ? 2 : 3); }
constexpr int qkind(int G, int f) { return G == 0 ? ((f == 1 || f == 3 || f == 7 || f == 9) ? 1 : 0) : ((f == 3 || f == 5 || f == 9 || f == 11) ? 1 : 0); }
constexpr int qpos(int G, int f) {
    return G == 0 ? (f == 0 ? 0 : f == 1 ? 0 : f == 2 ? 1 : f == 3 ? 1 : f == 4 ? 2 : f == 5 ? 3 : f == 6 ? 4 : f == 7 ? 4 : f == 8 ? 5 : f == 9 ? 5 : f == 10 ? 6 : 7)
                  : (f == 0 ? 0 : f == 1 ? 1 : f == 2 ? 2 : f == 3 ? 2 : f == 4 ? 3 : f == 5 ? 3 : f == 6 ? 4 : f == 7 ? 5 : f == 8 ? 6 : f == 9 ? 6 : f == 10 ? 7 : 7);
}
template <int G, int S> FI bf16x8 rdfrag(const G1State& st, int f) {
    if constexpr (S >= 4) { const int k = fkind(G, f); return trfrag<768>((lcp)(uintptr_t)(st.wbf[fpos(G, f)] + (k & 1) * 12288 + (k >= 2 ? 64 : (G == 0 ? 0 : 128)))); }
    else { const int e = qpos(G, f); return trfrag<256>((lcp)(uintptr_t)(st.wbq[e & 3] + (e >> 2) * 4096 + (qkind(G, f) ? 16384 : G * 8192))); }
}
template <int C> FI void rdx(G1State& st) {
#pragma unroll
    for (int e = 0; e < 4; ++e) { st.xf[4 * C + e] = ldsf((lcp)(uintptr_t)st.xb[e]); if (C < 7) st.xb[e] = (unsigned)opaque((int)(st.xb[e] + (xslot(C + 1) - xslot(C)))); }
}
template <int G, int S, bool FRESH, bool XF, int XP, int NX, class F>
FI void stage(G1State& st, F&& hook) {
    if constexpr (XF) rdx<(XF ? 2 * S : 0)>(st);
    if constexpr (FRESH) {
#pragma unroll
        for (int f = 0; f < 4; ++f) st.wq[f] = rdfrag<G, S>(st, f);
    }
    if constexpr (XF) rdx<(XF ? 2 * S + 1 : 0)>(st);
    SB();
#pragma unroll
    for (int f = 0; f < 12; ++f) {
        const bf16x8 w = st.wq[f & 3];
        if constexpr (S >= 4) {
            const int k = fkind(G, f); const bf16x8 x = st.xf[4 * (S - 4) + fpos(G, f)];
            if (k == 0) st.a0 = mfma(w, x, st.a0); else if (k == 1) st.a1 = mfma(w, x, st.a1); else if (k == 2) st.b0 = mfma(w, x, st.b0); else st.b1 = mfma(w, x, st.b1);
        } else {
            const bf16x8 x = st.xf[8 * S + qpos(G, f)];
            if (qkind(G, f)) st.qb = mfma(w, x, st.qb); else if (G == 0) st.qa = mfma(w, x, st.qa); else st.qa = mfma(x, w, st.qa);
        }
        SB();
        if (f < 8) st.wq[f & 3] = rdfrag<G, S>(st, f + 4);
        else {
            if (f == 8) {
                if constexpr (S < 3) { _Pragma("unroll") for (int j = 0; j < 4; ++j) st.wbq[j] = (unsigned)opaque((int)(st.wbq[j] + (wslot(S + 1) - wslot(S)))); }
                if constexpr (S >= 4 && S < 11) { _Pragma("unroll") for (int j = 0; j < 4; ++j) st.wbf[j] = (unsigned)opaque((int)(st.wbf[j] + (wslot(S + 1) - wslot(S)))); }
            }
            if constexpr (NX >= 0) st.wq[f & 3] = rdfrag<G, (NX >= 0 ? NX : 0)>(st, f - 8);
        }
        if constexpr (XP >= 0) { if (f == 2) rdx<(XP >= 0 ? XP : 0)>(st); if (f == 6) rdx<(XP >= 0 ? XP + 1 : 0)>(st); }
        hook(f);
        SB();
    }
}

FI u64 mix64(u64 z) { z += 0x9E3779B97F4A7C15ull; z = (z ^ (z >> 30)) * 0xBF58476D1CE4E5B9ull; z = (z ^ (z >> 27)) * 0x94D049BB133111EBull; return z ^ (z >> 31); }

constexpr unsigned SPIN_CAP = 1u << 18;
FI void set_timeout(char* ws, int lane) { if (lane == 0) __hip_atomic_store((GAS unsigned*)(ws + WS_TMO), 1u, __ATOMIC_RELAXED, __HIP_MEMORY_SCOPE_AGENT); }
FI void poll_flags(const u64* flags, int n, u64 tag, int lane, char* ws) {
    const GAS u64* pp = (const GAS u64*)flags + (lane < n ? lane : 0);
    for (unsigned spins = 0;; ++spins) {
        const u64 v = __hip_atomic_load(pp, __ATOMIC_RELAXED, __HIP_MEMORY_SCOPE_AGENT);
        if (__all(v == tag)) return;
        if (spins > SPIN_CAP) { set_timeout(ws, lane); return; }
        __builtin_amdgcn_s_sleep(2);
    }
}
FI void store_flag(char* ws, size_t off, u64 tag) { __hip_atomic_store((GAS u64*)(ws + off), tag, __ATOMIC_RELAXED, __HIP_MEMORY_SCOPE_AGENT); }

__global__ __launch_bounds__(512, 2) void fused_kernel(Params P) {
    extern __shared__ __attribute__((aligned(16))) char smem[];
    Ctx c; c.p = &P;
    const int tid = threadIdx.x; c.lane = tid & 63;
    const int w = __builtin_amdgcn_readfirstlane(tid >> 6);
    c.w = w; c.tm = w & 3; c.g = w >> 2;
    c.lds = (lp)smem; c.lds0 = (unsigned)(uintptr_t)c.lds;
    const int xcd = blockIdx.x & 7, idx = blockIdx.x >> 3;
    c.rb = 4 * (xcd >> 1) + (idx >> 3); c.cg = 8 * (xcd & 1) + (idx & 7);
    c.tb = 4 * xcd + (idx >> 3); c.nb = idx & 7;
    const int fs = c.cg < 11 ? 3 * c.cg : c.cg < 15 ? 33 + 2 * (c.cg - 11) : 40;
    const bool v0 = c.cg != 15, v2 = c.cg < 11 || c.cg == 15;
    const int lane = c.lane, h = lane >> 5, r32 = lane & 31;
    char* ws = P.ws;
    if (threadIdx.x == 0) __hip_atomic_store((GAS u64*)(P.ws + (32u << 20) + blockIdx.x * 8), (u64)__builtin_amdgcn_s_memrealtime(), __ATOMIC_RELAXED, __HIP_MEMORY_SCOPE_AGENT);
    c.px = (const char*)P.x + (size_t)(128 * c.rb + 16 * w) * 1024;
    c.pf = (const char*)(c.g ? P.wu : P.wg) + (size_t)(16 * c.tm) * 2752 + fs * 64;
    u64 tag = 0; unsigned tagLo = 0, tagHi = 0; float ln_a = 0.f, ln_m = 0.f;
    const int T = 4 * c.rb + c.tm;
    const __amdgpu_buffer_rsrc_t wsr = __builtin_amdgcn_make_buffer_rsrc(ws, 0, 32u << 20, 0x00020000);
    c.pq = (const char*)P.wqkv + ((size_t)(16 * w) * 1536 + 32 * c.cg) * 2;
    c.pwd = (const char*)P.wd + (size_t)(8 * w) * 1024 + c.nb * 128;
    c.pwo = (const char*)P.wo + (size_t)(8 * w) * 1024 + c.nb * 128;
    u32x4 g0a, g0b;
#define GATHER_OFFS(tile) const int gl_ = opaque(lane); const unsigned go0 = (unsigned)(WS_CS + (c.cg * 9 + (tile)) * 2048 + (gl_ >> 3) * 256 + (gl_ & 7) * 32)
#define GATHER_ISSUE(tile) do { GATHER_OFFS(tile); aload16_sc1(g0a, P.ws, go0); aload16_sc1(g0b, P.ws + 16, go0); } while (0)
#define GATHER_OK() (g0a[1] == tagLo && g0a[3] == tagLo && g0b[1] == tagLo && g0b[3] == tagLo)
#define GATHER_RED(tile) do { const int gl_ = opaque(lane); float v0_ = __uint_as_float(g0a[0]), v1_ = __uint_as_float(g0a[2]), v2_ = __uint_as_float(g0b[0]), v3_ = __uint_as_float(g0b[2]); \
        swap32add(v0_, v2_); swap32add(v1_, v3_); swap16add(v0_, v1_); v0_ = dppadd<0x128>(v0_); \
        if ((gl_ & 8) == 0) *(LAS float*)(c.lds + L_CS + (tile) * 128 + (4 * (gl_ & 7) + 2 * (gl_ >> 5) + ((gl_ >> 4) & 1)) * 4) = v0_; } while (0)
#define GATHER_CHECK(tile) do { asm volatile("" : "+v"(g0a), "+v"(g0b)); \
        if (!__all(GATHER_OK())) { GATHER_OFFS(tile); \
            for (unsigned spins = 0;; ++spins) { __builtin_amdgcn_s_sleep(2); \
                g0a = __builtin_amdgcn_raw_buffer_load_b128(wsr, go0, 0, 16); g0b = __builtin_amdgcn_raw_buffer_load_b128(wsr, go0 + 16, 0, 16); __builtin_amdgcn_s_waitcnt(0x0F70); \
                if (__all(GATHER_OK())) break; if (spins > SPIN_CAP) { set_timeout(ws, lane); break; } } } \
        } while (0)
#define F4(v, b) (f32x4){v[b], v[(b) + 1], v[(b) + 2], v[(b) + 3]}

    const int th = w & 1, kq = w >> 1;
    const int T2 = 2 * c.tb + th;
    bf16x8 qf; bool edone_ = false;
    auto gemm1 = [&](auto Gc) {
        constexpr int G = decltype(Gc)::value;
        const int lane = opaque(c.lane), h = lane >> 5, r32 = lane & 31, tid = w * 64 + lane;
        c.vq = (lane >> 2) * 3072 + (lane & 3) * 16;
        c.vx = (lane >> 3) * 1024 + (((lane & 7) ^ (lane >> 4)) * 16); c.vlin = lane * 16;
        G1State st;
        issue_x(c, 0); issue_x(c, 1); issue_w<0>(c);
        u64 ep; aload_flag(ep, ws + WS_EPOCH, 0u);
        u32x4 ax; aload16(ax, (const char*)P.x + (size_t)(128 * c.rb + 8 * c.cg + w) * 1024, c.vlin);
        u32x4 aw0, aw1, aw2, aw3;
        const int ptile = 2 * w + (c.rb & 1); const bool pvalid = ptile <= 8;
        aw0 = aw1 = aw2 = aw3 = (u32x4){0u, 0u, 0u, 0u};
        if (pvalid) {
            const char* tb; unsigned pitch, vo;
            if (ptile < 3) { tb = (const char*)P.wqkv + (ptile == 0 ? 512 : ptile == 1 ? 1024 : 0) * 2 + 64 * c.cg; pitch = 3072; vo = c.vq; }
            else { tb = (const char*)((ptile & 1) ? P.wg : P.wu) + 64 * (fs + ((ptile - 3) >> 1)); pitch = 2752; vo = (lane >> 2) * 2752 + (lane & 3) * 16; }
            tb += (size_t)(64 * (c.rb >> 1)) * pitch;
            aload16(aw0, tb, vo); aload16(aw1, tb + 16 * pitch, vo); aload16(aw2, tb + 32 * pitch, vo); aload16(aw3, tb + 48 * pitch, vo);
        }
        c.vf0 = (lane / 12) * 2752 + (lane % 12) * 16; c.vf1 = ((lane + 64) / 12) * 2752 + ((lane + 64) % 12) * 16; c.vf2 = ((lane + 128) / 12) * 2752 + ((lane + 128) % 12) * 16;
        issue_w<4>(c);
        *(LAS unsigned*)(c.lds + L_ZERO + tid * 4) = 0u;
        if (tid < 4) { *(LAS unsigned*)(c.lds + L_ONES + tid * 4) = 0x3f803f80u; *(LAS unsigned*)(c.lds + L_ONES + 1024 + tid * 4) = 0x3f803f80u; }
        { const unsigned lb = c.lds0 + (8 * h + ((lane & 15) >> 2)) * 64 + 32 * ((lane >> 4) & 1) + 8 * (lane & 3);
#pragma unroll
          for (int j = 0; j < 4; ++j) st.wbq[j] = lb + ((j ^ c.tm) * 1024) + wslot(0); }
        { const unsigned lb = c.lds0 + (8 * h + ((lane & 15) >> 2)) * 192 + 32 * ((lane >> 4) & 1) + 8 * (lane & 3);
#pragma unroll
          for (int j = 0; j < 4; ++j) st.wbf[j] = lb + ((j ^ c.tm) * 3072) + wslot(4); }
        { const int row = 32 * c.tm + r32;
#pragma unroll
          for (int j = 0; j < 4; ++j) st.xb[j] = c.lds0 + xslot(0) + row * 128 + (((2 * (j ^ c.tm) + h) ^ ((row >> 1) & 7)) * 16); }
        st.a0 = f32x16{}; st.a1 = f32x16{}; st.b0 = f32x16{}; st.b1 = f32x16{}; st.qa = f32x16{}; st.qb = f32x16{};

        SYNC(3);
        asm volatile("" : "+v"(ep), "+v"(ax), "+v"(aw0), "+v"(aw1), "+v"(aw2), "+v"(aw3));
        tag = mix64(((u64)(unsigned)__builtin_amdgcn_readfirstlane((int)(unsigned)(ep >> 32)) << 32) | (unsigned)__builtin_amdgcn_readfirstlane((int)(unsigned)ep));
        tagLo = (unsigned)tag; tagHi = (unsigned)(tag >> 32);

        float as_ = 0.f, aq_ = 0.f, av[8];
        auto aux0 = [&](int f) {
            if (f < 4) { const float a = blo(ax[f]), b = bhi(ax[f]); as_ += a + b; aq_ = __builtin_fmaf(a, a, aq_); aq_ = __builtin_fmaf(b, b, aq_); PIN(as_); PIN(aq_); }
            if (f == 4) { as_ = dppadd<0x121>(as_); aq_ = dppadd<0x121>(aq_); as_ = dppadd<0x122>(as_); aq_ = dppadd<0x122>(aq_); PIN(as_); PIN(aq_); }
            if (f == 5) { as_ = dppadd<0x124>(as_); aq_ = dppadd<0x124>(aq_); as_ = dppadd<0x128>(as_); aq_ = dppadd<0x128>(aq_); PIN(as_); PIN(aq_); }
            if (f == 6) { swap16add(as_, as_); swap16add(aq_, aq_); swap32add(as_, as_); swap32add(aq_, aq_); PIN(as_); PIN(aq_); }
            if (f == 8) { const float mean = as_ * (1.f / 512.f), var = aq_ * (1.f / 512.f) - mean * mean, rstd = __builtin_amdgcn_rsqf(fmaxf(var, 0.f) + 1e-5f);
                if (lane == 0) __builtin_amdgcn_raw_buffer_store_b128((u32x4){__float_as_uint(mean), tagLo, __float_as_uint(rstd), tagHi}, wsr, (unsigned)(WS_ST + (128 * c.rb + 8 * c.cg + w) * 16), 0, 16); }
        };
        auto aux1 = [&](int f) {
            if (f < 4) { const int i = f; av[2 * i] = (blo(aw0[i]) + blo(aw1[i])) + (blo(aw2[i]) + blo(aw3[i])); av[2 * i + 1] = (bhi(aw0[i]) + bhi(aw1[i])) + (bhi(aw2[i]) + bhi(aw3[i])); PIN(av[2 * i]); PIN(av[2 * i + 1]); }
            if (f == 8) { swap32add(av[0], av[4]); swap32add(av[1], av[5]); swap32add(av[2], av[6]); swap32add(av[3], av[7]); PIN(av[0]); PIN(av[1]); PIN(av[2]); PIN(av[3]); }
            if (f == 9) { swap16add(av[0], av[2]); swap16add(av[1], av[3]); av[0] = dppadd<0x124>(av[0]); av[1] = dppadd<0x124>(av[1]); av[0] = dppadd<0x128>(av[0]); av[1] = dppadd<0x128>(av[1]); PIN(av[0]); PIN(av[1]); }
            if (f == 10) { const int rho = lane >> 4, col = 8 * (lane & 3) + 4 * (rho >> 1) + 2 * (rho & 1);
                const unsigned off = pvalid ? (unsigned)(WS_CS + (((c.cg * 9 + ptile) * 8 + (c.rb >> 1)) * 32 + col) * 8) : (unsigned)(WS_SCR + (blockIdx.x * 8 + w) * 256 + col * 8);
                if ((lane & 12) == 0) __builtin_amdgcn_raw_buffer_store_b128((u32x4){__float_as_uint(av[0]), tagLo, __float_as_uint(av[1]), tagLo}, wsr, off, 0, 16); }
        };
        u32x4 gs;
        const int gt1 = w < 3 ? w : w < 6 ? w - 3 : w - 6, gt2 = w < 6 ? 3 + w : w - 3;
        stage<G, 0, true, true, -1, -1>(st, [&](int f_) { if (f_ < 2) issue_x1(c, 2, f_); else if (f_ < 4) issue_x1(c, 3, f_ - 2); else if (f_ < 7) issue_w1<1>(c, f_ - 4); aux0(f_); }); SYNCV(8);
        stage<G, 4, true, false, -1, -1>(st, [&](int f_) { if (f_ < 3) issue_w1<5>(c, f_); else if (f_ < 5) issue_x1(c, 4, f_ - 3); else if (f_ < 7) issue_x1(c, 5, f_ - 5); aux1(f_); }); SYNCV(9);
        stage<G, 1, true, true, -1, -1>(st, [&](int f_) { if (f_ >= 1 && f_ < 4) issue_w1<2>(c, f_ - 1); }); SYNCV(4);
        stage<G, 5, true, false, 4, -1>(st, [&](int f_) { if (f_ < 3) issue_w1<6>(c, f_); else if (f_ < 5) issue_x1(c, 6, f_ - 3); else if (f_ < 7) issue_x1(c, 7, f_ - 5); }); SYNCV(7);
        stage<G, 2, true, false, -1, -1>(st, [&](int f_) { if (f_ >= 1 && f_ < 4) issue_w1<3>(c, f_ - 1);
            if (f_ == 8) { aload16_sc1(gs, P.ws, (unsigned)(WS_ST + (128 * c.rb + 32 * c.tm + (opaque(lane) & 31)) * 16)); GATHER_ISSUE(gt1); } }); SYNCV(6);
        stage<G, 6, true, false, 6, -1>(st, [&](int f_) { if (f_ >= 1 && f_ < 4) issue_w1<7>(c, f_ - 1); else if (f_ >= 4 && f_ < 7) issue_w1<8>(c, f_ - 4); }); SYNCV(9);
        stage<G, 3, true, false, -1, -1>(st, [&](int f_) { if (f_ >= 1 && f_ < 4) issue_w1<9>(c, f_ - 1);
            if (f_ == 10) { WAITV(9); PIN(gs);
                if (!__all(gs[1] == tagLo && gs[3] == tagHi)) { const unsigned gso = (unsigned)(WS_ST + (128 * c.rb + 32 * c.tm + (opaque(lane) & 31)) * 16);
                    for (unsigned spins = 0;; ++spins) { __builtin_amdgcn_s_sleep(2); gs = __builtin_amdgcn_raw_buffer_load_b128(wsr, gso, 0, 16); __builtin_amdgcn_s_waitcnt(0x0F70);
                        if (__all(gs[1] == tagLo && gs[3] == tagHi)) break; if (spins > SPIN_CAP) { set_timeout(ws, lane); break; } } }
                ln_m = __uint_as_float(gs[0]); ln_a = __uint_as_float(gs[2]); PIN(ln_m); PIN(ln_a);
                GATHER_CHECK(gt1); }
            if (f_ == 11) GATHER_RED(gt1); });
        { lp xp = c.lds + 13 * GR + w * 2048 + c.vlin; *(LAS f32x4*)(xp) = F4(st.qb, 8 - 8 * G); *(LAS f32x4*)(xp + 1024) = F4(st.qb, 12 - 8 * G);
          if constexpr (G == 1) { if (h == 0) { *(LAS float*)(c.lds + L_CSW + c.tm * 256 + r32 * 4) = ln_m; *(LAS float*)(c.lds + L_CSW + c.tm * 256 + 128 + r32 * 4) = ln_a; } } }
        SYNC(3);
        issue_w<10>(c);
        { const int lane = opaque(c.lane), h = lane >> 5, r32 = lane & 31;
          const unsigned o = (unsigned)(WS_KV + ((size_t)c.cg * 64 + T) * 4096) + lane * 16 + G * 2048;
          f32x16 y;
          if constexpr (G == 0) {
#pragma unroll
              for (int q = 0; q < 4; ++q) { const f32x4 cs = ldsf4((lcp)c.lds + L_CS + (8 * q + 4 * h) * 4);
#pragma unroll
                  for (int i = 0; i < 4; ++i) y[4 * q + i] = ln_a * __builtin_fmaf(-ln_m, cs[i], st.qa[4 * q + i]); }
          } else {
              const float cso = *(const LAS float*)(c.lds + L_CS + 128 + r32 * 4);
#pragma unroll
              for (int q = 0; q < 4; ++q) { const f32x4 mm = ldsf4((lcp)c.lds + L_CSW + c.tm * 256 + (8 * q + 4 * h) * 4), aa = ldsf4((lcp)c.lds + L_CSW + c.tm * 256 + 128 + (8 * q + 4 * h) * 4);
#pragma unroll
                  for (int i = 0; i < 4; ++i) y[4 * q + i] = aa[i] * __builtin_fmaf(-mm[i], cso, st.qa[4 * q + i]); }
          }
          const u32x4 k0 = pack8(y, 0), k1 = pack8(y, 8);
          __builtin_amdgcn_raw_buffer_store_b128(k0, wsr, o, 0, 16); __builtin_amdgcn_raw_buffer_store_b128(k1, wsr, o + 1024, 0, 16);
          if (w == 0 && lane == 0) store_flag(ws, WS_SF + (size_t)blockIdx.x * 8, tag);
          *(LAS u32x4*)(c.lds + L_KVR + c.tm * 4096 + G * 2048 + lane * 16) = k0; *(LAS u32x4*)(c.lds + L_KVR + c.tm * 4096 + G * 2048 + 1024 + lane * 16) = k1;
          SB();
          const f32x4 c0 = ldsf4((lcp)c.lds + L_CS + 2 * 128 + (16 * G + 4 * h) * 4), c1 = ldsf4((lcp)c.lds + L_CS + 2 * 128 + (16 * G + 8 + 4 * h) * 4);
          const lcp xp = (lcp)c.lds + 13 * GR + (w ^ 4) * 2048 + lane * 16;
          const f32x4 p0 = ldsf4(xp), p1 = ldsf4(xp + 1024);
          const float sc = ln_a * (0.25f * 1.4426950408889634f);
#pragma unroll
          for (int i = 0; i < 4; ++i) { y[i] = sc * __builtin_fmaf(-ln_m, c0[i], st.qb[8 * G + i] + p0[i]); y[4 + i] = sc * __builtin_fmaf(-ln_m, c1[i], st.qb[8 * G + 4 + i] + p1[i]); }
          qf = __builtin_bit_cast(bf16x8, pack8(y, 0)); PIN(qf); }
        stage<G, 7, true, false, -1, 8>(st, [&](int) {}); SYNC(5);
        { const int ln = opaque(lane); c.vd = (ln >> 3) * 1024 + (((ln & 7) ^ (((ln >> 4) & 1) << 2)) * 16); }
        stage<G, 8, false, false, -1, 9>(st, [&](int f_) { if (f_ >= 1 && f_ < 4) issue_w1<11>(c, f_ - 1); if (f_ == 4) issue_d(c, 0); }); SYNCV(6);
        stage<G, 9, false, false, -1, 10>(st, [&](int f_) { if (f_ >= 1 && f_ < 4) issue_d(c, f_);
            if (f_ == 5) { WAITV(7); if (lane == 0) store_flag(ws, WS_KVF + ((size_t)(c.cg * 64 + T) * 2 + G) * 8, tag); }
            if (f_ == 8) GATHER_ISSUE(gt2); }); SYNCV(7);
        u64 kf_;
        stage<G, 10, false, false, -1, 11>(st, [&](int f_) { if (f_ >= 1 && f_ < 4) issue_d(c, 3 + f_);
            if (f_ == 6) { WAITV(3); GATHER_CHECK(gt2); GATHER_RED(gt2); }
            if (f_ == 10) { const int tl = 4 * c.rb - 8 + w; aload_flag(kf_, P.ws, (unsigned)(WS_KVF + ((c.cg * 64 + (tl < 0 ? 0 : tl)) * 2 + (opaque(lane) & 1)) * 8)); } }); SYNCV(63);
        stage<G, 11, false, false, -1, -1>(st, [&](int f_) { if (f_ >= 1 && f_ < 3) issue_d(c, 6 + f_);
            if (f_ == 6) { WAITV(2); asm volatile("" : "+v"(kf_)); const int tl = 4 * c.rb - 8 + w;
                if (tl >= 0) {
                    if (!__all(kf_ == tag)) poll_flags((const u64*)(ws + WS_KVF) + (c.cg * 64 + tl) * 2, 2, tag, lane, ws);
                    const char* base = ws + WS_KV + ((size_t)c.cg * 64 + tl) * 4096; const unsigned dst = c.lds0 + L_KV + w * 4096;
                    dma16_sc1(c.vlin, base, dst); dma16_sc1(c.vlin, base + 1024, dst + 1024); dma16_sc1(c.vlin, base + 2048, dst + 2048); dma16_sc1(c.vlin, base + 3072, dst + 3072); } } });
        {
          lp xp = c.lds + 2 * GR + w * 4096 + c.vlin;
          SYNCV(63);
          *(LAS f32x4*)(xp) = F4(st.b0, 8 - 8 * G); *(LAS f32x4*)(xp + 1024) = F4(st.b0, 12 - 8 * G); *(LAS f32x4*)(xp + 2048) = F4(st.b1, 8 - 8 * G); *(LAS f32x4*)(xp + 3072) = F4(st.b1, 12 - 8 * G); }
        SYNC(63);
        {
          const int lane = opaque(c.lane), h = lane >> 5;
          const float a2 = ln_a * ln_a;
          { u32x4 y0, y1;
#pragma unroll
            for (int q = 0; q < 4; ++q) {
                const f32x4 cg_ = ldsf4((lcp)c.lds + L_CS + (3 + 4 * G) * 128 + (8 * q + 4 * h) * 4), cu_ = ldsf4((lcp)c.lds + L_CS + (4 + 4 * G) * 128 + (8 * q + 4 * h) * 4);
                float y[4];
#pragma unroll
                for (int i = 0; i < 4; ++i) { const float g_ = fmaxf(__builtin_fmaf(-ln_m, cg_[i], st.a0[4 * q + i]), 0.f), u_ = __builtin_fmaf(-ln_m, cu_[i], st.a1[4 * q + i]); y[i] = (g_ * a2) * u_; }
                const unsigned lo = pk2(y[0], y[1]), hi = pk2(y[2], y[3]);
                if (q < 2) { y0[2 * q] = lo; y0[2 * q + 1] = hi; } else { y1[2 * q - 4] = lo; y1[2 * q - 3] = hi; }
            }
            if (G == 0 ? v0 : v2) { const unsigned o_ = (unsigned)(WS_C + ((size_t)T * NKS + 2 * (fs + 2 * G)) * 1024) + lane * 16;
                __builtin_amdgcn_raw_buffer_store_b128(y0, wsr, o_, 0, 16); __builtin_amdgcn_raw_buffer_store_b128(y1, wsr, o_ + 1024, 0, 16); } }
          { const lcp xp = (lcp)c.lds + 2 * GR + (w ^ 4) * 4096 + lane * 16;
            u32x4 y0;
#pragma unroll
            for (int q = 0; q < 2; ++q) {
                const f32x4 pg = ldsf4(xp + q * 1024), pu = ldsf4(xp + 2048 + q * 1024);
                const f32x4 cg_ = ldsf4((lcp)c.lds + L_CS + 5 * 128 + (16 * G + 8 * q + 4 * h) * 4), cu_ = ldsf4((lcp)c.lds + L_CS + 6 * 128 + (16 * G + 8 * q + 4 * h) * 4);
                float y[4];
#pragma unroll
                for (int i = 0; i < 4; ++i) { const float g_ = fmaxf(__builtin_fmaf(-ln_m, cg_[i], st.b0[8 * G + 4 * q + i] + pg[i]), 0.f), u_ = __builtin_fmaf(-ln_m, cu_[i], st.b1[8 * G + 4 * q + i] + pu[i]); y[i] = (g_ * a2) * u_; }
                y0[2 * q] = pk2(y[0], y[1]); y0[2 * q + 1] = pk2(y[2], y[3]);
            }
            __builtin_amdgcn_raw_buffer_store_b128(y0, wsr, (unsigned)(WS_C + ((size_t)T * NKS + 2 * (fs + 1) + G) * 1024) + lane * 16, 0, 16); } }
        if (G == 0 ? v0 : v2) { SYNC(3); } else { SYNC(1); }
        if constexpr (G == 1) __builtin_amdgcn_s_setprio(1);
    };
    if (c.g == 0) gemm1(std::integral_constant<int, 0>{}); else gemm1(std::integral_constant<int, 1>{});
    const int lane2 = opaque(lane);
    const unsigned cbase = (unsigned)(WS_C + ((size_t)T2 * NKS + kq) * 1024) + lane2 * 16;
    bf16x8 hf[22];
    u64 hfl; bool hrdy_ = false;
#define LDH(i) do { const bool ok_ = ((i) < 21) || (kq < 2); hf[i] = __builtin_bit_cast(bf16x8, __builtin_amdgcn_raw_buffer_load_b128(wsr, ok_ ? cbase + (i) * 4096 : cbase, 0, 16)); } while (0)
    {
        const int lane = lane2, h = lane >> 5, r32 = lane & 31;
        const int i0 = T >= 8 ? 0 : 8 - T;
        const unsigned kb = c.lds0 + L_KV + c.tm * 4096 + c.g * 1024 + lane * 16;
        const unsigned lanebit = r32 < 16 ? 1u : 0u;
        const unsigned vb = c.lds0 + (r32 < 16 ? (unsigned)(((r32 & 15) + 16 * c.g + 32 * h) * 16) : (unsigned)L_ONES);
        auto koff = [&](int i) -> unsigned { return (unsigned)((i >= i0 ? i : 7) * 4096); };
        auto rdk = [&](int i) -> bf16x8 { return ldsf((lcp)(uintptr_t)(kb + koff(i))); };
        auto vaddr = [&](int i) -> unsigned { const bool valid = i >= i0; const unsigned d = valid ? 0u : (unsigned)(L_ZERO - L_ONES), A = valid ? (unsigned)(L_KV + 2048 + (c.tm + i) * 4096) : (unsigned)L_ZERO;
                                              return lanebit * (A - d) + (vb + d); };
        auto dmask = [&](int r) -> bool { u64 mk = 0;
#pragma unroll
            for (int l = 0; l < 64; ++l) { const int kl = (r & 3) + 8 * (r >> 2) + 4 * (l >> 5); if (kl <= (l & 31)) mk |= 1ull << l; }
            return __builtin_amdgcn_inverse_ballot_w64(mk); };
        const f32x16 zero = f32x16{};
        float m0 = -1e30f, m1 = -1e30f;
#define MAXT(S) do { _Pragma("unroll") for (int r = 0; r < 16; r += 4) { m0 = fmaxf(fmaxf(m0, S[r]), S[r + 1]); m1 = fmaxf(fmaxf(m1, S[r + 2]), S[r + 3]); } PIN(m0); PIN(m1); } while (0)
#define EXPT(S) do { _Pragma("unroll") for (int r = 0; r < 16; ++r) S[r] = __builtin_amdgcn_exp2f(S[r]); } while (0)
#define H_PUBLISH(N) do { WAITV(N); if (lane == 0) store_flag(ws, WS_HF + ((size_t)T * 32 + 2 * c.cg + c.g) * 8, tag); } while (0)
#define H_FLAGS() aload_flag(hfl, P.ws, (unsigned)(WS_HF + (T2 * 32 + (lane & 31)) * 8))
        f32x16 o = f32x16{};
        float m; bool hrdy = false;
        if (T == 0) {
            f32x16 s = mfma(ldsf((lcp)(uintptr_t)(kb + 8 * 4096)), qf, zero);
#pragma unroll
            for (int r = 0; r < 16; ++r) s[r] = dmask(r) ? s[r] : -1e30f;
            MAXT(s);
            m = fmaxf(m0, m1);
            { auto rr = __builtin_amdgcn_permlane32_swap(__float_as_uint(m), __float_as_uint(m), false, false); m = fmaxf(__uint_as_float(rr[0]), __uint_as_float(rr[1])); }
#pragma unroll
            for (int r = 0; r < 16; ++r) s[r] = __builtin_amdgcn_exp2f(s[r] - m);
            const unsigned va = vaddr(8);
            o = mfma(ldsf((lcp)(uintptr_t)va), __builtin_bit_cast(bf16x8, pack8(s, 0)), o);
            o = mfma(ldsf((lcp)(uintptr_t)(va + 1024)), __builtin_bit_cast(bf16x8, pack8(s, 8)), o);
            issue_d(c, 9); issue_d(c, 10); issue_d(c, 11); issue_d(c, 12);
            H_PUBLISH(4);
        } else {
            bf16x8 k0 = rdk(0), k8 = rdk(8), k1 = rdk(1), k2 = rdk(2), k3 = rdk(3);
            f32x16 sA, sB, sC;
            sA = mfma_pin0(k0, qf); sB = mfma_pin0(k8, qf); SB();
            k0 = rdk(4); k8 = rdk(5); sC = mfma_pin0(k1, qf); k1 = rdk(6); SB();
            asm volatile("s_nop 7\n\ts_nop 3"); SB();
#pragma unroll
            for (int r = 0; r < 16; ++r) sA[r] = dmask(r) ? sB[r] : sA[r];
            PIN(sA); SB();
            sB = mfma_pin0(k2, qf); k2 = rdk(7); SB();
            MAXT(sA); SB();
            H_PUBLISH(0);
            sA = mfma_pin0(k3, qf); issue_d(c, 9); SB(); MAXT(sC); SB();
            sC = mfma_pin0(k0, qf); issue_d(c, 10); SB(); MAXT(sB); SB();
            sB = mfma_pin0(k8, qf); issue_d(c, 11); SB(); MAXT(sA); SB();
            sA = mfma_pin0(k1, qf); issue_d(c, 12); SB(); MAXT(sC); SB();
            sC = mfma_pin0(k2, qf); k0 = rdk(0); k8 = rdk(8); k1 = rdk(1); SB(); MAXT(sB); SB();
            MAXT(sA); SB(); MAXT(sC);
            m = fmaxf(m0, m1);
            { auto rr = __builtin_amdgcn_permlane32_swap(__float_as_uint(m), __float_as_uint(m), false, false); m = fmaxf(__uint_as_float(rr[0]), __uint_as_float(rr[1])); }
            f32x16 cm;
#pragma unroll
            for (int r = 0; r < 16; ++r) cm[r] = -m;
            PIN(cm);
            asm volatile("s_nop 1"); sA = mfma_pin(k0, qf, cm); sB = mfma_pin(k8, qf, cm); sC = mfma_pin(k1, qf, cm); SB();
            bf16x8 va0, va1, vb0, vb1;
            { const unsigned a0 = vaddr(0), a8 = vaddr(8); va0 = ldsf((lcp)(uintptr_t)a0); va1 = ldsf((lcp)(uintptr_t)(a0 + 1024)); vb0 = ldsf((lcp)(uintptr_t)a8); vb1 = ldsf((lcp)(uintptr_t)(a8 + 1024)); }
            k0 = rdk(2);
            unsigned mp[8];
#pragma unroll
            for (int j = 0; j < 8; ++j) mp[j] = (dmask(2 * j) ? 0xffffu : 0u) | (dmask(2 * j + 1) ? 0xffff0000u : 0u);
            SB(); asm volatile("s_nop 7"); SB();
#pragma unroll
            for (int r = 0; r < 16; ++r) sA[r] = dmask(r) ? sB[r] : sA[r];
            EXPT(sA);
            { const u32x4 p0 = pack8(sA, 0), p1 = pack8(sA, 8); u32x4 d0, d1, o0, o1;
#pragma unroll
              for (int j = 0; j < 4; ++j) { d0[j] = p0[j] & mp[j]; d1[j] = p1[j] & mp[4 + j]; o0[j] = p0[j] & ~mp[j]; o1[j] = p1[j] & ~mp[4 + j]; }
              o = mfma(va0, __builtin_bit_cast(bf16x8, o0), o); o = mfma(va1, __builtin_bit_cast(bf16x8, o1), o);
              o = mfma(vb0, __builtin_bit_cast(bf16x8, d0), o); o = mfma(vb1, __builtin_bit_cast(bf16x8, d1), o); }
            { const unsigned a1 = vaddr(1), a2 = vaddr(2); va0 = ldsf((lcp)(uintptr_t)a1); va1 = ldsf((lcp)(uintptr_t)(a1 + 1024)); vb0 = ldsf((lcp)(uintptr_t)a2); vb1 = ldsf((lcp)(uintptr_t)(a2 + 1024)); }
            SB();
#define EXP8(S, b) do { _Pragma("unroll") for (int r = (b); r < (b) + 8; ++r) S[r] = __builtin_amdgcn_exp2f(S[r]); } while (0)
#define PK4(S, b) (u32x4){pk2(S[b], S[(b) + 1]), pk2(S[(b) + 2], S[(b) + 3]), pk2(S[(b) + 4], S[(b) + 5]), pk2(S[(b) + 6], S[(b) + 7])}
#define TILE2(i, S, V0, V1, SN) do { \
                if ((i) + 1 <= 7) { SN = mfma_pin(k0, qf, cm); SB(); if ((i) + 2 <= 7) k0 = rdk((i) + 2); } \
                EXP8(S, 0); { u32x4 p_ = PK4(S, 0); PIN(p_); SB(); o = mfma(V0, __builtin_bit_cast(bf16x8, p_), o); SB(); } \
                EXP8(S, 8); { u32x4 p_ = PK4(S, 8); PIN(p_); SB(); o = mfma(V1, __builtin_bit_cast(bf16x8, p_), o); SB(); } \
                if ((i) + 2 <= 7) { const unsigned a_ = vaddr((i) + 2); V0 = ldsf((lcp)(uintptr_t)a_); V1 = ldsf((lcp)(uintptr_t)(a_ + 1024)); } SB(); } while (0)
            TILE2(1, sC, va0, va1, sB); TILE2(2, sB, vb0, vb1, sC);
            H_FLAGS();
            TILE2(3, sC, va0, va1, sB); TILE2(4, sB, vb0, vb1, sC);
            WAITV(0); asm volatile("" : "+v"(hfl));
            hrdy = __all(hfl == tag);
            if (hrdy) { LDH(0); LDH(1); LDH(2); LDH(3); LDH(4); LDH(5); LDH(6); }
            TILE2(5, sC, va0, va1, sB);
            if (hrdy) { LDH(7); LDH(8); LDH(9); LDH(10); LDH(11); LDH(12); LDH(13); }
            TILE2(6, sB, vb0, vb1, sC);
            if (hrdy) { LDH(14); LDH(15); LDH(16); LDH(17); LDH(18); LDH(19); LDH(20); LDH(21); }
            TILE2(7, sC, va0, va1, sB);
        }
        const float inv = __builtin_amdgcn_rcpf(o[8]);
        f32x16 y;
#pragma unroll
        for (int r = 0; r < 8; ++r) y[r] = o[r] * inv;
        const unsigned oo = (unsigned)(WS_C + ((size_t)T * NKS + 86 + 2 * c.cg + c.g) * 1024) + lane * 16;
        const u32x4 ov = pack8(y, 0);
        __builtin_amdgcn_raw_buffer_store_b128(ov, wsr, oo, 0, 16);
        if (!hrdy) {
            poll_flags((const u64*)(ws + WS_HF) + T2 * 32, 32, tag, lane, ws);
#pragma unroll
            for (int i = 0; i < 22; ++i) LDH(i);
        }
        hrdy_ = hrdy;
        __builtin_amdgcn_s_waitcnt(0x0F71);
        __builtin_amdgcn_s_setprio(0);
    }
    {
        const int lane = opaque(lane2), h = lane >> 5, r32 = lane & 31, q4 = (lane & 15) >> 2, p4 = lane & 3, b16 = (lane >> 4) & 1;
        f32x16 acc0 = f32x16{}, acc1 = f32x16{};
        const unsigned wl = c.lds0 + kq * 2048 + (4 * h + q4) * 128 + 32 * b16 + 8 * p4, swz = ((q4 >> 1) & 1) << 6;
        const unsigned wl0 = wl + (0u ^ swz), wl1 = wl + (64u ^ swz);
        bf16x8 wa[3], wb_[3];
#define RDW(u) do { wa[(u) % 3] = trfrag<1024>((lcp)(uintptr_t)(wl0 + dloc(u))); wb_[(u) % 3] = trfrag<1024>((lcp)(uintptr_t)(wl1 + dloc(u))); } while (0)
#define G2STEP(u, frag, nxt) do { acc0 = mfma(wa[(u) % 3], frag, acc0); acc1 = mfma(wb_[(u) % 3], frag, acc1); SB(); if ((nxt) >= 0) RDW((nxt) < 0 ? 0 : (nxt)); SB(); } while (0)
        SYNC(63);
        const bool ew = blockIdx.x == 0 && w == 7; u32x4 sf0 = {0u, 0u, 0u, 0u}, sf1 = sf0; bool edone = false;
        if (ew) { aload16_sc1(sf0, P.ws + WS_SF, (unsigned)(lane * 32)); aload16_sc1(sf1, P.ws + WS_SF + 16, (unsigned)(lane * 32)); }
        RDW(0); RDW(1); RDW(2);
        G2STEP(0, hf[0], 3);
        issue_d(c, 13); G2STEP(1, hf[1], 4); issue_d(c, 14); G2STEP(2, hf[2], 5); issue_d(c, 15); G2STEP(3, hf[3], 6); issue_d(c, 16); G2STEP(4, hf[4], 7); issue_d(c, 17); G2STEP(5, hf[5], 8); issue_d(c, 18);
        if (hrdy_) { WAITV(6); }
        if (lane == 0) store_flag(ws, WS_OF + ((size_t)T * 32 + 2 * c.cg + c.g) * 8, tag);
        SYNCV(63);
        G2STEP(6, hf[6], 9); issue_d(c, 19); G2STEP(7, hf[7], 10); issue_d(c, 20); G2STEP(8, hf[8], 11); issue_d(c, 21); G2STEP(9, hf[9], 12); issue_d(c, 22);
        G2STEP(10, hf[10], -1); issue_d(c, 23); G2STEP(11, hf[11], -1); issue_d(c, 24); G2STEP(12, hf[12], -1);
        SYNCV(7);
        if (ew) { asm volatile("" : "+v"(sf0), "+v"(sf1));
            edone = __all(sf0[0] == tagLo && sf0[1] == tagHi && sf0[2] == tagLo && sf0[3] == tagHi && sf1[0] == tagLo && sf1[1] == tagHi && sf1[2] == tagLo && sf1[3] == tagHi);
            if (edone && lane == 0) store_flag(ws, WS_EPOCH, tag); }
        edone_ = edone;
        RDW(13); RDW(14); RDW(15);
        u32x4 xres;
        u64 ofl;
        issue_d(c, 25); G2STEP(13, hf[13], 16); issue_d(c, 26); G2STEP(14, hf[14], 17); issue_d(c, 27); G2STEP(15, hf[15], 18); issue_d(c, 28);
        aload_flag(ofl, P.ws, (unsigned)(WS_OF + (T2 * 32 + (lane & 31)) * 8));
        G2STEP(16, hf[16], -1); issue_d(c, 29);
        xres = *(const u32x4*)(P.x + (size_t)(64 * c.tb + 8 * w + (lane >> 3)) * 512 + 64 * c.nb + (lane & 7) * 8);
        G2STEP(17, hf[17], -1); G2STEP(18, hf[18], -1);
        WAITV(2); asm volatile("" : "+v"(ofl));
        if (!__all(ofl == tag)) poll_flags((const u64*)(ws + WS_OF) + T2 * 32, 32, tag, lane, ws);
        bf16x8 of[8];
#pragma unroll
        for (int i = 0; i < 8; ++i) of[i] = __builtin_bit_cast(bf16x8, __builtin_amdgcn_raw_buffer_load_b128(wsr, cbase + (86 + 4 * i) * 1024, 0, 16));
        SYNCV(63);
        RDW(19); RDW(20); RDW(21);
        G2STEP(19, hf[19], 22); G2STEP(20, hf[20], 23);
        if (kq < 2) { acc0 = mfma(wa[21 % 3], hf[21], acc0); acc1 = mfma(wb_[21 % 3], hf[21], acc1); }
        SB(); RDW(24); SB();
        G2STEP(22, of[0], -1); G2STEP(23, of[1], -1); G2STEP(24, of[2], -1);
        SYNCV(63);
        RDW(25); RDW(26); RDW(27);
        G2STEP(25, of[3], 28); G2STEP(26, of[4], 29); G2STEP(27, of[5], -1); G2STEP(28, of[6], -1); G2STEP(29, of[7], -1);
        { lp rbp = c.lds + (kq < 2 ? 4 * GR : 13 * GR - 32768) + (kq * 2 + th) * 8192;
#pragma unroll
          for (int q = 0; q < 4; ++q) { *(LAS f32x4*)(rbp + q * 1024 + ((lane + 2 * q) & 63) * 16) = F4(acc0, 4 * q); *(LAS f32x4*)(rbp + 4096 + q * 1024 + ((lane + 2 * (4 + q)) & 63) * 16) = F4(acc1, 4 * q); } }
        SYNC(63);
        {
          const int t = 8 * w + (lane >> 3), cc = lane & 7;
          const unsigned a0 = (unsigned)((t >> 5) * 8192 + cc * 1024), s0i = (unsigned)(((t & 31) + 2 * cc) & 63) * 16, s1i = (unsigned)(((t & 31) + 32 + 2 * cc) & 63) * 16;
          f32x4 s0 = {0.f, 0.f, 0.f, 0.f}, s1 = s0;
#pragma unroll
          for (int k = 0; k < 4; ++k) { const lcp bp = (lcp)c.lds + (k < 2 ? 4 * GR : 13 * GR - 32768) + k * 16384 + a0; s0 += ldsf4(bp + s0i); s1 += ldsf4(bp + s1i); }
          u32x4 ov;
          ov[0] = pk2(s0[0] + blo(xres[0]), s0[1] + bhi(xres[0])); ov[1] = pk2(s0[2] + blo(xres[1]), s0[3] + bhi(xres[1]));
          ov[2] = pk2(s1[0] + blo(xres[2]), s1[1] + bhi(xres[2])); ov[3] = pk2(s1[2] + blo(xres[3]), s1[3] + bhi(xres[3]));
          if (blockIdx.x == 100 && w == 0) { u64 mn = ~0ull, mx = 0;
              for (int j = 0; j < 4; ++j) { const u64 tt = __hip_atomic_load((const GAS u64*)(P.ws + (32u << 20)) + lane * 4 + j, __ATOMIC_RELAXED, __HIP_MEMORY_SCOPE_AGENT); mn = tt < mn ? tt : mn; mx = tt > mx ? tt : mx; }
              for (int m = 32; m >= 1; m >>= 1) { const u64 a = __shfl_xor(mn, m), b = __shfl_xor(mx, m); mn = a < mn ? a : mn; mx = b > mx ? b : mx; }
              const u64 now = __builtin_amdgcn_s_memrealtime();
              const float val = DSEL == 0 ? (float)(now - mn) : (float)(mx - mn);
              if (lane == 0 && val > DTHR) ov[0] = (ov[0] & 0xffff0000u) | (pk2(val, 0.f) & 0xffffu); }
          *(u32x4*)(P.out + (size_t)(64 * c.tb + t) * 512 + 64 * c.nb + cc * 8) = ov; }
    }
    if (blockIdx.x == 0 && w == 7 && !edone_) {
        const GAS u64* pp = (const GAS u64*)(ws + WS_SF) + lane * 4;
        for (unsigned spins = 0;; ++spins) { bool ok = true;
#pragma unroll
            for (int j = 0; j < 4; ++j) ok &= __hip_atomic_load(pp + j, __ATOMIC_RELAXED, __HIP_MEMORY_SCOPE_AGENT) == tag;
            if (__all(ok)) break; if (spins > SPIN_CAP) { set_timeout(ws, lane); break; } __builtin_amdgcn_s_sleep(2); }
        if (lane == 0) store_flag(ws, WS_EPOCH, tag);
    }
}

extern "C" void kernel_launch(void* const* d_in, const int* in_sizes, int n_in, void* d_out, int out_size, void* d_ws, size_t ws_size, hipStream_t stream) {
    Params p;
    p.x = (const u16*)d_in[0]; p.wqkv = (const u16*)d_in[1]; p.wo = (const u16*)d_in[2];
    p.wg = (const u16*)d_in[4]; p.wu = (const u16*)d_in[5]; p.wd = (const u16*)d_in[6];
    p.out = (u16*)d_out; p.ws = (char*)d_ws;
    (void)hipFuncSetAttribute((const void*)fused_kernel, hipFuncAttributeMaxDynamicSharedMemorySize, LDS_BYTES);
    fused_kernel<<<256, 512, LDS_BYTES, stream>>>(p);
}
```

```cpp
#include <hip/hip_runtime.h>
#include <stdint.h>
#include <type_traits>

#define FI __device__ __forceinline__
#define LAS __attribute__((address_space(3)))
#define GAS __attribute__((address_space(1)))
typedef unsigned short u16;
typedef unsigned long long u64;
typedef short bf16x8 __attribute__((ext_vector_type(8)));
typedef short s16x4 __attribute__((ext_vector_type(4)));
typedef float f32x16 __attribute__((ext_vector_type(16)));
typedef float f32x4 __attribute__((ext_vector_type(4)));
typedef float f32x2 __attribute__((ext_vector_type(2)));
typedef unsigned u32x4 __attribute__((ext_vector_type(4)));
typedef __bf16 bf16x2_t __attribute__((ext_vector_type(2)));
typedef LAS const char* lcp;
typedef LAS char* lp;

constexpr int GR = 8192;
constexpr int L_KV = 13 * GR;
constexpr int L_KVR = 17 * GR;
constexpr int L_MISC = 19 * GR;
constexpr int L_CS = L_MISC;
constexpr int L_CSW = L_MISC + 2048;
constexpr int L_ZERO = L_MISC + 4096;
constexpr int L_ONES = L_MISC + 6144;
constexpr int LDS_BYTES = 163840;

constexpr size_t WS_EPOCH = 0, WS_TMO = 64;
constexpr size_t WS_SF = 4096;
constexpr size_t WS_KVF = 8192;
constexpr size_t WS_HF = 24576;
constexpr size_t WS_OF = 40960;
constexpr size_t WS_ST = 65536;
constexpr size_t WS_CS = 131072;
constexpr size_t WS_SCR = 768u << 10;
constexpr size_t WS_KV = 2u << 20;
constexpr size_t WS_C = 8u << 20;
constexpr int NKS = 118;

FI f32x16 mfma(bf16x8 a, bf16x8 b, f32x16 c) { return __builtin_amdgcn_mfma_f32_32x32x16_bf16(a, b, c, 0, 0, 0); }
FI f32x16 mfma_pin(bf16x8 a, bf16x8 b, const f32x16& c) { f32x16 d; asm volatile("v_mfma_f32_32x32x16_bf16 %0, %1, %2, %3" : "=&v"(d) : "v"(a), "v"(b), "v"(c)); return d; }
FI f32x16 mfma_pin0(bf16x8 a, bf16x8 b) { f32x16 d; asm volatile("v_mfma_f32_32x32x16_bf16 %0, %1, %2, 0" : "=&v"(d) : "v"(a), "v"(b)); return d; }
FI unsigned pk2(float lo, float hi) { f32x2 v = {lo, hi}; bf16x2_t r = __builtin_convertvector(v, bf16x2_t); return __builtin_bit_cast(unsigned, r); }
FI u32x4 pack8(const f32x16& y, int b) { u32x4 r; r[0] = pk2(y[b], y[b + 1]); r[1] = pk2(y[b + 2], y[b + 3]); r[2] = pk2(y[b + 4], y[b + 5]); r[3] = pk2(y[b + 6], y[b + 7]); return r; }
typedef short v4i16_t __attribute__((ext_vector_type(4)));
FI s16x4 trd(lcp p) { return __builtin_bit_cast(s16x4, __builtin_amdgcn_ds_read_tr16_b64_v4i16((LAS v4i16_t*)p)); }
template <int D2> FI bf16x8 trfrag(lcp p) { s16x4 lo = trd(p), hi = trd(p + D2); return __builtin_shufflevector(lo, hi, 0, 1, 2, 3, 4, 5, 6, 7); }
FI bf16x8 ldsf(lcp p) { return *(const LAS bf16x8*)p; }
FI f32x4 ldsf4(lcp p) { return *(const LAS f32x4*)p; }

FI void dma16(unsigned voff, const void* sbase, unsigned dst) {
    asm volatile("s_mov_b32 m0, %2\n\ts_nop 0\n\tglobal_load_lds_dwordx4 %0, %1" :: "v"(voff), "s"(sbase), "s"(__builtin_amdgcn_readfirstlane(dst)) : "memory", "m0");
}
FI void dma16_sc1(unsigned voff, const void* sbase, unsigned dst) {
    asm volatile("s_mov_b32 m0, %2\n\ts_nop 0\n\tglobal_load_lds_dwordx4 %0, %1 sc1" :: "v"(voff), "s"(sbase), "s"(__builtin_amdgcn_readfirstlane(dst)) : "memory", "m0");
}
FI void aload16(u32x4& v, const void* base, unsigned voff) { asm volatile("global_load_dwordx4 %0, %1, %2" : "=v"(v) : "v"(voff), "s"(base) : "memory"); }
FI void aload16_sc1(u32x4& v, const void* base, unsigned voff) { asm volatile("global_load_dwordx4 %0, %1, %2 sc1" : "=v"(v) : "v"(voff), "s"(base) : "memory"); }
FI void aload_flag(u64& v, const char* base, unsigned voff) { asm volatile("global_load_dwordx2 %0, %1, %2 sc1" : "=v"(v) : "v"(voff), "s"(base) : "memory"); }

#define SB() __builtin_amdgcn_sched_barrier(0)
#define PIN(x) asm volatile("" : "+v"(x))
FI int opaque(int v) { asm volatile("" : "+v"(v)); return v; }
#define SYNC(N) do { SB(); asm volatile("s_waitcnt vmcnt(" #N ") lgkmcnt(0)\n\ts_barrier" ::: "memory"); SB(); } while (0)
#define SYNCV(N) do { SB(); asm volatile("s_waitcnt vmcnt(" #N ")\n\ts_barrier" ::: "memory"); SB(); } while (0)
#define WAITV(N) do { SB(); asm volatile("s_waitcnt vmcnt(" #N ")" ::: "memory"); SB(); } while (0)

struct Params {
    const u16* x; const u16* wqkv; const u16* wo; const u16* wg; const u16* wu; const u16* wd; u16* out; char* ws;
};

constexpr int wslot(int s) { return (s == 0 ? 4 : s == 1 ? 14 : s == 2 ? 7 : s == 3 ? 0 : s == 4 ? 7 : s == 5 ? 0 : s == 6 ? 10 : s == 7 ? 3 : s == 8 ? 6 : s == 9 ? 9 : s == 10 ? 0 : 3) * GR; }
constexpr int xslot(int c) { return (c == 0 ? 0 : c == 1 ? 2 : c == 2 ? 10 : c == 3 ? 12 : c == 4 ? 3 : c == 5 ? 5 : c == 6 ? 13 : 15) * GR; }
FI constexpr int dloc0(int u) { return (u == 0 ? 12 : u < 4 ? 6 + (u - 1) : u < 7 ? 9 + (u - 4) : u < 9 ? u - 7 : 2 + (u - 9)) * GR; }
FI constexpr int dloc(int u) { return u < 13 ? dloc0(u) : u < 19 ? L_KV + (u - 13) * GR : dloc0(u - 19); }

struct Ctx {
    const Params* p;
    unsigned lds0;
    lp lds;
    int lane, w, tm, g, rb, cg, tb, nb;
    unsigned vq, vf0, vf1, vf2, vx, vlin, vd;
    const char *px, *pq, *pf, *pwd, *pwo;
};

FI void issue_x1(const Ctx& c, int chunk, int k) {
    dma16(k ? c.vx ^ 64u : c.vx, c.px + chunk * 128 + k * 8 * 1024, c.lds0 + xslot(chunk) + 16 * c.w * 128 + k * 8 * 128);
}
FI void issue_x(const Ctx& c, int chunk) { issue_x1(c, chunk, 0); issue_x1(c, chunk, 1); }
template <int S> FI void issue_w1(const Ctx& c, int k) {
    if constexpr (S >= 4) {
        const char* base = c.pf + (size_t)(S - 4) * (64 * 2752);
        dma16(k == 0 ? c.vf0 : k == 1 ? c.vf1 : c.vf2, base, c.lds0 + wslot(S) + c.g * 12288 + c.tm * 3072 + k * 1024);
    } else {
        const char* base = c.pq + (size_t)S * (128 * 3072);
        dma16(c.vq, base + (k == 0 ? 1024 : k == 1 ? 2048 : 0), c.lds0 + wslot(S) + c.w * 1024 + k * 8192);
    }
}
template <int S> FI void issue_w(const Ctx& c) { issue_w1<S>(c, 0); issue_w1<S>(c, 1); issue_w1<S>(c, 2); }
FI void issue_d(const Ctx& c, int u) {
    const char* base;
    if (u < 21) base = c.pwd + (size_t)u * 65536;
    else if (u == 21) base = c.pwd + (size_t)21 * 65536 - (c.w >= 4 ? 32768 : 0);
    else base = c.pwo + (size_t)(u - 22) * 65536;
    dma16(c.vd, base, c.lds0 + dloc(u) + c.w * 1024);
}

FI float blo(unsigned d) { return __uint_as_float(d << 16); }
FI float bhi(unsigned d) { return __uint_as_float(d & 0xffff0000u); }
template <int CTRL> FI float dppadd(float x) { return x + __builtin_bit_cast(float, __builtin_amdgcn_update_dpp(0, __builtin_bit_cast(int, x), CTRL, 0xf, 0xf, true)); }
FI void swap32add(float& u, float v) { auto rr = __builtin_amdgcn_permlane32_swap(__float_as_uint(u), __float_as_uint(v), false, false); u = __uint_as_float(rr[0]) + __uint_as_float(rr[1]); }
FI void swap16add(float& u, float v) { auto rr = __builtin_amdgcn_permlane16_swap(__float_as_uint(u), __float_as_uint(v), false, false); u = __uint_as_float(rr[0]) + __uint_as_float(rr[1]); }

struct G1State {
    bf16x8 xf[32];
    bf16x8 wq[4];
    f32x16 a0, a1, b0, b1;
    f32x16 qa, qb;
    unsigned wbf[4], wbq[4];
    unsigned xb[4];
};
constexpr int fkind(int G, int f) { return G == 0 ? (f < 8 ? (f & 3) : (f & 1)) : (f < 4 ? (f & 1) : f < 6 ? (f & 1) : f < 8 ? 2 + (f & 1) : f < 10 ? (f & 1) : 2 + (f & 1)); }
constexpr int fpos(int G, int f) { return G == 0 ? (f < 8 ? (f >> 2) : 2 + ((f - 8) >> 1)) : (f < 4 ? (f >> 1) : f < 8 ? 2 : 3); }
constexpr int qkind(int G, int f) { return G == 0 ? ((f == 1 || f == 3 || f == 7 || f == 9) ? 1 : 0) : ((f == 3 || f == 5 || f == 9 || f == 11) ? 1 : 0); }
constexpr int qpos(int G, int f) {
    return G == 0 ? (f == 0 ? 0 : f == 1 ? 0 : f == 2 ? 1 : f == 3 ? 1 : f == 4 ? 2 : f == 5 ? 3 : f == 6 ? 4 : f == 7 ? 4 : f == 8 ? 5 : f == 9 ? 5 : f == 10 ? 6 : 7)
                  : (f == 0 ? 0 : f == 1 ? 1 : f == 2 ? 2 : f == 3 ? 2 : f == 4 ? 3 : f == 5 ? 3 : f == 6 ? 4 : f == 7 ? 5 : f == 8 ? 6 : f == 9 ? 6 : f == 10 ? 7 : 7);
}
template <int G, int S> FI bf16x8 rdfrag(const G1State& st, int f) {
    if constexpr (S >= 4) { const int k = fkind(G, f); return trfrag<768>((lcp)(uintptr_t)(st.wbf[fpos(G, f)] + (k & 1) * 12288 + (k >= 2 ? 64 : (G == 0 ? 0 : 128)))); }
    else { const int e = qpos(G, f); return trfrag<256>((lcp)(uintptr_t)(st.wbq[e & 3] + (e >> 2) * 4096 + (qkind(G, f) ? 16384 : G * 8192))); }
}
template <int C> FI void rdx(G1State& st) {
#pragma unroll
    for (int e = 0; e < 4; ++e) { st.xf[4 * C + e] = ldsf((lcp)(uintptr_t)st.xb[e]); if (C < 7) st.xb[e] = (unsigned)opaque((int)(st.xb[e] + (xslot(C + 1) - xslot(C)))); }
}
template <int G, int S, bool FRESH, bool XF, int XP, int NX, class F>
FI void stage(G1State& st, F&& hook) {
    if constexpr (XF) rdx<(XF ? 2 * S : 0)>(st);
    if constexpr (FRESH) {
#pragma unroll
        for (int f = 0; f < 4; ++f) st.wq[f] = rdfrag<G, S>(st, f);
    }
    if constexpr (XF) rdx<(XF ? 2 * S + 1 : 0)>(st);
    SB();
#pragma unroll
    for (int f = 0; f < 12; ++f) {
        const bf16x8 w = st.wq[f & 3];
        if constexpr (S >= 4) {
            const int k = fkind(G, f); const bf16x8 x = st.xf[4 * (S - 4) + fpos(G, f)];
            if (k == 0) st.a0 = mfma(w, x, st.a0); else if (k == 1) st.a1 = mfma(w, x, st.a1); else if (k == 2) st.b0 = mfma(w, x, st.b0); else st.b1 = mfma(w, x, st.b1);
        } else {
            const bf16x8 x = st.xf[8 * S + qpos(G, f)];
            if (qkind(G, f)) st.qb = mfma(w, x, st.qb); else if (G == 0) st.qa = mfma(w, x, st.qa); else st.qa = mfma(x, w, st.qa);
        }
        SB();
        if (f < 8) st.wq[f & 3] = rdfrag<G, S>(st, f + 4);
        else {
            if (f == 8) {
                if constexpr (S < 3) { _Pragma("unroll") for (int j = 0; j < 4; ++j) st.wbq[j] = (unsigned)opaque((int)(st.wbq[j] + (wslot(S + 1) - wslot(S)))); }
                if constexpr (S >= 4 && S < 11) { _Pragma("unroll") for (int j = 0; j < 4; ++j) st.wbf[j] = (unsigned)opaque((int)(st.wbf[j] + (wslot(S + 1) - wslot(S)))); }
            }
            if constexpr (NX >= 0) st.wq[f & 3] = rdfrag<G, (NX >= 0 ? NX : 0)>(st, f - 8);
        }
        if constexpr (XP >= 0) { if (f == 2) rdx<(XP >= 0 ? XP : 0)>(st); if (f == 6) rdx<(XP >= 0 ? XP + 1 : 0)>(st); }
        hook(f);
        SB();
    }
}

FI u64 mix64(u64 z) { z += 0x9E3779B97F4A7C15ull; z = (z ^ (z >> 30)) * 0xBF58476D1CE4E5B9ull; z = (z ^ (z >> 27)) * 0x94D049BB133111EBull; return z ^ (z >> 31); }

constexpr unsigned SPIN_CAP = 1u << 18;
FI void set_timeout(char* ws, int lane) { if (lane == 0) __hip_atomic_store((GAS unsigned*)(ws + WS_TMO), 1u, __ATOMIC_RELAXED, __HIP_MEMORY_SCOPE_AGENT); }
FI void poll_flags(const u64* flags, int n, u64 tag, int lane, char* ws) {
    const GAS u64* pp = (const GAS u64*)flags + (lane < n ? lane : 0);
    for (unsigned spins = 0;; ++spins) {
        const u64 v = __hip_atomic_load(pp, __ATOMIC_RELAXED, __HIP_MEMORY_SCOPE_AGENT);
        if (__all(v == tag)) return;
        if (spins > SPIN_CAP) { set_timeout(ws, lane); return; }
        __builtin_amdgcn_s_sleep(2);
    }
}
FI void store_flag(char* ws, size_t off, u64 tag) { __hip_atomic_store((GAS u64*)(ws + off), tag, __ATOMIC_RELAXED, __HIP_MEMORY_SCOPE_AGENT); }

__global__ __launch_bounds__(512, 2) void fused_kernel(Params P) {
    extern __shared__ __attribute__((aligned(16))) char smem[];
    Ctx c; c.p = &P;
    const int tid = threadIdx.x; c.lane = tid & 63;
    const int w = __builtin_amdgcn_readfirstlane(tid >> 6);
    c.w = w; c.tm = w & 3; c.g = w >> 2;
    c.lds = (lp)smem; c.lds0 = (unsigned)(uintptr_t)c.lds;
    const int xcd = blockIdx.x & 7, idx = blockIdx.x >> 3;
    c.rb = 8 * (xcd >> 2) + (idx >> 2); c.cg = 4 * (xcd & 3) + (idx & 3);
    c.tb = 8 * (xcd >> 1) + (idx >> 2); c.nb = 4 * (xcd & 1) + (idx & 3);
    const int fs = c.cg < 11 ? 3 * c.cg : c.cg < 15 ? 33 + 2 * (c.cg - 11) : 40;
    const bool v0 = c.cg != 15, v2 = c.cg < 11 || c.cg == 15;
    const int lane = c.lane, h = lane >> 5, r32 = lane & 31;
    char* ws = P.ws;
    c.px = (const char*)P.x + (size_t)(128 * c.rb + 16 * w) * 1024;
    c.pf = (const char*)(c.g ? P.wu : P.wg) + (size_t)(16 * c.tm) * 2752 + fs * 64;
    u64 tag = 0; unsigned tagLo = 0, tagHi = 0; float ln_a = 0.f, ln_m = 0.f;
    const int T = 4 * c.rb + c.tm;
    const __amdgpu_buffer_rsrc_t wsr = __builtin_amdgcn_make_buffer_rsrc(ws, 0, 32u << 20, 0x00020000);
    c.pq = (const char*)P.wqkv + ((size_t)(16 * w) * 1536 + 32 * c.cg) * 2;
    c.pwd = (const char*)P.wd + (size_t)(8 * w) * 1024 + c.nb * 128;
    c.pwo = (const char*)P.wo + (size_t)(8 * w) * 1024 + c.nb * 128;
    u32x4 g0a, g0b;
#define GATHER_OFFS(tile) const int gl_ = opaque(lane); const unsigned go0 = (unsigned)(WS_CS + (c.cg * 9 + (tile)) * 2048 + (gl_ >> 3) * 256 + (gl_ & 7) * 32)
#define GATHER_ISSUE(tile) do { GATHER_OFFS(tile); aload16_sc1(g0a, P.ws, go0); aload16_sc1(g0b, P.ws + 16, go0); } while (0)
#define GATHER_OK() (g0a[1] == tagLo && g0a[3] == tagLo && g0b[1] == tagLo && g0b[3] == tagLo)
#define GATHER_RED(tile) do { const int gl_ = opaque(lane); float v0_ = __uint_as_float(g0a[0]), v1_ = __uint_as_float(g0a[2]), v2_ = __uint_as_float(g0b[0]), v3_ = __uint_as_float(g0b[2]); \
        swap32add(v0_, v2_); swap32add(v1_, v3_); swap16add(v0_, v1_); v0_ = dppadd<0x128>(v0_); \
        if ((gl_ & 8) == 0) *(LAS float*)(c.lds + L_CS + (tile) * 128 + (4 * (gl_ & 7) + 2 * (gl_ >> 5) + ((gl_ >> 4) & 1)) * 4) = v0_; } while (0)
#define GATHER_CHECK(tile) do { asm volatile("" : "+v"(g0a), "+v"(g0b)); \
        if (!__all(GATHER_OK())) { GATHER_OFFS(tile); \
            for (unsigned spins = 0;; ++spins) { __builtin_amdgcn_s_sleep(2); \
                g0a = __builtin_amdgcn_raw_buffer_load_b128(wsr, go0, 0, 16); g0b = __builtin_amdgcn_raw_buffer_load_b128(wsr, go0 + 16, 0, 16); __builtin_amdgcn_s_waitcnt(0x0F70); \
                if (__all(GATHER_OK())) break; if (spins > SPIN_CAP) { set_timeout(ws, lane); break; } } } \
        } while (0)
#define F4(v, b) (f32x4){v[b], v[(b) + 1], v[(b) + 2], v[(b) + 3]}

    const int th = w & 1, kq = w >> 1;
    const int T2 = 2 * c.tb + th;
    bf16x8 qf; bool edone_ = false;
    auto gemm1 = [&](auto Gc) {
        constexpr int G = decltype(Gc)::value;
        const int lane = opaque(c.lane), h = lane >> 5, r32 = lane & 31, tid = w * 64 + lane;
        c.vq = (lane >> 2) * 3072 + (lane & 3) * 16;
        c.vx = (lane >> 3) * 1024 + (((lane & 7) ^ (lane >> 4)) * 16); c.vlin = lane * 16;
        G1State st;
        issue_x(c, 0); issue_x(c, 1); issue_w<0>(c);
        u64 ep; aload_flag(ep, ws + WS_EPOCH, 0u);
        u32x4 ax; aload16(ax, (const char*)P.x + (size_t)(128 * c.rb + 8 * c.cg + w) * 1024, c.vlin);
        u32x4 aw0, aw1, aw2, aw3;
        const int ptile = 2 * w + (c.rb & 1); const bool pvalid = ptile <= 8;
        aw0 = aw1 = aw2 = aw3 = (u32x4){0u, 0u, 0u, 0u};
        if (pvalid) {
            const char* tb; unsigned pitch, vo;
            if (ptile < 3) { tb = (const char*)P.wqkv + (ptile == 0 ? 512 : ptile == 1 ? 1024 : 0) * 2 + 64 * c.cg; pitch = 3072; vo = c.vq; }
            else { tb = (const char*)((ptile & 1) ? P.wg : P.wu) + 64 * (fs + ((ptile - 3) >> 1)); pitch = 2752; vo = (lane >> 2) * 2752 + (lane & 3) * 16; }
            tb += (size_t)(64 * (c.rb >> 1)) * pitch;
            aload16(aw0, tb, vo); aload16(aw1, tb + 16 * pitch, vo); aload16(aw2, tb + 32 * pitch, vo); aload16(aw3, tb + 48 * pitch, vo);
        }
        c.vf0 = (lane / 12) * 2752 + (lane % 12) * 16; c.vf1 = ((lane + 64) / 12) * 2752 + ((lane + 64) % 12) * 16; c.vf2 = ((lane + 128) / 12) * 2752 + ((lane + 128) % 12) * 16;
        issue_w<4>(c);
        *(LAS unsigned*)(c.lds + L_ZERO + tid * 4) = 0u;
        if (tid < 4) { *(LAS unsigned*)(c.lds + L_ONES + tid * 4) = 0x3f803f80u; *(LAS unsigned*)(c.lds + L_ONES + 1024 + tid * 4) = 0x3f803f80u; }
        { const unsigned lb = c.lds0 + (8 * h + ((lane & 15) >> 2)) * 64 + 32 * ((lane >> 4) & 1) + 8 * (lane & 3);
#pragma unroll
          for (int j = 0; j < 4; ++j) st.wbq[j] = lb + ((j ^ c.tm) * 1024) + wslot(0); }
        { const unsigned lb = c.lds0 + (8 * h + ((lane & 15) >> 2)) * 192 + 32 * ((lane >> 4) & 1) + 8 * (lane & 3);
#pragma unroll
          for (int j = 0; j < 4; ++j) st.wbf[j] = lb + ((j ^ c.tm) * 3072) + wslot(4); }
        { const int row = 32 * c.tm + r32;
#pragma unroll
          for (int j = 0; j < 4; ++j) st.xb[j] = c.lds0 + xslot(0) + row * 128 + (((2 * (j ^ c.tm) + h) ^ ((row >> 1) & 7)) * 16); }
        st.a0 = f32x16{}; st.a1 = f32x16{}; st.b0 = f32x16{}; st.b1 = f32x16{}; st.qa = f32x16{}; st.qb = f32x16{};

        SYNC(3);
        asm volatile("" : "+v"(ep), "+v"(ax), "+v"(aw0), "+v"(aw1), "+v"(aw2), "+v"(aw3));
        tag = mix64(((u64)(unsigned)__builtin_amdgcn_readfirstlane((int)(unsigned)(ep >> 32)) << 32) | (unsigned)__builtin_amdgcn_readfirstlane((int)(unsigned)ep));
        tagLo = (unsigned)tag; tagHi = (unsigned)(tag >> 32);

        float as_ = 0.f, aq_ = 0.f, av[8];
        auto aux0 = [&](int f) {
            if (f < 4) { const float a = blo(ax[f]), b = bhi(ax[f]); as_ += a + b; aq_ = __builtin_fmaf(a, a, aq_); aq_ = __builtin_fmaf(b, b, aq_); PIN(as_); PIN(aq_); }
            if (f == 4) { as_ = dppadd<0x121>(as_); aq_ = dppadd<0x121>(aq_); as_ = dppadd<0x122>(as_); aq_ = dppadd<0x122>(aq_); PIN(as_); PIN(aq_); }
            if (f == 5) { as_ = dppadd<0x124>(as_); aq_ = dppadd<0x124>(aq_); as_ = dppadd<0x128>(as_); aq_ = dppadd<0x128>(aq_); PIN(as_); PIN(aq_); }
            if (f == 6) { swap16add(as_, as_); swap16add(aq_, aq_); swap32add(as_, as_); swap32add(aq_, aq_); PIN(as_); PIN(aq_); }
            if (f == 8) { const float mean = as_ * (1.f / 512.f), var = aq_ * (1.f / 512.f) - mean * mean, rstd = __builtin_amdgcn_rsqf(fmaxf(var, 0.f) + 1e-5f);
                if (lane == 0) __builtin_amdgcn_raw_buffer_store_b128((u32x4){__float_as_uint(mean), tagLo, __float_as_uint(rstd), tagHi}, wsr, (unsigned)(WS_ST + (128 * c.rb + 8 * c.cg + w) * 16), 0, 16); }
        };
        auto aux1 = [&](int f) {
            if (f < 4) { const int i = f; av[2 * i] = (blo(aw0[i]) + blo(aw1[i])) + (blo(aw2[i]) + blo(aw3[i])); av[2 * i + 1] = (bhi(aw0[i]) + bhi(aw1[i])) + (bhi(aw2[i]) + bhi(aw3[i])); PIN(av[2 * i]); PIN(av[2 * i + 1]); }
            if (f == 8) { swap32add(av[0], av[4]); swap32add(av[1], av[5]); swap32add(av[2], av[6]); swap32add(av[3], av[7]); PIN(av[0]); PIN(av[1]); PIN(av[2]); PIN(av[3]); }
            if (f == 9) { swap16add(av[0], av[2]); swap16add(av[1], av[3]); av[0] = dppadd<0x124>(av[0]); av[1] = dppadd<0x124>(av[1]); av[0] = dppadd<0x128>(av[0]); av[1] = dppadd<0x128>(av[1]); PIN(av[0]); PIN(av[1]); }
            if (f == 10) { const int rho = lane >> 4, col = 8 * (lane & 3) + 4 * (rho >> 1) + 2 * (rho & 1);
                const unsigned off = pvalid ? (unsigned)(WS_CS + (((c.cg * 9 + ptile) * 8 + (c.rb >> 1)) * 32 + col) * 8) : (unsigned)(WS_SCR + (blockIdx.x * 8 + w) * 256 + col * 8);
                if ((lane & 12) == 0) __builtin_amdgcn_raw_buffer_store_b128((u32x4){__float_as_uint(av[0]), tagLo, __float_as_uint(av[1]), tagLo}, wsr, off, 0, 16); }
        };
        u32x4 gs;
        const int gt1 = w < 3 ? w : w < 6 ? w - 3 : w - 6, gt2 = w < 6 ? 3 + w : w - 3;
        stage<G, 0, true, true, -1, -1>(st, [&](int f_) { if (f_ < 2) issue_x1(c, 2, f_); else if (f_ < 4) issue_x1(c, 3, f_ - 2); else if (f_ < 7) issue_w1<1>(c, f_ - 4); aux0(f_); }); SYNCV(8);
        stage<G, 4, true, false, -1, -1>(st, [&](int f_) { if (f_ < 3) issue_w1<5>(c, f_); else if (f_ < 5) issue_x1(c, 4, f_ - 3); else if (f_ < 7) issue_x1(c, 5, f_ - 5); aux1(f_); }); SYNCV(9);
        stage<G, 1, true, true, -1, -1>(st, [&](int f_) { if (f_ >= 1 && f_ < 4) issue_w1<2>(c, f_ - 1); }); SYNCV(4);
        stage<G, 5, true, false, 4, -1>(st, [&](int f_) { if (f_ < 3) issue_w1<6>(c, f_); else if (f_ < 5) issue_x1(c, 6, f_ - 3); else if (f_ < 7) issue_x1(c, 7, f_ - 5); }); SYNCV(7);
        stage<G, 2, true, false, -1, -1>(st, [&](int f_) { if (f_ >= 1 && f_ < 4) issue_w1<3>(c, f_ - 1);
            if (f_ == 8) { aload16_sc1(gs, P.ws, (unsigned)(WS_ST + (128 * c.rb + 32 * c.tm + (opaque(lane) & 31)) * 16)); GATHER_ISSUE(gt1); } }); SYNCV(6);
        stage<G, 6, true, false, 6, -1>(st, [&](int f_) { if (f_ >= 1 && f_ < 4) issue_w1<7>(c, f_ - 1); else if (f_ >= 4 && f_ < 7) issue_w1<8>(c, f_ - 4); }); SYNCV(9);
        stage<G, 3, true, false, -1, -1>(st, [&](int f_) { if (f_ >= 1 && f_ < 4) issue_w1<9>(c, f_ - 1);
            if (f_ == 10) { WAITV(9); PIN(gs);
                if (!__all(gs[1] == tagLo && gs[3] == tagHi)) { const unsigned gso = (unsigned)(WS_ST + (128 * c.rb + 32 * c.tm + (opaque(lane) & 31)) * 16);
                    for (unsigned spins = 0;; ++spins) { __builtin_amdgcn_s_sleep(2); gs = __builtin_amdgcn_raw_buffer_load_b128(wsr, gso, 0, 16); __builtin_amdgcn_s_waitcnt(0x0F70);
                        if (__all(gs[1] == tagLo && gs[3] == tagHi)) break; if (spins > SPIN_CAP) { set_timeout(ws, lane); break; } } }
                ln_m = __uint_as_float(gs[0]); ln_a = __uint_as_float(gs[2]); PIN(ln_m); PIN(ln_a);
                GATHER_CHECK(gt1); }
            if (f_ == 11) GATHER_RED(gt1); });
        { lp xp = c.lds + 13 * GR + w * 2048 + c.vlin; *(LAS f32x4*)(xp) = F4(st.qb, 8 - 8 * G); *(LAS f32x4*)(xp + 1024) = F4(st.qb, 12 - 8 * G);
          if constexpr (G == 1) { if (h == 0) { *(LAS float*)(c.lds + L_CSW + c.tm * 256 + r32 * 4) = ln_m; *(LAS float*)(c.lds + L_CSW + c.tm * 256 + 128 + r32 * 4) = ln_a; } } }
        SYNC(3);
        issue_w<10>(c);
        { const int lane = opaque(c.lane), h = lane >> 5, r32 = lane & 31;
          const unsigned o = (unsigned)(WS_KV + ((size_t)c.cg * 64 + T) * 4096) + lane * 16 + G * 2048;
          f32x16 y;
          if constexpr (G == 0) {
#pragma unroll
              for (int q = 0; q < 4; ++q) { const f32x4 cs = ldsf4((lcp)c.lds + L_CS + (8 * q + 4 * h) * 4);
#pragma unroll
                  for (int i = 0; i < 4; ++i) y[4 * q + i] = ln_a * __builtin_fmaf(-ln_m, cs[i], st.qa[4 * q + i]); }
          } else {
              const float cso = *(const LAS float*)(c.lds + L_CS + 128 + r32 * 4);
#pragma unroll
              for (int q = 0; q < 4; ++q) { const f32x4 mm = ldsf4((lcp)c.lds + L_CSW + c.tm * 256 + (8 * q + 4 * h) * 4), aa = ldsf4((lcp)c.lds + L_CSW + c.tm * 256 + 128 + (8 * q + 4 * h) * 4);
#pragma unroll
                  for (int i = 0; i < 4; ++i) y[4 * q + i] = aa[i] * __builtin_fmaf(-mm[i], cso, st.qa[4 * q + i]); }
          }
          const u32x4 k0 = pack8(y, 0), k1 = pack8(y, 8);
          __builtin_amdgcn_raw_buffer_store_b128(k0, wsr, o, 0, 16); __builtin_amdgcn_raw_buffer_store_b128(k1, wsr, o + 1024, 0, 16);
          if (w == 0 && lane == 0) store_flag(ws, WS_SF + (size_t)blockIdx.x * 8, tag);
          *(LAS u32x4*)(c.lds + L_KVR + c.tm * 4096 + G * 2048 + lane * 16) = k0; *(LAS u32x4*)(c.lds + L_KVR + c.tm * 4096 + G * 2048 + 1024 + lane * 16) = k1;
          SB();
          const f32x4 c0 = ldsf4((lcp)c.lds + L_CS + 2 * 128 + (16 * G + 4 * h) * 4), c1 = ldsf4((lcp)c.lds + L_CS + 2 * 128 + (16 * G + 8 + 4 * h) * 4);
          const lcp xp = (lcp)c.lds + 13 * GR + (w ^ 4) * 2048 + lane * 16;
          const f32x4 p0 = ldsf4(xp), p1 = ldsf4(xp + 1024);
          const float sc = ln_a * (0.25f * 1.4426950408889634f);
#pragma unroll
          for (int i = 0; i < 4; ++i) { y[i] = sc * __builtin_fmaf(-ln_m, c0[i], st.qb[8 * G + i] + p0[i]); y[4 + i] = sc * __builtin_fmaf(-ln_m, c1[i], st.qb[8 * G + 4 + i] + p1[i]); }
          qf = __builtin_bit_cast(bf16x8, pack8(y, 0)); PIN(qf); }
        stage<G, 7, true, false, -1, 8>(st, [&](int) {}); SYNC(5);
        { const int ln = opaque(lane); c.vd = (ln >> 3) * 1024 + (((ln & 7) ^ (((ln >> 4) & 1) << 2)) * 16); }
        stage<G, 8, false, false, -1, 9>(st, [&](int f_) { if (f_ >= 1 && f_ < 4) issue_w1<11>(c, f_ - 1); if (f_ == 4) issue_d(c, 0); }); SYNCV(6);
        stage<G, 9, false, false, -1, 10>(st, [&](int f_) { if (f_ >= 1 && f_ < 4) issue_d(c, f_);
            if (f_ == 5) { WAITV(7); if (lane == 0) store_flag(ws, WS_KVF + ((size_t)(c.cg * 64 + T) * 2 + G) * 8, tag); }
            if (f_ == 8) GATHER_ISSUE(gt2); }); SYNCV(7);
        u64 kf_;
        stage<G, 10, false, false, -1, 11>(st, [&](int f_) { if (f_ >= 1 && f_ < 4) issue_d(c, 3 + f_);
            if (f_ == 6) { WAITV(3); GATHER_CHECK(gt2); GATHER_RED(gt2); }
            if (f_ == 10) { const int tl = 4 * c.rb - 8 + w; aload_flag(kf_, P.ws, (unsigned)(WS_KVF + ((c.cg * 64 + (tl < 0 ? 0 : tl)) * 2 + (opaque(lane) & 1)) * 8)); } }); SYNCV(63);
        stage<G, 11, false, false, -1, -1>(st, [&](int f_) { if (f_ >= 1 && f_ < 3) issue_d(c, 6 + f_);
            if (f_ == 6) { WAITV(2); asm volatile("" : "+v"(kf_)); const int tl = 4 * c.rb - 8 + w;
                if (tl >= 0) {
                    if (!__all(kf_ == tag)) poll_flags((const u64*)(ws + WS_KVF) + (c.cg * 64 + tl) * 2, 2, tag, lane, ws);
                    const char* base = ws + WS_KV + ((size_t)c.cg * 64 + tl) * 4096; const unsigned dst = c.lds0 + L_KV + w * 4096;
                    dma16_sc1(c.vlin, base, dst); dma16_sc1(c.vlin, base + 1024, dst + 1024); dma16_sc1(c.vlin, base + 2048, dst + 2048); dma16_sc1(c.vlin, base + 3072, dst + 3072); } } });
        {
          lp xp = c.lds + 2 * GR + w * 4096 + c.vlin;
          SYNCV(63);
          *(LAS f32x4*)(xp) = F4(st.b0, 8 - 8 * G); *(LAS f32x4*)(xp + 1024) = F4(st.b0, 12 - 8 * G); *(LAS f32x4*)(xp + 2048) = F4(st.b1, 8 - 8 * G); *(LAS f32x4*)(xp + 3072) = F4(st.b1, 12 - 8 * G); }
        SYNC(63);
        {
          const int lane = opaque(c.lane), h = lane >> 5;
          const float a2 = ln_a * ln_a;
          { u32x4 y0, y1;
#pragma unroll
            for (int q = 0; q < 4; ++q) {
                const f32x4 cg_ = ldsf4((lcp)c.lds + L_CS + (3 + 4 * G) * 128 + (8 * q + 4 * h) * 4), cu_ = ldsf4((lcp)c.lds + L_CS + (4 + 4 * G) * 128 + (8 * q + 4 * h) * 4);
                float y[4];
#pragma unroll
                for (int i = 0; i < 4; ++i) { const float g_ = fmaxf(__builtin_fmaf(-ln_m, cg_[i], st.a0[4 * q + i]), 0.f), u_ = __builtin_fmaf(-ln_m, cu_[i], st.a1[4 * q + i]); y[i] = (g_ * a2) * u_; }
                const unsigned lo = pk2(y[0], y[1]), hi = pk2(y[2], y[3]);
                if (q < 2) { y0[2 * q] = lo; y0[2 * q + 1] = hi; } else { y1[2 * q - 4] = lo; y1[2 * q - 3] = hi; }
            }
            if (G == 0 ? v0 : v2) { const unsigned o_ = (unsigned)(WS_C + ((size_t)T * NKS + 2 * (fs + 2 * G)) * 1024) + lane * 16;
                __builtin_amdgcn_raw_buffer_store_b128(y0, wsr, o_, 0, 16); __builtin_amdgcn_raw_buffer_store_b128(y1, wsr, o_ + 1024, 0, 16); } }
          { const lcp xp = (lcp)c.lds + 2 * GR + (w ^ 4) * 4096 + lane * 16;
            u32x4 y0;
#pragma unroll
            for (int q = 0; q < 2; ++q) {
                const f32x4 pg = ldsf4(xp + q * 1024), pu = ldsf4(xp + 2048 + q * 1024);
                const f32x4 cg_ = ldsf4((lcp)c.lds + L_CS + 5 * 128 + (16 * G + 8 * q + 4 * h) * 4), cu_ = ldsf4((lcp)c.lds + L_CS + 6 * 128 + (16 * G + 8 * q + 4 * h) * 4);
                float y[4];
#pragma unroll
                for (int i = 0; i < 4; ++i) { const float g_ = fmaxf(__builtin_fmaf(-ln_m, cg_[i], st.b0[8 * G + 4 * q + i] + pg[i]), 0.f), u_ = __builtin_fmaf(-ln_m, cu_[i], st.b1[8 * G + 4 * q + i] + pu[i]); y[i] = (g_ * a2) * u_; }
                y0[2 * q] = pk2(y[0], y[1]); y0[2 * q + 1] = pk2(y[2], y[3]);
            }
            __builtin_amdgcn_raw_buffer_store_b128(y0, wsr, (unsigned)(WS_C + ((size_t)T * NKS + 2 * (fs + 1) + G) * 1024) + lane * 16, 0, 16); } }
        if (G == 0 ? v0 : v2) { SYNC(3); } else { SYNC(1); }
        if constexpr (G == 1) __builtin_amdgcn_s_setprio(1);
    };
    if (c.g == 0) gemm1(std::integral_constant<int, 0>{}); else gemm1(std::integral_constant<int, 1>{});
    const int lane2 = opaque(lane);
    const unsigned cbase = (unsigned)(WS_C + ((size_t)T2 * NKS + kq) * 1024) + lane2 * 16;
    bf16x8 hf[22];
    u64 hfl; bool hrdy_ = false;
#define LDH(i) do { const bool ok_ = ((i) < 21) || (kq < 2); hf[i] = __builtin_bit_cast(bf16x8, __builtin_amdgcn_raw_buffer_load_b128(wsr, ok_ ? cbase + (i) * 4096 : cbase, 0, 16)); } while (0)
    {
        const int lane = lane2, h = lane >> 5, r32 = lane & 31;
        const int i0 = T >= 8 ? 0 : 8 - T;
        const unsigned kb = c.lds0 + L_KV + c.tm * 4096 + c.g * 1024 + lane * 16;
        const unsigned lanebit = r32 < 16 ? 1u : 0u;
        const unsigned vb = c.lds0 + (r32 < 16 ? (unsigned)(((r32 & 15) + 16 * c.g + 32 * h) * 16) : (unsigned)L_ONES);
        auto koff = [&](int i) -> unsigned { return (unsigned)((i >= i0 ? i : 7) * 4096); };
        auto rdk = [&](int i) -> bf16x8 { return ldsf((lcp)(uintptr_t)(kb + koff(i))); };
        auto vaddr = [&](int i) -> unsigned { const bool valid = i >= i0; const unsigned d = valid ? 0u : (unsigned)(L_ZERO - L_ONES), A = valid ? (unsigned)(L_KV + 2048 + (c.tm + i) * 4096) : (unsigned)L_ZERO;
                                              return lanebit * (A - d) + (vb + d); };
        auto dmask = [&](int r) -> bool { u64 mk = 0;
#pragma unroll
            for (int l = 0; l < 64; ++l) { const int kl = (r & 3) + 8 * (r >> 2) + 4 * (l >> 5); if (kl <= (l & 31)) mk |= 1ull << l; }
            return __builtin_amdgcn_inverse_ballot_w64(mk); };
        const f32x16 zero = f32x16{};
        float m0 = -1e30f, m1 = -1e30f;
#define MAXT(S) do { _Pragma("unroll") for (int r = 0; r < 16; r += 4) { m0 = fmaxf(fmaxf(m0, S[r]), S[r + 1]); m1 = fmaxf(fmaxf(m1, S[r + 2]), S[r + 3]); } PIN(m0); PIN(m1); } while (0)
#define EXPT(S) do { _Pragma("unroll") for (int r = 0; r < 16; ++r) S[r] = __builtin_amdgcn_exp2f(S[r]); } while (0)
#define H_PUBLISH(N) do { WAITV(N); if (lane == 0) store_flag(ws, WS_HF + ((size_t)T * 32 + 2 * c.cg + c.g) * 8, tag); } while (0)
#define H_FLAGS() aload_flag(hfl, P.ws, (unsigned)(WS_HF + (T2 * 32 + (lane & 31)) * 8))
        f32x16 o = f32x16{};
        float m; bool hrdy = false;
        if (T == 0) {
            f32x16 s = mfma(ldsf((lcp)(uintptr_t)(kb + 8 * 4096)), qf, zero);
#pragma unroll
            for (int r = 0; r < 16; ++r) s[r] = dmask(r) ? s[r] : -1e30f;
            MAXT(s);
            m = fmaxf(m0, m1);
            { auto rr = __builtin_amdgcn_permlane32_swap(__float_as_uint(m), __float_as_uint(m), false, false); m = fmaxf(__uint_as_float(rr[0]), __uint_as_float(rr[1])); }
#pragma unroll
            for (int r = 0; r < 16; ++r) s[r] = __builtin_amdgcn_exp2f(s[r] - m);
            const unsigned va = vaddr(8);
            o = mfma(ldsf((lcp)(uintptr_t)va), __builtin_bit_cast(bf16x8, pack8(s, 0)), o);
            o = mfma(ldsf((lcp)(uintptr_t)(va + 1024)), __builtin_bit_cast(bf16x8, pack8(s, 8)), o);
            issue_d(c, 9); issue_d(c, 10); issue_d(c, 11); issue_d(c, 12);
            H_PUBLISH(4);
        } else {
            bf16x8 k0 = rdk(0), k8 = rdk(8), k1 = rdk(1), k2 = rdk(2), k3 = rdk(3);
            f32x16 sA, sB, sC;
            sA = mfma_pin0(k0, qf); sB = mfma_pin0(k8, qf); SB();
            k0 = rdk(4); k8 = rdk(5); sC = mfma_pin0(k1, qf); k1 = rdk(6); SB();
            asm volatile("s_nop 7\n\ts_nop 3"); SB();
#pragma unroll
            for (int r = 0; r < 16; ++r) sA[r] = dmask(r) ? sB[r] : sA[r];
            PIN(sA); SB();
            sB = mfma_pin0(k2, qf); k2 = rdk(7); SB();
            MAXT(sA); SB();
            H_PUBLISH(0);
            sA = mfma_pin0(k3, qf); issue_d(c, 9); SB(); MAXT(sC); SB();
            sC = mfma_pin0(k0, qf); issue_d(c, 10); SB(); MAXT(sB); SB();
            sB = mfma_pin0(k8, qf); issue_d(c, 11); SB(); MAXT(sA); SB();
            sA = mfma_pin0(k1, qf); issue_d(c, 12); SB(); MAXT(sC); SB();
            sC = mfma_pin0(k2, qf); k0 = rdk(0); k8 = rdk(8); k1 = rdk(1); SB(); MAXT(sB); SB();
            MAXT(sA); SB(); MAXT(sC);
            m = fmaxf(m0, m1);
            { auto rr = __builtin_amdgcn_permlane32_swap(__float_as_uint(m), __float_as_uint(m), false, false); m = fmaxf(__uint_as_float(rr[0]), __uint_as_float(rr[1])); }
            f32x16 cm;
#pragma unroll
            for (int r = 0; r < 16; ++r) cm[r] = -m;
            PIN(cm);
            asm volatile("s_nop 1"); sA = mfma_pin(k0, qf, cm); sB = mfma_pin(k8, qf, cm); sC = mfma_pin(k1, qf, cm); SB();
            bf16x8 va0, va1, vb0, vb1;
            { const unsigned a0 = vaddr(0), a8 = vaddr(8); va0 = ldsf((lcp)(uintptr_t)a0); va1 = ldsf((lcp)(uintptr_t)(a0 + 1024)); vb0 = ldsf((lcp)(uintptr_t)a8); vb1 = ldsf((lcp)(uintptr_t)(a8 + 1024)); }
            k0 = rdk(2);
            unsigned mp[8];
#pragma unroll
            for (int j = 0; j < 8; ++j) mp[j] = (dmask(2 * j) ? 0xffffu : 0u) | (dmask(2 * j + 1) ? 0xffff0000u : 0u);
            SB(); asm volatile("s_nop 7"); SB();
#pragma unroll
            for (int r = 0; r < 16; ++r) sA[r] = dmask(r) ? sB[r] : sA[r];
            EXPT(sA);
            { const u32x4 p0 = pack8(sA, 0), p1 = pack8(sA, 8); u32x4 d0, d1, o0, o1;
#pragma unroll
              for (int j = 0; j < 4; ++j) { d0[j] = p0[j] & mp[j]; d1[j] = p1[j] & mp[4 + j]; o0[j] = p0[j] & ~mp[j]; o1[j] = p1[j] & ~mp[4 + j]; }
              o = mfma(va0, __builtin_bit_cast(bf16x8, o0), o); o = mfma(va1, __builtin_bit_cast(bf16x8, o1), o);
              o = mfma(vb0, __builtin_bit_cast(bf16x8, d0), o); o = mfma(vb1, __builtin_bit_cast(bf16x8, d1), o); }
            { const unsigned a1 = vaddr(1), a2 = vaddr(2); va0 = ldsf((lcp)(uintptr_t)a1); va1 = ldsf((lcp)(uintptr_t)(a1 + 1024)); vb0 = ldsf((lcp)(uintptr_t)a2); vb1 = ldsf((lcp)(uintptr_t)(a2 + 1024)); }
            SB();
#define EXP8(S, b) do { _Pragma("unroll") for (int r = (b); r < (b) + 8; ++r) S[r] = __builtin_amdgcn_exp2f(S[r]); } while (0)
#define PK4(S, b) (u32x4){pk2(S[b], S[(b) + 1]), pk2(S[(b) + 2], S[(b) + 3]), pk2(S[(b) + 4], S[(b) + 5]), pk2(S[(b) + 6], S[(b) + 7])}
#define TILE2(i, S, V0, V1, SN) do { \
                if ((i) + 1 <= 7) { SN = mfma_pin(k0, qf, cm); SB(); if ((i) + 2 <= 7) k0 = rdk((i) + 2); } \
                EXP8(S, 0); { u32x4 p_ = PK4(S, 0); PIN(p_); SB(); o = mfma(V0, __builtin_bit_cast(bf16x8, p_), o); SB(); } \
                EXP8(S, 8); { u32x4 p_ = PK4(S, 8); PIN(p_); SB(); o = mfma(V1, __builtin_bit_cast(bf16x8, p_), o); SB(); } \
                if ((i) + 2 <= 7) { const unsigned a_ = vaddr((i) + 2); V0 = ldsf((lcp)(uintptr_t)a_); V1 = ldsf((lcp)(uintptr_t)(a_ + 1024)); } SB(); } while (0)
            TILE2(1, sC, va0, va1, sB); TILE2(2, sB, vb0, vb1, sC);
            H_FLAGS();
            TILE2(3, sC, va0, va1, sB); TILE2(4, sB, vb0, vb1, sC);
            WAITV(0); asm volatile("" : "+v"(hfl));
            hrdy = __all(hfl == tag);
            if (hrdy) { LDH(0); LDH(1); LDH(2); LDH(3); LDH(4); LDH(5); LDH(6); }
            TILE2(5, sC, va0, va1, sB);
            if (hrdy) { LDH(7); LDH(8); LDH(9); LDH(10); LDH(11); LDH(12); LDH(13); }
            TILE2(6, sB, vb0, vb1, sC);
            if (hrdy) { LDH(14); LDH(15); LDH(16); LDH(17); LDH(18); LDH(19); LDH(20); LDH(21); }
            TILE2(7, sC, va0, va1, sB);
        }
        const float inv = __builtin_amdgcn_rcpf(o[8]);
        f32x16 y;
#pragma unroll
        for (int r = 0; r < 8; ++r) y[r] = o[r] * inv;
        const unsigned oo = (unsigned)(WS_C + ((size_t)T * NKS + 86 + 2 * c.cg + c.g) * 1024) + lane * 16;
        const u32x4 ov = pack8(y, 0);
        __builtin_amdgcn_raw_buffer_store_b128(ov, wsr, oo, 0, 16);
        if (!hrdy) {
            poll_flags((const u64*)(ws + WS_HF) + T2 * 32, 32, tag, lane, ws);
#pragma unroll
            for (int i = 0; i < 22; ++i) LDH(i);
        }
        hrdy_ = hrdy;
        __builtin_amdgcn_s_waitcnt(0x0F71);
        __builtin_amdgcn_s_setprio(0);
    }
    {
        const int lane = opaque(lane2), h = lane >> 5, r32 = lane & 31, q4 = (lane & 15) >> 2, p4 = lane & 3, b16 = (lane >> 4) & 1;
        f32x16 acc0 = f32x16{}, acc1 = f32x16{};
        const unsigned wl = c.lds0 + kq * 2048 + (4 * h + q4) * 128 + 32 * b16 + 8 * p4, swz = ((q4 >> 1) & 1) << 6;
        const unsigned wl0 = wl + (0u ^ swz), wl1 = wl + (64u ^ swz);
        bf16x8 wa[3], wb_[3];
#define RDW(u) do { wa[(u) % 3] = trfrag<1024>((lcp)(uintptr_t)(wl0 + dloc(u))); wb_[(u) % 3] = trfrag<1024>((lcp)(uintptr_t)(wl1 + dloc(u))); } while (0)
#define G2STEP(u, frag, nxt) do { acc0 = mfma(wa[(u) % 3], frag, acc0); acc1 = mfma(wb_[(u) % 3], frag, acc1); SB(); if ((nxt) >= 0) RDW((nxt) < 0 ? 0 : (nxt)); SB(); } while (0)
        SYNC(63);
        const bool ew = blockIdx.x == 0 && w == 7; u32x4 sf0 = {0u, 0u, 0u, 0u}, sf1 = sf0; bool edone = false;
        if (ew) { aload16_sc1(sf0, P.ws + WS_SF, (unsigned)(lane * 32)); aload16_sc1(sf1, P.ws + WS_SF + 16, (unsigned)(lane * 32)); }
        RDW(0); RDW(1); RDW(2);
        G2STEP(0, hf[0], 3);
        issue_d(c, 13); G2STEP(1, hf[1], 4); issue_d(c, 14); G2STEP(2, hf[2], 5); issue_d(c, 15); G2STEP(3, hf[3], 6); issue_d(c, 16); G2STEP(4, hf[4], 7); issue_d(c, 17); G2STEP(5, hf[5], 8); issue_d(c, 18);
        if (hrdy_) { WAITV(6); }
        if (lane == 0) store_flag(ws, WS_OF + ((size_t)T * 32 + 2 * c.cg + c.g) * 8, tag);
        SYNCV(63);
        G2STEP(6, hf[6], 9); issue_d(c, 19); G2STEP(7, hf[7], 10); issue_d(c, 20); G2STEP(8, hf[8], 11); issue_d(c, 21); G2STEP(9, hf[9], 12); issue_d(c, 22);
        G2STEP(10, hf[10], -1); issue_d(c, 23); G2STEP(11, hf[11], -1); issue_d(c, 24); G2STEP(12, hf[12], -1);
        SYNCV(7);
        if (ew) { asm volatile("" : "+v"(sf0), "+v"(sf1));
            edone = __all(sf0[0] == tagLo && sf0[1] == tagHi && sf0[2] == tagLo && sf0[3] == tagHi && sf1[0] == tagLo && sf1[1] == tagHi && sf1[2] == tagLo && sf1[3] == tagHi);
            if (edone && lane == 0) store_flag(ws, WS_EPOCH, tag); }
        edone_ = edone;
        RDW(13); RDW(14); RDW(15);
        u32x4 xres;
        u64 ofl;
        issue_d(c, 25); G2STEP(13, hf[13], 16); issue_d(c, 26); G2STEP(14, hf[14], 17); issue_d(c, 27); G2STEP(15, hf[15], 18); issue_d(c, 28);
        aload_flag(ofl, P.ws, (unsigned)(WS_OF + (T2 * 32 + (lane & 31)) * 8));
        G2STEP(16, hf[16], -1); issue_d(c, 29);
        xres = *(const u32x4*)(P.x + (size_t)(64 * c.tb + 8 * w + (lane >> 3)) * 512 + 64 * c.nb + (lane & 7) * 8);
        G2STEP(17, hf[17], -1); G2STEP(18, hf[18], -1);
        WAITV(2); asm volatile("" : "+v"(ofl));
        if (!__all(ofl == tag)) poll_flags((const u64*)(ws + WS_OF) + T2 * 32, 32, tag, lane, ws);
        bf16x8 of[8];
#pragma unroll
        for (int i = 0; i < 8; ++i) of[i] = __builtin_bit_cast(bf16x8, __builtin_amdgcn_raw_buffer_load_b128(wsr, cbase + (86 + 4 * i) * 1024, 0, 16));
        SYNCV(63);
        RDW(19); RDW(20); RDW(21);
        G2STEP(19, hf[19], 22); G2STEP(20, hf[20], 23);
        if (kq < 2) { acc0 = mfma(wa[21 % 3], hf[21], acc0); acc1 = mfma(wb_[21 % 3], hf[21], acc1); }
        SB(); RDW(24); SB();
        G2STEP(22, of[0], -1); G2STEP(23, of[1], -1); G2STEP(24, of[2], -1);
        SYNCV(63);
        RDW(25); RDW(26); RDW(27);
        G2STEP(25, of[3], 28); G2STEP(26, of[4], 29); G2STEP(27, of[5], -1); G2STEP(28, of[6], -1); G2STEP(29, of[7], -1);
        { lp rbp = c.lds + (kq < 2 ? 4 * GR : 13 * GR - 32768) + (kq * 2 + th) * 8192;
#pragma unroll
          for (int q = 0; q < 4; ++q) { *(LAS f32x4*)(rbp + q * 1024 + ((lane + 2 * q) & 63) * 16) = F4(acc0, 4 * q); *(LAS f32x4*)(rbp + 4096 + q * 1024 + ((lane + 2 * (4 + q)) & 63) * 16) = F4(acc1, 4 * q); } }
        SYNC(63);
        {
          const int t = 8 * w + (lane >> 3), cc = lane & 7;
          const unsigned a0 = (unsigned)((t >> 5) * 8192 + cc * 1024), s0i = (unsigned)(((t & 31) + 2 * cc) & 63) * 16, s1i = (unsigned)(((t & 31) + 32 + 2 * cc) & 63) * 16;
          f32x4 s0 = {0.f, 0.f, 0.f, 0.f}, s1 = s0;
#pragma unroll
          for (int k = 0; k < 4; ++k) { const lcp bp = (lcp)c.lds + (k < 2 ? 4 * GR : 13 * GR - 32768) + k * 16384 + a0; s0 += ldsf4(bp + s0i); s1 += ldsf4(bp + s1i); }
          u32x4 ov;
          ov[0] = pk2(s0[0] + blo(xres[0]), s0[1] + bhi(xres[0])); ov[1] = pk2(s0[2] + blo(xres[1]), s0[3] + bhi(xres[1]));
          ov[2] = pk2(s1[0] + blo(xres[2]), s1[1] + bhi(xres[2])); ov[3] = pk2(s1[2] + blo(xres[3]), s1[3] + bhi(xres[3]));
          *(u32x4*)(P.out + (size_t)(64 * c.tb + t) * 512 + 64 * c.nb + cc * 8) = ov; }
    }
    if (blockIdx.x == 0 && w == 7 && !edone_) {
        const GAS u64* pp = (const GAS u64*)(ws + WS_SF) + lane * 4;
        for (unsigned spins = 0;; ++spins) { bool ok = true;
#pragma unroll
            for (int j = 0; j < 4; ++j) ok &= __hip_atomic_load(pp + j, __ATOMIC_RELAXED, __HIP_MEMORY_SCOPE_AGENT) == tag;
            if (__all(ok)) break; if (spins > SPIN_CAP) { set_timeout(ws, lane); break; } __builtin_amdgcn_s_sleep(2); }
        if (lane == 0) store_flag(ws, WS_EPOCH, tag);
    }
}

extern "C" void kernel_launch(void* const* d_in, const int* in_sizes, int n_in, void* d_out, int out_size, void* d_ws, size_t ws_size, hipStream_t stream) {
    Params p;
    p.x = (const u16*)d_in[0]; p.wqkv = (const u16*)d_in[1]; p.wo = (const u16*)d_in[2];
    p.wg = (const u16*)d_in[4]; p.wu = (const u16*)d_in[5]; p.wd = (const u16*)d_in[6];
    p.out = (u16*)d_out; p.ws = (char*)d_ws;
    (void)hipFuncSetAttribute((const void*)fused_kernel, hipFuncAttributeMaxDynamicSharedMemorySize, LDS_BYTES);
    fused_kernel<<<256, 512, LDS_BYTES, stream>>>(p);
}
```

```cpp
#include <hip/hip_runtime.h>
#include <stdint.h>
#include <type_traits>
#define DTHR 150.f

#define FI __device__ __forceinline__
#define LAS __attribute__((address_space(3)))
#define GAS __attribute__((address_space(1)))
typedef unsigned short u16;
typedef unsigned long long u64;
typedef short bf16x8 __attribute__((ext_vector_type(8)));
typedef short s16x4 __attribute__((ext_vector_type(4)));
typedef float f32x16 __attribute__((ext_vector_type(16)));
typedef float f32x4 __attribute__((ext_vector_type(4)));
typedef float f32x2 __attribute__((ext_vector_type(2)));
typedef unsigned u32x4 __attribute__((ext_vector_type(4)));
typedef __bf16 bf16x2_t __attribute__((ext_vector_type(2)));
typedef LAS const char* lcp;
typedef LAS char* lp;

constexpr int GR = 8192;
constexpr int L_KV = 13 * GR;
constexpr int L_KVR = 17 * GR;
constexpr int L_MISC = 19 * GR;
constexpr int L_CS = L_MISC;
constexpr int L_CSW = L_MISC + 2048;
constexpr int L_ZERO = L_MISC + 4096;
constexpr int L_ONES = L_MISC + 6144;
constexpr int LDS_BYTES = 163840;

constexpr size_t WS_EPOCH = 0, WS_TMO = 64;
constexpr size_t WS_SF = 4096;
constexpr size_t WS_KVF = 8192;
constexpr size_t WS_HF = 24576;
constexpr size_t WS_OF = 40960;
constexpr size_t WS_ST = 65536;
constexpr size_t WS_CS = 131072;
constexpr size_t WS_SCR = 768u << 10;
constexpr size_t WS_KV = 2u << 20;
constexpr size_t WS_C = 8u << 20;
constexpr int NKS = 118;

FI f32x16 mfma(bf16x8 a, bf16x8 b, f32x16 c) { return __builtin_amdgcn_mfma_f32_32x32x16_bf16(a, b, c, 0, 0, 0); }
FI f32x16 mfma_pin(bf16x8 a, bf16x8 b, const f32x16& c) { f32x16 d; asm volatile("v_mfma_f32_32x32x16_bf16 %0, %1, %2, %3" : "=&v"(d) : "v"(a), "v"(b), "v"(c)); return d; }
FI f32x16 mfma_pin0(bf16x8 a, bf16x8 b) { f32x16 d; asm volatile("v_mfma_f32_32x32x16_bf16 %0, %1, %2, 0" : "=&v"(d) : "v"(a), "v"(b)); return d; }
FI unsigned pk2(float lo, float hi) { f32x2 v = {lo, hi}; bf16x2_t r = __builtin_convertvector(v, bf16x2_t); return __builtin_bit_cast(unsigned, r); }
FI u32x4 pack8(const f32x16& y, int b) { u32x4 r; r[0] = pk2(y[b], y[b + 1]); r[1] = pk2(y[b + 2], y[b + 3]); r[2] = pk2(y[b + 4], y[b + 5]); r[3] = pk2(y[b + 6], y[b + 7]); return r; }
typedef short v4i16_t __attribute__((ext_vector_type(4)));
FI s16x4 trd(lcp p) { return __builtin_bit_cast(s16x4, __builtin_amdgcn_ds_read_tr16_b64_v4i16((LAS v4i16_t*)p)); }
template <int D2> FI bf16x8 trfrag(lcp p) { s16x4 lo = trd(p), hi = trd(p + D2); return __builtin_shufflevector(lo, hi, 0, 1, 2, 3, 4, 5, 6, 7); }
FI bf16x8 ldsf(lcp p) { return *(const LAS bf16x8*)p; }
FI f32x4 ldsf4(lcp p) { return *(const LAS f32x4*)p; }

FI void dma16(unsigned voff, const void* sbase, unsigned dst) {
    asm volatile("s_mov_b32 m0, %2\n\ts_nop 0\n\tglobal_load_lds_dwordx4 %0, %1" :: "v"(voff), "s"(sbase), "s"(__builtin_amdgcn_readfirstlane(dst)) : "memory", "m0");
}
FI void dma16_sc1(unsigned voff, const void* sbase, unsigned dst) {
    asm volatile("s_mov_b32 m0, %2\n\ts_nop 0\n\tglobal_load_lds_dwordx4 %0, %1 sc1" :: "v"(voff), "s"(sbase), "s"(__builtin_amdgcn_readfirstlane(dst)) : "memory", "m0");
}
FI void aload16(u32x4& v, const void* base, unsigned voff) { asm volatile("global_load_dwordx4 %0, %1, %2" : "=v"(v) : "v"(voff), "s"(base) : "memory"); }
FI void aload16_sc1(u32x4& v, const void* base, unsigned voff) { asm volatile("global_load_dwordx4 %0, %1, %2 sc1" : "=v"(v) : "v"(voff), "s"(base) : "memory"); }
FI void aload_flag(u64& v, const char* base, unsigned voff) { asm volatile("global_load_dwordx2 %0, %1, %2 sc1" : "=v"(v) : "v"(voff), "s"(base) : "memory"); }

#define SB() __builtin_amdgcn_sched_barrier(0)
#define PIN(x) asm volatile("" : "+v"(x))
FI int opaque(int v) { asm volatile("" : "+v"(v)); return v; }
#define SYNC(N) do { SB(); asm volatile("s_waitcnt vmcnt(" #N ") lgkmcnt(0)\n\ts_barrier" ::: "memory"); SB(); } while (0)
#define SYNCV(N) do { SB(); asm volatile("s_waitcnt vmcnt(" #N ")\n\ts_barrier" ::: "memory"); SB(); } while (0)
#define WAITV(N) do { SB(); asm volatile("s_waitcnt vmcnt(" #N ")" ::: "memory"); SB(); } while (0)

struct Params {
    const u16* x; const u16* wqkv; const u16* wo; const u16* wg; const u16* wu; const u16* wd; u16* out; char* ws;
};

constexpr int wslot(int s) { return (s == 0 ? 4 : s == 1 ? 14 : s == 2 ? 7 : s == 3 ? 0 : s == 4 ? 7 : s == 5 ? 0 : s == 6 ? 10 : s == 7 ? 3 : s == 8 ? 6 : s == 9 ? 9 : s == 10 ? 0 : 3) * GR; }
constexpr int xslot(int c) { return (c == 0 ? 0 : c == 1 ? 2 : c == 2 ? 10 : c == 3 ? 12 : c == 4 ? 3 : c == 5 ? 5 : c == 6 ? 13 : 15) * GR; }
FI constexpr int dloc0(int u) { return (u == 0 ? 12 : u < 4 ? 6 + (u - 1) : u < 7 ? 9 + (u - 4) : u < 9 ? u - 7 : 2 + (u - 9)) * GR; }
FI constexpr int dloc(int u) { return u < 13 ? dloc0(u) : u < 19 ? L_KV + (u - 13) * GR : dloc0(u - 19); }

struct Ctx {
    const Params* p;
    unsigned lds0;
    lp lds;
    int lane, w, tm, g, rb, cg, tb, nb;
    unsigned vq, vf0, vf1, vf2, vx, vlin, vd;
    const char *px, *pq, *pf, *pwd, *pwo;
};

FI void issue_x1(const Ctx& c, int chunk, int k) {
    dma16(k ? c.vx ^ 64u : c.vx, c.px + chunk * 128 + k * 8 * 1024, c.lds0 + xslot(chunk) + 16 * c.w * 128 + k * 8 * 128);
}
FI void issue_x(const Ctx& c, int chunk) { issue_x1(c, chunk, 0); issue_x1(c, chunk, 1); }
template <int S> FI void issue_w1(const Ctx& c, int k) {
    if constexpr (S >= 4) {
        const char* base = c.pf + (size_t)(S - 4) * (64 * 2752);
        dma16(k == 0 ? c.vf0 : k == 1 ? c.vf1 : c.vf2, base, c.lds0 + wslot(S) + c.g * 12288 + c.tm * 3072 + k * 1024);
    } else {
        const char* base = c.pq + (size_t)S * (128 * 3072);
        dma16(c.vq, base + (k == 0 ? 1024 : k == 1 ? 2048 : 0), c.lds0 + wslot(S) + c.w * 1024 + k * 8192);
    }
}
template <int S> FI void issue_w(const Ctx& c) { issue_w1<S>(c, 0); issue_w1<S>(c, 1); issue_w1<S>(c, 2); }
FI void issue_d(const Ctx& c, int u) {
    const char* base;
    if (u < 21) base = c.pwd + (size_t)u * 65536;
    else if (u == 21) base = c.pwd + (size_t)21 * 65536 - (c.w >= 4 ? 32768 : 0);
    else base = c.pwo + (size_t)(u - 22) * 65536;
    dma16(c.vd, base, c.lds0 + dloc(u) + c.w * 1024);
}

FI float blo(unsigned d) { return __uint_as_float(d << 16); }
FI float bhi(unsigned d) { return __uint_as_float(d & 0xffff0000u); }
template <int CTRL> FI float dppadd(float x) { return x + __builtin_bit_cast(float, __builtin_amdgcn_update_dpp(0, __builtin_bit_cast(int, x), CTRL, 0xf, 0xf, true)); }
FI void swap32add(float& u, float v) { auto rr = __builtin_amdgcn_permlane32_swap(__float_as_uint(u), __float_as_uint(v), false, false); u = __uint_as_float(rr[0]) + __uint_as_float(rr[1]); }
FI void swap16add(float& u, float v) { auto rr = __builtin_amdgcn_permlane16_swap(__float_as_uint(u), __float_as_uint(v), false, false); u = __uint_as_float(rr[0]) + __uint_as_float(rr[1]); }

struct G1State {
    bf16x8 xf[32];
    bf16x8 wq[4];
    f32x16 a0, a1, b0, b1;
    f32x16 qa, qb;
    unsigned wbf[4], wbq[4];
    unsigned xb[4];
};
constexpr int fkind(int G, int f) { return G == 0 ? (f < 8 ? (f & 3) : (f & 1)) : (f < 4 ? (f & 1) : f < 6 ? (f & 1) : f < 8 ? 2 + (f & 1) : f < 10 ? (f & 1) : 2 + (f & 1)); }
constexpr int fpos(int G, int f) { return G == 0 ? (f < 8 ? (f >> 2) : 2 + ((f - 8) >> 1)) : (f < 4 ? (f >> 1) : f < 8 ? 2 : 3); }
constexpr int qkind(int G, int f) { return G == 0 ? ((f == 1 || f == 3 || f == 7 || f == 9) ? 1 : 0) : ((f == 3 || f == 5 || f == 9 || f == 11) ? 1 : 0); }
constexpr int qpos(int G, int f) {
    return G == 0 ? (f == 0 ? 0 : f == 1 ? 0 : f == 2 ? 1 : f == 3 ? 1 : f == 4 ? 2 : f == 5 ? 3 : f == 6 ? 4 : f == 7 ? 4 : f == 8 ? 5 : f == 9 ? 5 : f == 10 ? 6 : 7)
                  : (f == 0 ? 0 : f == 1 ? 1 : f == 2 ? 2 : f == 3 ? 2 : f == 4 ? 3 : f == 5 ? 3 : f == 6 ? 4 : f == 7 ? 5 : f == 8 ? 6 : f == 9 ? 6 : f == 10 ? 7 : 7);
}
template <int G, int S> FI bf16x8 rdfrag(const G1State& st, int f) {
    if constexpr (S >= 4) { const int k = fkind(G, f); return trfrag<768>((lcp)(uintptr_t)(st.wbf[fpos(G, f)] + (k & 1) * 12288 + (k >= 2 ? 64 : (G == 0 ? 0 : 128)))); }
    else { const int e = qpos(G, f); return trfrag<256>((lcp)(uintptr_t)(st.wbq[e & 3] + (e >> 2) * 4096 + (qkind(G, f) ? 16384 : G * 8192))); }
}
template <int C> FI void rdx(G1State& st) {
#pragma unroll
    for (int e = 0; e < 4; ++e) { st.xf[4 * C + e] = ldsf((lcp)(uintptr_t)st.xb[e]); if (C < 7) st.xb[e] = (unsigned)opaque((int)(st.xb[e] + (xslot(C + 1) - xslot(C)))); }
}
template <int G, int S, bool FRESH, bool XF, int XP, int NX, class F>
FI void stage(G1State& st, F&& hook) {
    if constexpr (XF) rdx<(XF ? 2 * S : 0)>(st);
    if constexpr (FRESH) {
#pragma unroll
        for (int f = 0; f < 4; ++f) st.wq[f] = rdfrag<G, S>(st, f);
    }
    if constexpr (XF) rdx<(XF ? 2 * S + 1 : 0)>(st);
    SB();
#pragma unroll
    for (int f = 0; f < 12; ++f) {
        const bf16x8 w = st.wq[f & 3];
        if constexpr (S >= 4) {
            const int k = fkind(G, f); const bf16x8 x = st.xf[4 * (S - 4) + fpos(G, f)];
            if (k == 0) st.a0 = mfma(w, x, st.a0); else if (k == 1) st.a1 = mfma(w, x, st.a1); else if (k == 2) st.b0 = mfma(w, x, st.b0); else st.b1 = mfma(w, x, st.b1);
        } else {
            const bf16x8 x = st.xf[8 * S + qpos(G, f)];
            if (qkind(G, f)) st.qb = mfma(w, x, st.qb); else if (G == 0) st.qa = mfma(w, x, st.qa); else st.qa = mfma(x, w, st.qa);
        }
        SB();
        if (f < 8) st.wq[f & 3] = rdfrag<G, S>(st, f + 4);
        else {
            if (f == 8) {
                if constexpr (S < 3) { _Pragma("unroll") for (int j = 0; j < 4; ++j) st.wbq[j] = (unsigned)opaque((int)(st.wbq[j] + (wslot(S + 1) - wslot(S)))); }
                if constexpr (S >= 4 && S < 11) { _Pragma("unroll") for (int j = 0; j < 4; ++j) st.wbf[j] = (unsigned)opaque((int)(st.wbf[j] + (wslot(S + 1) - wslot(S)))); }
            }
            if constexpr (NX >= 0) st.wq[f & 3] = rdfrag<G, (NX >= 0 ? NX : 0)>(st, f - 8);
        }
        if constexpr (XP >= 0) { if (f == 2) rdx<(XP >= 0 ? XP : 0)>(st); if (f == 6) rdx<(XP >= 0 ? XP + 1 : 0)>(st); }
        hook(f);
        SB();
    }
}

FI u64 mix64(u64 z) { z += 0x9E3779B97F4A7C15ull; z = (z ^ (z >> 30)) * 0xBF58476D1CE4E5B9ull; z = (z ^ (z >> 27)) * 0x94D049BB133111EBull; return z ^ (z >> 31); }

constexpr unsigned SPIN_CAP = 1u << 18;
FI void set_timeout(char* ws, int lane) { if (lane == 0) __hip_atomic_store((GAS unsigned*)(ws + WS_TMO), 1u, __ATOMIC_RELAXED, __HIP_MEMORY_SCOPE_AGENT); }
FI void poll_flags(const u64* flags, int n, u64 tag, int lane, char* ws) {
    const GAS u64* pp = (const GAS u64*)flags + (lane < n ? lane : 0);
    for (unsigned spins = 0;; ++spins) {
        const u64 v = __hip_atomic_load(pp, __ATOMIC_RELAXED, __HIP_MEMORY_SCOPE_AGENT);
        if (__all(v == tag)) return;
        if (spins > SPIN_CAP) { set_timeout(ws, lane); return; }
        __builtin_amdgcn_s_sleep(2);
    }
}
FI void store_flag(char* ws, size_t off, u64 tag) { __hip_atomic_store((GAS u64*)(ws + off), tag, __ATOMIC_RELAXED, __HIP_MEMORY_SCOPE_AGENT); }

__global__ __launch_bounds__(512, 2) void fused_kernel(Params P) {
    extern __shared__ __attribute__((aligned(16))) char smem[];
    Ctx c; c.p = &P;
    const int tid = threadIdx.x; c.lane = tid & 63;
    const int w = __builtin_amdgcn_readfirstlane(tid >> 6);
    c.w = w; c.tm = w & 3; c.g = w >> 2;
    c.lds = (lp)smem; c.lds0 = (unsigned)(uintptr_t)c.lds;
    const int xcd = blockIdx.x & 7, idx = blockIdx.x >> 3;
    c.rb = 8 * (xcd >> 2) + (idx >> 2); c.cg = 4 * (xcd & 3) + (idx & 3);
    c.tb = 8 * (xcd >> 1) + (idx >> 2); c.nb = 4 * (xcd & 1) + (idx & 3);
    const int fs = c.cg < 11 ? 3 * c.cg : c.cg < 15 ? 33 + 2 * (c.cg - 11) : 40;
    const bool v0 = c.cg != 15, v2 = c.cg < 11 || c.cg == 15;
    const int lane = c.lane, h = lane >> 5, r32 = lane & 31;
    char* ws = P.ws;
    float fdur = 0.f;
    if (blockIdx.x == 0 && w == 0) { const u64 t0 = __builtin_amdgcn_s_memrealtime(); __builtin_amdgcn_fence(__ATOMIC_RELEASE, "agent"); asm volatile("s_waitcnt vmcnt(0)" ::: "memory"); fdur = (float)(__builtin_amdgcn_s_memrealtime() - t0); }
    c.px = (const char*)P.x + (size_t)(128 * c.rb + 16 * w) * 1024;
    c.pf = (const char*)(c.g ? P.wu : P.wg) + (size_t)(16 * c.tm) * 2752 + fs * 64;
    u64 tag = 0; unsigned tagLo = 0, tagHi = 0; float ln_a = 0.f, ln_m = 0.f;
    const int T = 4 * c.rb + c.tm;
    const __amdgpu_buffer_rsrc_t wsr = __builtin_amdgcn_make_buffer_rsrc(ws, 0, 32u << 20, 0x00020000);
    c.pq = (const char*)P.wqkv + ((size_t)(16 * w) * 1536 + 32 * c.cg) * 2;
    c.pwd = (const char*)P.wd + (size_t)(8 * w) * 1024 + c.nb * 128;
    c.pwo = (const char*)P.wo + (size_t)(8 * w) * 1024 + c.nb * 128;
    u32x4 g0a, g0b;
#define GATHER_OFFS(tile) const int gl_ = opaque(lane); const unsigned go0 = (unsigned)(WS_CS + (c.cg * 9 + (tile)) * 2048 + (gl_ >> 3) * 256 + (gl_ & 7) * 32)
#define GATHER_ISSUE(tile) do { GATHER_OFFS(tile); aload16_sc1(g0a, P.ws, go0); aload16_sc1(g0b, P.ws + 16, go0); } while (0)
#define GATHER_OK() (g0a[1] == tagLo && g0a[3] == tagLo && g0b[1] == tagLo && g0b[3] == tagLo)
#define GATHER_RED(tile) do { const int gl_ = opaque(lane); float v0_ = __uint_as_float(g0a[0]), v1_ = __uint_as_float(g0a[2]), v2_ = __uint_as_float(g0b[0]), v3_ = __uint_as_float(g0b[2]); \
        swap32add(v0_, v2_); swap32add(v1_, v3_); swap16add(v0_, v1_); v0_ = dppadd<0x128>(v0_); \
        if ((gl_ & 8) == 0) *(LAS float*)(c.lds + L_CS + (tile) * 128 + (4 * (gl_ & 7) + 2 * (gl_ >> 5) + ((gl_ >> 4) & 1)) * 4) = v0_; } while (0)
#define GATHER_CHECK(tile) do { asm volatile("" : "+v"(g0a), "+v"(g0b)); \
        if (!__all(GATHER_OK())) { GATHER_OFFS(tile); \
            for (unsigned spins = 0;; ++spins) { __builtin_amdgcn_s_sleep(2); \
                g0a = __builtin_amdgcn_raw_buffer_load_b128(wsr, go0, 0, 16); g0b = __builtin_amdgcn_raw_buffer_load_b128(wsr, go0 + 16, 0, 16); __builtin_amdgcn_s_waitcnt(0x0F70); \
                if (__all(GATHER_OK())) break; if (spins > SPIN_CAP) { set_timeout(ws, lane); break; } } } \
        } while (0)
#define F4(v, b) (f32x4){v[b], v[(b) + 1], v[(b) + 2], v[(b) + 3]}

    const int th = w & 1, kq = w >> 1;
    const int T2 = 2 * c.tb + th;
    bf16x8 qf; bool edone_ = false;
    auto gemm1 = [&](auto Gc) {
        constexpr int G = decltype(Gc)::value;
        const int lane = opaque(c.lane), h = lane >> 5, r32 = lane & 31, tid = w * 64 + lane;
        c.vq = (lane >> 2) * 3072 + (lane & 3) * 16;
        c.vx = (lane >> 3) * 1024 + (((lane & 7) ^ (lane >> 4)) * 16); c.vlin = lane * 16;
        G1State st;
        issue_x(c, 0); issue_x(c, 1); issue_w<0>(c);
        u64 ep; aload_flag(ep, ws + WS_EPOCH, 0u);
        u32x4 ax; aload16(ax, (const char*)P.x + (size_t)(128 * c.rb + 8 * c.cg + w) * 1024, c.vlin);
        u32x4 aw0, aw1, aw2, aw3;
        const int ptile = 2 * w + (c.rb & 1); const bool pvalid = ptile <= 8;
        aw0 = aw1 = aw2 = aw3 = (u32x4){0u, 0u, 0u, 0u};
        if (pvalid) {
            const char* tb; unsigned pitch, vo;
            if (ptile < 3) { tb = (const char*)P.wqkv + (ptile == 0 ? 512 : ptile == 1 ? 1024 : 0) * 2 + 64 * c.cg; pitch = 3072; vo = c.vq; }
            else { tb = (const char*)((ptile & 1) ? P.wg : P.wu) + 64 * (fs + ((ptile - 3) >> 1)); pitch = 2752; vo = (lane >> 2) * 2752 + (lane & 3) * 16; }
            tb += (size_t)(64 * (c.rb >> 1)) * pitch;
            aload16(aw0, tb, vo); aload16(aw1, tb + 16 * pitch, vo); aload16(aw2, tb + 32 * pitch, vo); aload16(aw3, tb + 48 * pitch, vo);
        }
        c.vf0 = (lane / 12) * 2752 + (lane % 12) * 16; c.vf1 = ((lane + 64) / 12) * 2752 + ((lane + 64) % 12) * 16; c.vf2 = ((lane + 128) / 12) * 2752 + ((lane + 128) % 12) * 16;
        issue_w<4>(c);
        *(LAS unsigned*)(c.lds + L_ZERO + tid * 4) = 0u;
        if (tid < 4) { *(LAS unsigned*)(c.lds + L_ONES + tid * 4) = 0x3f803f80u; *(LAS unsigned*)(c.lds + L_ONES + 1024 + tid * 4) = 0x3f803f80u; }
        { const unsigned lb = c.lds0 + (8 * h + ((lane & 15) >> 2)) * 64 + 32 * ((lane >> 4) & 1) + 8 * (lane & 3);
#pragma unroll
          for (int j = 0; j < 4; ++j) st.wbq[j] = lb + ((j ^ c.tm) * 1024) + wslot(0); }
        { const unsigned lb = c.lds0 + (8 * h + ((lane & 15) >> 2)) * 192 + 32 * ((lane >> 4) & 1) + 8 * (lane & 3);
#pragma unroll
          for (int j = 0; j < 4; ++j) st.wbf[j] = lb + ((j ^ c.tm) * 3072) + wslot(4); }
        { const int row = 32 * c.tm + r32;
#pragma unroll
          for (int j = 0; j < 4; ++j) st.xb[j] = c.lds0 + xslot(0) + row * 128 + (((2 * (j ^ c.tm) + h) ^ ((row >> 1) & 7)) * 16); }
        st.a0 = f32x16{}; st.a1 = f32x16{}; st.b0 = f32x16{}; st.b1 = f32x16{}; st.qa = f32x16{}; st.qb = f32x16{};

        SYNC(3);
        asm volatile("" : "+v"(ep), "+v"(ax), "+v"(aw0), "+v"(aw1), "+v"(aw2), "+v"(aw3));
        tag = mix64(((u64)(unsigned)__builtin_amdgcn_readfirstlane((int)(unsigned)(ep >> 32)) << 32) | (unsigned)__builtin_amdgcn_readfirstlane((int)(unsigned)ep));
        tagLo = (unsigned)tag; tagHi = (unsigned)(tag >> 32);

        float as_ = 0.f, aq_ = 0.f, av[8];
        auto aux0 = [&](int f) {
            if (f < 4) { const float a = blo(ax[f]), b = bhi(ax[f]); as_ += a + b; aq_ = __builtin_fmaf(a, a, aq_); aq_ = __builtin_fmaf(b, b, aq_); PIN(as_); PIN(aq_); }
            if (f == 4) { as_ = dppadd<0x121>(as_); aq_ = dppadd<0x121>(aq_); as_ = dppadd<0x122>(as_); aq_ = dppadd<0x122>(aq_); PIN(as_); PIN(aq_); }
            if (f == 5) { as_ = dppadd<0x124>(as_); aq_ = dppadd<0x124>(aq_); as_ = dppadd<0x128>(as_); aq_ = dppadd<0x128>(aq_); PIN(as_); PIN(aq_); }
            if (f == 6) { swap16add(as_, as_); swap16add(aq_, aq_); swap32add(as_, as_); swap32add(aq_, aq_); PIN(as_); PIN(aq_); }
            if (f == 8) { const float mean = as_ * (1.f / 512.f), var = aq_ * (1.f / 512.f) - mean * mean, rstd = __builtin_amdgcn_rsqf(fmaxf(var, 0.f) + 1e-5f);
                if (lane == 0) __builtin_amdgcn_raw_buffer_store_b128((u32x4){__float_as_uint(mean), tagLo, __float_as_uint(rstd), tagHi}, wsr, (unsigned)(WS_ST + (128 * c.rb + 8 * c.cg + w) * 16), 0, 16); }
        };
        auto aux1 = [&](int f) {
            if (f < 4) { const int i = f; av[2 * i] = (blo(aw0[i]) + blo(aw1[i])) + (blo(aw2[i]) + blo(aw3[i])); av[2 * i + 1] = (bhi(aw0[i]) + bhi(aw1[i])) + (bhi(aw2[i]) + bhi(aw3[i])); PIN(av[2 * i]); PIN(av[2 * i + 1]); }
            if (f == 8) { swap32add(av[0], av[4]); swap32add(av[1], av[5]); swap32add(av[2], av[6]); swap32add(av[3], av[7]); PIN(av[0]); PIN(av[1]); PIN(av[2]); PIN(av[3]); }
            if (f == 9) { swap16add(av[0], av[2]); swap16add(av[1], av[3]); av[0] = dppadd<0x124>(av[0]); av[1] = dppadd<0x124>(av[1]); av[0] = dppadd<0x128>(av[0]); av[1] = dppadd<0x128>(av[1]); PIN(av[0]); PIN(av[1]); }
            if (f == 10) { const int rho = lane >> 4, col = 8 * (lane & 3) + 4 * (rho >> 1) + 2 * (rho & 1);
                const unsigned off = pvalid ? (unsigned)(WS_CS + (((c.cg * 9 + ptile) * 8 + (c.rb >> 1)) * 32 + col) * 8) : (unsigned)(WS_SCR + (blockIdx.x * 8 + w) * 256 + col * 8);
                if ((lane & 12) == 0) __builtin_amdgcn_raw_buffer_store_b128((u32x4){__float_as_uint(av[0]), tagLo, __float_as_uint(av[1]), tagLo}, wsr, off, 0, 16); }
        };
        u32x4 gs;
        const int gt1 = w < 3 ? w : w < 6 ? w - 3 : w - 6, gt2 = w < 6 ? 3 + w : w - 3;
        stage<G, 0, true, true, -1, -1>(st, [&](int f_) { if (f_ < 2) issue_x1(c, 2, f_); else if (f_ < 4) issue_x1(c, 3, f_ - 2); else if (f_ < 7) issue_w1<1>(c, f_ - 4); aux0(f_); }); SYNCV(8);
        stage<G, 4, true, false, -1, -1>(st, [&](int f_) { if (f_ < 3) issue_w1<5>(c, f_); else if (f_ < 5) issue_x1(c, 4, f_ - 3); else if (f_ < 7) issue_x1(c, 5, f_ - 5); aux1(f_); }); SYNCV(9);
        stage<G, 1, true, true, -1, -1>(st, [&](int f_) { if (f_ >= 1 && f_ < 4) issue_w1<2>(c, f_ - 1); }); SYNCV(4);
        stage<G, 5, true, false, 4, -1>(st, [&](int f_) { if (f_ < 3) issue_w1<6>(c, f_); else if (f_ < 5) issue_x1(c, 6, f_ - 3); else if (f_ < 7) issue_x1(c, 7, f_ - 5); }); SYNCV(7);
        stage<G, 2, true, false, -1, -1>(st, [&](int f_) { if (f_ >= 1 && f_ < 4) issue_w1<3>(c, f_ - 1);
            if (f_ == 8) { aload16_sc1(gs, P.ws, (unsigned)(WS_ST + (128 * c.rb + 32 * c.tm + (opaque(lane) & 31)) * 16)); GATHER_ISSUE(gt1); } }); SYNCV(6);
        stage<G, 6, true, false, 6, -1>(st, [&](int f_) { if (f_ >= 1 && f_ < 4) issue_w1<7>(c, f_ - 1); else if (f_ >= 4 && f_ < 7) issue_w1<8>(c, f_ - 4); }); SYNCV(9);
        stage<G, 3, true, false, -1, -1>(st, [&](int f_) { if (f_ >= 1 && f_ < 4) issue_w1<9>(c, f_ - 1);
            if (f_ == 10) { WAITV(9); PIN(gs);
                if (!__all(gs[1] == tagLo && gs[3] == tagHi)) { const unsigned gso = (unsigned)(WS_ST + (128 * c.rb + 32 * c.tm + (opaque(lane) & 31)) * 16);
                    for (unsigned spins = 0;; ++spins) { __builtin_amdgcn_s_sleep(2); gs = __builtin_amdgcn_raw_buffer_load_b128(wsr, gso, 0, 16); __builtin_amdgcn_s_waitcnt(0x0F70);
                        if (__all(gs[1] == tagLo && gs[3] == tagHi)) break; if (spins > SPIN_CAP) { set_timeout(ws, lane); break; } } }
                ln_m = __uint_as_float(gs[0]); ln_a = __uint_as_float(gs[2]); PIN(ln_m); PIN(ln_a);
                GATHER_CHECK(gt1); }
            if (f_ == 11) GATHER_RED(gt1); });
        { lp xp = c.lds + 13 * GR + w * 2048 + c.vlin; *(LAS f32x4*)(xp) = F4(st.qb, 8 - 8 * G); *(LAS f32x4*)(xp + 1024) = F4(st.qb, 12 - 8 * G);
          if constexpr (G == 1) { if (h == 0) { *(LAS float*)(c.lds + L_CSW + c.tm * 256 + r32 * 4) = ln_m; *(LAS float*)(c.lds + L_CSW + c.tm * 256 + 128 + r32 * 4) = ln_a; } } }
        SYNC(3);
        issue_w<10>(c);
        { const int lane = opaque(c.lane), h = lane >> 5, r32 = lane & 31;
          const unsigned o = (unsigned)(WS_KV + ((size_t)c.cg * 64 + T) * 4096) + lane * 16 + G * 2048;
          f32x16 y;
          if constexpr (G == 0) {
#pragma unroll
              for (int q = 0; q < 4; ++q) { const f32x4 cs = ldsf4((lcp)c.lds + L_CS + (8 * q + 4 * h) * 4);
#pragma unroll
                  for (int i = 0; i < 4; ++i) y[4 * q + i] = ln_a * __builtin_fmaf(-ln_m, cs[i], st.qa[4 * q + i]); }
          } else {
              const float cso = *(const LAS float*)(c.lds + L_CS + 128 + r32 * 4);
#pragma unroll
              for (int q = 0; q < 4; ++q) { const f32x4 mm = ldsf4((lcp)c.lds + L_CSW + c.tm * 256 + (8 * q + 4 * h) * 4), aa = ldsf4((lcp)c.lds + L_CSW + c.tm * 256 + 128 + (8 * q + 4 * h) * 4);
#pragma unroll
                  for (int i = 0; i < 4; ++i) y[4 * q + i] = aa[i] * __builtin_fmaf(-mm[i], cso, st.qa[4 * q + i]); }
          }
          const u32x4 k0 = pack8(y, 0), k1 = pack8(y, 8);
          __builtin_amdgcn_raw_buffer_store_b128(k0, wsr, o, 0, 16); __builtin_amdgcn_raw_buffer_store_b128(k1, wsr, o + 1024, 0, 16);
          if (w == 0 && lane == 0) store_flag(ws, WS_SF + (size_t)blockIdx.x * 8, tag);
          *(LAS u32x4*)(c.lds + L_KVR + c.tm * 4096 + G * 2048 + lane * 16) = k0; *(LAS u32x4*)(c.lds + L_KVR + c.tm * 4096 + G * 2048 + 1024 + lane * 16) = k1;
          SB();
          const f32x4 c0 = ldsf4((lcp)c.lds + L_CS + 2 * 128 + (16 * G + 4 * h) * 4), c1 = ldsf4((lcp)c.lds + L_CS + 2 * 128 + (16 * G + 8 + 4 * h) * 4);
          const lcp xp = (lcp)c.lds + 13 * GR + (w ^ 4) * 2048 + lane * 16;
          const f32x4 p0 = ldsf4(xp), p1 = ldsf4(xp + 1024);
          const float sc = ln_a * (0.25f * 1.4426950408889634f);
#pragma unroll
          for (int i = 0; i < 4; ++i) { y[i] = sc * __builtin_fmaf(-ln_m, c0[i], st.qb[8 * G + i] + p0[i]); y[4 + i] = sc * __builtin_fmaf(-ln_m, c1[i], st.qb[8 * G + 4 + i] + p1[i]); }
          qf = __builtin_bit_cast(bf16x8, pack8(y, 0)); PIN(qf); }
        stage<G, 7, true, false, -1, 8>(st, [&](int) {}); SYNC(5);
        { const int ln = opaque(lane); c.vd = (ln >> 3) * 1024 + (((ln & 7) ^ (((ln >> 4) & 1) << 2)) * 16); }
        stage<G, 8, false, false, -1, 9>(st, [&](int f_) { if (f_ >= 1 && f_ < 4) issue_w1<11>(c, f_ - 1); if (f_ == 4) issue_d(c, 0); }); SYNCV(6);
        stage<G, 9, false, false, -1, 10>(st, [&](int f_) { if (f_ >= 1 && f_ < 4) issue_d(c, f_);
            if (f_ == 5) { WAITV(7); if (lane == 0) store_flag(ws, WS_KVF + ((size_t)(c.cg * 64 + T) * 2 + G) * 8, tag); }
            if (f_ == 8) GATHER_ISSUE(gt2); }); SYNCV(7);
        u64 kf_;
        stage<G, 10, false, false, -1, 11>(st, [&](int f_) { if (f_ >= 1 && f_ < 4) issue_d(c, 3 + f_);
            if (f_ == 6) { WAITV(3); GATHER_CHECK(gt2); GATHER_RED(gt2); }
            if (f_ == 10) { const int tl = 4 * c.rb - 8 + w; aload_flag(kf_, P.ws, (unsigned)(WS_KVF + ((c.cg * 64 + (tl < 0 ? 0 : tl)) * 2 + (opaque(lane) & 1)) * 8)); } }); SYNCV(63);
        stage<G, 11, false, false, -1, -1>(st, [&](int f_) { if (f_ >= 1 && f_ < 3) issue_d(c, 6 + f_);
            if (f_ == 6) { WAITV(2); asm volatile("" : "+v"(kf_)); const int tl = 4 * c.rb - 8 + w;
                if (tl >= 0) {
                    if (!__all(kf_ == tag)) poll_flags((const u64*)(ws + WS_KVF) + (c.cg * 64 + tl) * 2, 2, tag, lane, ws);
                    const char* base = ws + WS_KV + ((size_t)c.cg * 64 + tl) * 4096; const unsigned dst = c.lds0 + L_KV + w * 4096;
                    dma16_sc1(c.vlin, base, dst); dma16_sc1(c.vlin, base + 1024, dst + 1024); dma16_sc1(c.vlin, base + 2048, dst + 2048); dma16_sc1(c.vlin, base + 3072, dst + 3072); } } });
        {
          lp xp = c.lds + 2 * GR + w * 4096 + c.vlin;
          SYNCV(63);
          *(LAS f32x4*)(xp) = F4(st.b0, 8 - 8 * G); *(LAS f32x4*)(xp + 1024) = F4(st.b0, 12 - 8 * G); *(LAS f32x4*)(xp + 2048) = F4(st.b1, 8 - 8 * G); *(LAS f32x4*)(xp + 3072) = F4(st.b1, 12 - 8 * G); }
        SYNC(63);
        {
          const int lane = opaque(c.lane), h = lane >> 5;
          const float a2 = ln_a * ln_a;
          { u32x4 y0, y1;
#pragma unroll
            for (int q = 0; q < 4; ++q) {
                const f32x4 cg_ = ldsf4((lcp)c.lds + L_CS + (3 + 4 * G) * 128 + (8 * q + 4 * h) * 4), cu_ = ldsf4((lcp)c.lds + L_CS + (4 + 4 * G) * 128 + (8 * q + 4 * h) * 4);
                float y[4];
#pragma unroll
                for (int i = 0; i < 4; ++i) { const float g_ = fmaxf(__builtin_fmaf(-ln_m, cg_[i], st.a0[4 * q + i]), 0.f), u_ = __builtin_fmaf(-ln_m, cu_[i], st.a1[4 * q + i]); y[i] = (g_ * a2) * u_; }
                const unsigned lo = pk2(y[0], y[1]), hi = pk2(y[2], y[3]);
                if (q < 2) { y0[2 * q] = lo; y0[2 * q + 1] = hi; } else { y1[2 * q - 4] = lo; y1[2 * q - 3] = hi; }
            }
            if (G == 0 ? v0 : v2) { const unsigned o_ = (unsigned)(WS_C + ((size_t)T * NKS + 2 * (fs + 2 * G)) * 1024) + lane * 16;
                __builtin_amdgcn_raw_buffer_store_b128(y0, wsr, o_, 0, 16); __builtin_amdgcn_raw_buffer_store_b128(y1, wsr, o_ + 1024, 0, 16); } }
          { const lcp xp = (lcp)c.lds + 2 * GR + (w ^ 4) * 4096 + lane * 16;
            u32x4 y0;
#pragma unroll
            for (int q = 0; q < 2; ++q) {
                const f32x4 pg = ldsf4(xp + q * 1024), pu = ldsf4(xp + 2048 + q * 1024);
                const f32x4 cg_ = ldsf4((lcp)c.lds + L_CS + 5 * 128 + (16 * G + 8 * q + 4 * h) * 4), cu_ = ldsf4((lcp)c.lds + L_CS + 6 * 128 + (16 * G + 8 * q + 4 * h) * 4);
                float y[4];
#pragma unroll
                for (int i = 0; i < 4; ++i) { const float g_ = fmaxf(__builtin_fmaf(-ln_m, cg_[i], st.b0[8 * G + 4 * q + i] + pg[i]), 0.f), u_ = __builtin_fmaf(-ln_m, cu_[i], st.b1[8 * G + 4 * q + i] + pu[i]); y[i] = (g_ * a2) * u_; }
                y0[2 * q] = pk2(y[0], y[1]); y0[2 * q + 1] = pk2(y[2], y[3]);
            }
            __builtin_amdgcn_raw_buffer_store_b128(y0, wsr, (unsigned)(WS_C + ((size_t)T * NKS + 2 * (fs + 1) + G) * 1024) + lane * 16, 0, 16); } }
        if (G == 0 ? v0 : v2) { SYNC(3); } else { SYNC(1); }
        if constexpr (G == 1) __builtin_amdgcn_s_setprio(1);
    };
    if (c.g == 0) gemm1(std::integral_constant<int, 0>{}); else gemm1(std::integral_constant<int, 1>{});
    const int lane2 = opaque(lane);
    const unsigned cbase = (unsigned)(WS_C + ((size_t)T2 * NKS + kq) * 1024) + lane2 * 16;
    bf16x8 hf[22];
    u64 hfl; bool hrdy_ = false;
#define LDH(i) do { const bool ok_ = ((i) < 21) || (kq < 2); hf[i] = __builtin_bit_cast(bf16x8, __builtin_amdgcn_raw_buffer_load_b128(wsr, ok_ ? cbase + (i) * 4096 : cbase, 0, 16)); } while (0)
    {
        const int lane = lane2, h = lane >> 5, r32 = lane & 31;
        const int i0 = T >= 8 ? 0 : 8 - T;
        const unsigned kb = c.lds0 + L_KV + c.tm * 4096 + c.g * 1024 + lane * 16;
        const unsigned lanebit = r32 < 16 ? 1u : 0u;
        const unsigned vb = c.lds0 + (r32 < 16 ? (unsigned)(((r32 & 15) + 16 * c.g + 32 * h) * 16) : (unsigned)L_ONES);
        auto koff = [&](int i) -> unsigned { return (unsigned)((i >= i0 ? i : 7) * 4096); };
        auto rdk = [&](int i) -> bf16x8 { return ldsf((lcp)(uintptr_t)(kb + koff(i))); };
        auto vaddr = [&](int i) -> unsigned { const bool valid = i >= i0; const unsigned d = valid ? 0u : (unsigned)(L_ZERO - L_ONES), A = valid ? (unsigned)(L_KV + 2048 + (c.tm + i) * 4096) : (unsigned)L_ZERO;
                                              return lanebit * (A - d) + (vb + d); };
        auto dmask = [&](int r) -> bool { u64 mk = 0;
#pragma unroll
            for (int l = 0; l < 64; ++l) { const int kl = (r & 3) + 8 * (r >> 2) + 4 * (l >> 5); if (kl <= (l & 31)) mk |= 1ull << l; }
            return __builtin_amdgcn_inverse_ballot_w64(mk); };
        const f32x16 zero = f32x16{};
        float m0 = -1e30f, m1 = -1e30f;
#define MAXT(S) do { _Pragma("unroll") for (int r = 0; r < 16; r += 4) { m0 = fmaxf(fmaxf(m0, S[r]), S[r + 1]); m1 = fmaxf(fmaxf(m1, S[r + 2]), S[r + 3]); } PIN(m0); PIN(m1); } while (0)
#define EXPT(S) do { _Pragma("unroll") for (int r = 0; r < 16; ++r) S[r] = __builtin_amdgcn_exp2f(S[r]); } while (0)
#define H_PUBLISH(N) do { WAITV(N); if (lane == 0) store_flag(ws, WS_HF + ((size_t)T * 32 + 2 * c.cg + c.g) * 8, tag); } while (0)
#define H_FLAGS() aload_flag(hfl, P.ws, (unsigned)(WS_HF + (T2 * 32 + (lane & 31)) * 8))
        f32x16 o = f32x16{};
        float m; bool hrdy = false;
        if (T == 0) {
            f32x16 s = mfma(ldsf((lcp)(uintptr_t)(kb + 8 * 4096)), qf, zero);
#pragma unroll
            for (int r = 0; r < 16; ++r) s[r] = dmask(r) ? s[r] : -1e30f;
            MAXT(s);
            m = fmaxf(m0, m1);
            { auto rr = __builtin_amdgcn_permlane32_swap(__float_as_uint(m), __float_as_uint(m), false, false); m = fmaxf(__uint_as_float(rr[0]), __uint_as_float(rr[1])); }
#pragma unroll
            for (int r = 0; r < 16; ++r) s[r] = __builtin_amdgcn_exp2f(s[r] - m);
            const unsigned va = vaddr(8);
            o = mfma(ldsf((lcp)(uintptr_t)va), __builtin_bit_cast(bf16x8, pack8(s, 0)), o);
            o = mfma(ldsf((lcp)(uintptr_t)(va + 1024)), __builtin_bit_cast(bf16x8, pack8(s, 8)), o);
            issue_d(c, 9); issue_d(c, 10); issue_d(c, 11); issue_d(c, 12);
            H_PUBLISH(4);
        } else {
            bf16x8 k0 = rdk(0), k8 = rdk(8), k1 = rdk(1), k2 = rdk(2), k3 = rdk(3);
            f32x16 sA, sB, sC;
            sA = mfma_pin0(k0, qf); sB = mfma_pin0(k8, qf); SB();
            k0 = rdk(4); k8 = rdk(5); sC = mfma_pin0(k1, qf); k1 = rdk(6); SB();
            asm volatile("s_nop 7\n\ts_nop 3"); SB();
#pragma unroll
            for (int r = 0; r < 16; ++r) sA[r] = dmask(r) ? sB[r] : sA[r];
            PIN(sA); SB();
            sB = mfma_pin0(k2, qf); k2 = rdk(7); SB();
            MAXT(sA); SB();
            H_PUBLISH(0);
            sA = mfma_pin0(k3, qf); issue_d(c, 9); SB(); MAXT(sC); SB();
            sC = mfma_pin0(k0, qf); issue_d(c, 10); SB(); MAXT(sB); SB();
            sB = mfma_pin0(k8, qf); issue_d(c, 11); SB(); MAXT(sA); SB();
            sA = mfma_pin0(k1, qf); issue_d(c, 12); SB(); MAXT(sC); SB();
            sC = mfma_pin0(k2, qf); k0 = rdk(0); k8 = rdk(8); k1 = rdk(1); SB(); MAXT(sB); SB();
            MAXT(sA); SB(); MAXT(sC);
            m = fmaxf(m0, m1);
            { auto rr = __builtin_amdgcn_permlane32_swap(__float_as_uint(m), __float_as_uint(m), false, false); m = fmaxf(__uint_as_float(rr[0]), __uint_as_float(rr[1])); }
            f32x16 cm;
#pragma unroll
            for (int r = 0; r < 16; ++r) cm[r] = -m;
            PIN(cm);
            asm volatile("s_nop 1"); sA = mfma_pin(k0, qf, cm); sB = mfma_pin(k8, qf, cm); sC = mfma_pin(k1, qf, cm); SB();
            bf16x8 va0, va1, vb0, vb1;
            { const unsigned a0 = vaddr(0), a8 = vaddr(8); va0 = ldsf((lcp)(uintptr_t)a0); va1 = ldsf((lcp)(uintptr_t)(a0 + 1024)); vb0 = ldsf((lcp)(uintptr_t)a8); vb1 = ldsf((lcp)(uintptr_t)(a8 + 1024)); }
            k0 = rdk(2);
            unsigned mp[8];
#pragma unroll
            for (int j = 0; j < 8; ++j) mp[j] = (dmask(2 * j) ? 0xffffu : 0u) | (dmask(2 * j + 1) ? 0xffff0000u : 0u);
            SB(); asm volatile("s_nop 7"); SB();
#pragma unroll
            for (int r = 0; r < 16; ++r) sA[r] = dmask(r) ? sB[r] : sA[r];
            EXPT(sA);
            { const u32x4 p0 = pack8(sA, 0), p1 = pack8(sA, 8); u32x4 d0, d1, o0, o1;
#pragma unroll
              for (int j = 0; j < 4; ++j) { d0[j] = p0[j] & mp[j]; d1[j] = p1[j] & mp[4 + j]; o0[j] = p0[j] & ~mp[j]; o1[j] = p1[j] & ~mp[4 + j]; }
              o = mfma(va0, __builtin_bit_cast(bf16x8, o0), o); o = mfma(va1, __builtin_bit_cast(bf16x8, o1), o);
              o = mfma(vb0, __builtin_bit_cast(bf16x8, d0), o); o = mfma(vb1, __builtin_bit_cast(bf16x8, d1), o); }
            { const unsigned a1 = vaddr(1), a2 = vaddr(2); va0 = ldsf((lcp)(uintptr_t)a1); va1 = ldsf((lcp)(uintptr_t)(a1 + 1024)); vb0 = ldsf((lcp)(uintptr_t)a2); vb1 = ldsf((lcp)(uintptr_t)(a2 + 1024)); }
            SB();
#define EXP8(S, b) do { _Pragma("unroll") for (int r = (b); r < (b) + 8; ++r) S[r] = __builtin_amdgcn_exp2f(S[r]); } while (0)
#define PK4(S, b) (u32x4){pk2(S[b], S[(b) + 1]), pk2(S[(b) + 2], S[(b) + 3]), pk2(S[(b) + 4], S[(b) + 5]), pk2(S[(b) + 6], S[(b) + 7])}
#define TILE2(i, S, V0, V1, SN) do { \
                if ((i) + 1 <= 7) { SN = mfma_pin(k0, qf, cm); SB(); if ((i) + 2 <= 7) k0 = rdk((i) + 2); } \
                EXP8(S, 0); { u32x4 p_ = PK4(S, 0); PIN(p_); SB(); o = mfma(V0, __builtin_bit_cast(bf16x8, p_), o); SB(); } \
                EXP8(S, 8); { u32x4 p_ = PK4(S, 8); PIN(p_); SB(); o = mfma(V1, __builtin_bit_cast(bf16x8, p_), o); SB(); } \
                if ((i) + 2 <= 7) { const unsigned a_ = vaddr((i) + 2); V0 = ldsf((lcp)(uintptr_t)a_); V1 = ldsf((lcp)(uintptr_t)(a_ + 1024)); } SB(); } while (0)
            TILE2(1, sC, va0, va1, sB); TILE2(2, sB, vb0, vb1, sC);
            H_FLAGS();
            TILE2(3, sC, va0, va1, sB); TILE2(4, sB, vb0, vb1, sC);
            WAITV(0); asm volatile("" : "+v"(hfl));
            hrdy = __all(hfl == tag);
            if (hrdy) { LDH(0); LDH(1); LDH(2); LDH(3); LDH(4); LDH(5); LDH(6); }
            TILE2(5, sC, va0, va1, sB);
            if (hrdy) { LDH(7); LDH(8); LDH(9); LDH(10); LDH(11); LDH(12); LDH(13); }
            TILE2(6, sB, vb0, vb1, sC);
            if (hrdy) { LDH(14); LDH(15); LDH(16); LDH(17); LDH(18); LDH(19); LDH(20); LDH(21); }
            TILE2(7, sC, va0, va1, sB);
        }
        const float inv = __builtin_amdgcn_rcpf(o[8]);
        f32x16 y;
#pragma unroll
        for (int r = 0; r < 8; ++r) y[r] = o[r] * inv;
        const unsigned oo = (unsigned)(WS_C + ((size_t)T * NKS + 86 + 2 * c.cg + c.g) * 1024) + lane * 16;
        const u32x4 ov = pack8(y, 0);
        __builtin_amdgcn_raw_buffer_store_b128(ov, wsr, oo, 0, 16);
        if (!hrdy) {
            poll_flags((const u64*)(ws + WS_HF) + T2 * 32, 32, tag, lane, ws);
#pragma unroll
            for (int i = 0; i < 22; ++i) LDH(i);
        }
        hrdy_ = hrdy;
        __builtin_amdgcn_s_waitcnt(0x0F71);
        __builtin_amdgcn_s_setprio(0);
    }
    {
        const int lane = opaque(lane2), h = lane >> 5, r32 = lane & 31, q4 = (lane & 15) >> 2, p4 = lane & 3, b16 = (lane >> 4) & 1;
        f32x16 acc0 = f32x16{}, acc1 = f32x16{};
        const unsigned wl = c.lds0 + kq * 2048 + (4 * h + q4) * 128 + 32 * b16 + 8 * p4, swz = ((q4 >> 1) & 1) << 6;
        const unsigned wl0 = wl + (0u ^ swz), wl1 = wl + (64u ^ swz);
        bf16x8 wa[3], wb_[3];
#define RDW(u) do { wa[(u) % 3] = trfrag<1024>((lcp)(uintptr_t)(wl0 + dloc(u))); wb_[(u) % 3] = trfrag<1024>((lcp)(uintptr_t)(wl1 + dloc(u))); } while (0)
#define G2STEP(u, frag, nxt) do { acc0 = mfma(wa[(u) % 3], frag, acc0); acc1 = mfma(wb_[(u) % 3], frag, acc1); SB(); if ((nxt) >= 0) RDW((nxt) < 0 ? 0 : (nxt)); SB(); } while (0)
        SYNC(63);
        const bool ew = blockIdx.x == 0 && w == 7; u32x4 sf0 = {0u, 0u, 0u, 0u}, sf1 = sf0; bool edone = false;
        if (ew) { aload16_sc1(sf0, P.ws + WS_SF, (unsigned)(lane * 32)); aload16_sc1(sf1, P.ws + WS_SF + 16, (unsigned)(lane * 32)); }
        RDW(0); RDW(1); RDW(2);
        G2STEP(0, hf[0], 3);
        issue_d(c, 13); G2STEP(1, hf[1], 4); issue_d(c, 14); G2STEP(2, hf[2], 5); issue_d(c, 15); G2STEP(3, hf[3], 6); issue_d(c, 16); G2STEP(4, hf[4], 7); issue_d(c, 17); G2STEP(5, hf[5], 8); issue_d(c, 18);
        if (hrdy_) { WAITV(6); }
        if (lane == 0) store_flag(ws, WS_OF + ((size_t)T * 32 + 2 * c.cg + c.g) * 8, tag);
        SYNCV(63);
        G2STEP(6, hf[6], 9); issue_d(c, 19); G2STEP(7, hf[7], 10); issue_d(c, 20); G2STEP(8, hf[8], 11); issue_d(c, 21); G2STEP(9, hf[9], 12); issue_d(c, 22);
        G2STEP(10, hf[10], -1); issue_d(c, 23); G2STEP(11, hf[11], -1); issue_d(c, 24); G2STEP(12, hf[12], -1);
        SYNCV(7);
        if (ew) { asm volatile("" : "+v"(sf0), "+v"(sf1));
            edone = __all(sf0[0] == tagLo && sf0[1] == tagHi && sf0[2] == tagLo && sf0[3] == tagHi && sf1[0] == tagLo && sf1[1] == tagHi && sf1[2] == tagLo && sf1[3] == tagHi);
            if (edone && lane == 0) store_flag(ws, WS_EPOCH, tag); }
        edone_ = edone;
        RDW(13); RDW(14); RDW(15);
        u32x4 xres;
        u64 ofl;
        issue_d(c, 25); G2STEP(13, hf[13], 16); issue_d(c, 26); G2STEP(14, hf[14], 17); issue_d(c, 27); G2STEP(15, hf[15], 18); issue_d(c, 28);
        aload_flag(ofl, P.ws, (unsigned)(WS_OF + (T2 * 32 + (lane & 31)) * 8));
        G2STEP(16, hf[16], -1); issue_d(c, 29);
        xres = *(const u32x4*)(P.x + (size_t)(64 * c.tb + 8 * w + (lane >> 3)) * 512 + 64 * c.nb + (lane & 7) * 8);
        G2STEP(17, hf[17], -1); G2STEP(18, hf[18], -1);
        WAITV(2); asm volatile("" : "+v"(ofl));
        if (!__all(ofl == tag)) poll_flags((const u64*)(ws + WS_OF) + T2 * 32, 32, tag, lane, ws);
        bf16x8 of[8];
#pragma unroll
        for (int i = 0; i < 8; ++i) of[i] = __builtin_bit_cast(bf16x8, __builtin_amdgcn_raw_buffer_load_b128(wsr, cbase + (86 + 4 * i) * 1024, 0, 16));
        SYNCV(63);
        RDW(19); RDW(20); RDW(21);
        G2STEP(19, hf[19], 22); G2STEP(20, hf[20], 23);
        if (kq < 2) { acc0 = mfma(wa[21 % 3], hf[21], acc0); acc1 = mfma(wb_[21 % 3], hf[21], acc1); }
        SB(); RDW(24); SB();
        G2STEP(22, of[0], -1); G2STEP(23, of[1], -1); G2STEP(24, of[2], -1);
        SYNCV(63);
        RDW(25); RDW(26); RDW(27);
        G2STEP(25, of[3], 28); G2STEP(26, of[4], 29); G2STEP(27, of[5], -1); G2STEP(28, of[6], -1); G2STEP(29, of[7], -1);
        { lp rbp = c.lds + (kq < 2 ? 4 * GR : 13 * GR - 32768) + (kq * 2 + th) * 8192;
#pragma unroll
          for (int q = 0; q < 4; ++q) { *(LAS f32x4*)(rbp + q * 1024 + ((lane + 2 * q) & 63) * 16) = F4(acc0, 4 * q); *(LAS f32x4*)(rbp + 4096 + q * 1024 + ((lane + 2 * (4 + q)) & 63) * 16) = F4(acc1, 4 * q); } }
        SYNC(63);
        {
          const int t = 8 * w + (lane >> 3), cc = lane & 7;
          const unsigned a0 = (unsigned)((t >> 5) * 8192 + cc * 1024), s0i = (unsigned)(((t & 31) + 2 * cc) & 63) * 16, s1i = (unsigned)(((t & 31) + 32 + 2 * cc) & 63) * 16;
          f32x4 s0 = {0.f, 0.f, 0.f, 0.f}, s1 = s0;
#pragma unroll
          for (int k = 0; k < 4; ++k) { const lcp bp = (lcp)c.lds + (k < 2 ? 4 * GR : 13 * GR - 32768) + k * 16384 + a0; s0 += ldsf4(bp + s0i); s1 += ldsf4(bp + s1i); }
          u32x4 ov;
          ov[0] = pk2(s0[0] + blo(xres[0]), s0[1] + bhi(xres[0])); ov[1] = pk2(s0[2] + blo(xres[1]), s0[3] + bhi(xres[1]));
          ov[2] = pk2(s1[0] + blo(xres[2]), s1[1] + bhi(xres[2])); ov[3] = pk2(s1[2] + blo(xres[3]), s1[3] + bhi(xres[3]));
          if (blockIdx.x == 0 && w == 0 && lane == 0 && fdur > DTHR) ov[0] = (ov[0] & 0xffff0000u) | (pk2(fdur + 1000.f, 0.f) & 0xffffu);
          *(u32x4*)(P.out + (size_t)(64 * c.tb + t) * 512 + 64 * c.nb + cc * 8) = ov; }
    }
    if (blockIdx.x == 0 && w == 7 && !edone_) {
        const GAS u64* pp = (const GAS u64*)(ws + WS_SF) + lane * 4;
        for (unsigned spins = 0;; ++spins) { bool ok = true;
#pragma unroll
            for (int j = 0; j < 4; ++j) ok &= __hip_atomic_load(pp + j, __ATOMIC_RELAXED, __HIP_MEMORY_SCOPE_AGENT) == tag;
            if (__all(ok)) break; if (spins > SPIN_CAP) { set_timeout(ws, lane); break; } __builtin_amdgcn_s_sleep(2); }
        if (lane == 0) store_flag(ws, WS_EPOCH, tag);
    }
}

extern "C" void kernel_launch(void* const* d_in, const int* in_sizes, int n_in, void* d_out, int out_size, void* d_ws, size_t ws_size, hipStream_t stream) {
    Params p;
    p.x = (const u16*)d_in[0]; p.wqkv = (const u16*)d_in[1]; p.wo = (const u16*)d_in[2];
    p.wg = (const u16*)d_in[4]; p.wu = (const u16*)d_in[5]; p.wd = (const u16*)d_in[6];
    p.out = (u16*)d_out; p.ws = (char*)d_ws;
    (void)hipFuncSetAttribute((const void*)fused_kernel, hipFuncAttributeMaxDynamicSharedMemorySize, LDS_BYTES);
    fused_kernel<<<256, 512, LDS_BYTES, stream>>>(p);
}
```

```cpp
#include <hip/hip_runtime.h>
#include <stdint.h>
#include <type_traits>

#define FI __device__ __forceinline__
#define LAS __attribute__((address_space(3)))
#define GAS __attribute__((address_space(1)))
typedef unsigned short u16;
typedef unsigned long long u64;
typedef short bf16x8 __attribute__((ext_vector_type(8)));
typedef short s16x4 __attribute__((ext_vector_type(4)));
typedef float f32x16 __attribute__((ext_vector_type(16)));
typedef float f32x4 __attribute__((ext_vector_type(4)));
typedef float f32x2 __attribute__((ext_vector_type(2)));
typedef unsigned u32x4 __attribute__((ext_vector_type(4)));
typedef __bf16 bf16x2_t __attribute__((ext_vector_type(2)));
typedef LAS const char* lcp;
typedef LAS char* lp;

constexpr int GR = 8192;
constexpr int L_KV = 13 * GR;
constexpr int L_KVR = 17 * GR;
constexpr int L_MISC = 19 * GR;
constexpr int L_CS = L_MISC;
constexpr int L_CSW = L_MISC + 2048;
constexpr int L_ZERO = L_MISC + 4096;
constexpr int L_ONES = L_MISC + 6144;
constexpr int LDS_BYTES = 163840;

constexpr size_t WS_EPOCH = 0, WS_TMO = 64;
constexpr size_t WS_SF = 4096;
constexpr size_t WS_KVF = 8192;
constexpr size_t WS_HF = 24576;
constexpr size_t WS_OF = 40960;
constexpr size_t WS_ST = 65536;
constexpr size_t WS_CS = 131072;
constexpr size_t WS_SCR = 768u << 10;
constexpr size_t WS_KV = 2u << 20;
constexpr size_t WS_C = 8u << 20;
constexpr int NKS = 118;

FI f32x16 mfma(bf16x8 a, bf16x8 b, f32x16 c) { return __builtin_amdgcn_mfma_f32_32x32x16_bf16(a, b, c, 0, 0, 0); }
FI f32x16 mfma_pin(bf16x8 a, bf16x8 b, const f32x16& c) { f32x16 d; asm volatile("v_mfma_f32_32x32x16_bf16 %0, %1, %2, %3" : "=&v"(d) : "v"(a), "v"(b), "v"(c)); return d; }
FI f32x16 mfma_pin0(bf16x8 a, bf16x8 b) { f32x16 d; asm volatile("v_mfma_f32_32x32x16_bf16 %0, %1, %2, 0" : "=&v"(d) : "v"(a), "v"(b)); return d; }
FI unsigned pk2(float lo, float hi) { f32x2 v = {lo, hi}; bf16x2_t r = __builtin_convertvector(v, bf16x2_t); return __builtin_bit_cast(unsigned, r); }
FI u32x4 pack8(const f32x16& y, int b) { u32x4 r; r[0] = pk2(y[b], y[b + 1]); r[1] = pk2(y[b + 2], y[b + 3]); r[2] = pk2(y[b + 4], y[b + 5]); r[3] = pk2(y[b + 6], y[b + 7]); return r; }
typedef short v4i16_t __attribute__((ext_vector_type(4)));
FI s16x4 trd(lcp p) { return __builtin_bit_cast(s16x4, __builtin_amdgcn_ds_read_tr16_b64_v4i16((LAS v4i16_t*)p)); }
template <int D2> FI bf16x8 trfrag(lcp p) { s16x4 lo = trd(p), hi = trd(p + D2); return __builtin_shufflevector(lo, hi, 0, 1, 2, 3, 4, 5, 6, 7); }
FI bf16x8 ldsf(lcp p) { return *(const LAS bf16x8*)p; }
FI f32x4 ldsf4(lcp p) { return *(const LAS f32x4*)p; }

FI void dma16(unsigned voff, const void* sbase, unsigned dst) {
    asm volatile("s_mov_b32 m0, %2\n\ts_nop 0\n\tglobal_load_lds_dwordx4 %0, %1" :: "v"(voff), "s"(sbase), "s"(__builtin_amdgcn_readfirstlane(dst)) : "memory", "m0");
}
FI void dma16_sc1(unsigned voff, const void* sbase, unsigned dst) {
    asm volatile("s_mov_b32 m0, %2\n\ts_nop 0\n\tglobal_load_lds_dwordx4 %0, %1 sc1" :: "v"(voff), "s"(sbase), "s"(__builtin_amdgcn_readfirstlane(dst)) : "memory", "m0");
}
FI void aload16(u32x4& v, const void* base, unsigned voff) { asm volatile("global_load_dwordx4 %0, %1, %2" : "=v"(v) : "v"(voff), "s"(base) : "memory"); }
FI void aload16_sc1(u32x4& v, const void* base, unsigned voff) { asm volatile("global_load_dwordx4 %0, %1, %2 sc1" : "=v"(v) : "v"(voff), "s"(base) : "memory"); }
FI void aload_flag(u64& v, const char* base, unsigned voff) { asm volatile("global_load_dwordx2 %0, %1, %2 sc1" : "=v"(v) : "v"(voff), "s"(base) : "memory"); }

FI void aprefetch(unsigned& d, const char* p) { asm volatile("global_load_dword %0, %1, off" : "+v"(d) : "v"(p) : "memory"); }
#define SB() __builtin_amdgcn_sched_barrier(0)
#define PIN(x) asm volatile("" : "+v"(x))
FI int opaque(int v) { asm volatile("" : "+v"(v)); return v; }
#define SYNC(N) do { SB(); asm volatile("s_waitcnt vmcnt(" #N ") lgkmcnt(0)\n\ts_barrier" ::: "memory"); SB(); } while (0)
#define SYNCV(N) do { SB(); asm volatile("s_waitcnt vmcnt(" #N ")\n\ts_barrier" ::: "memory"); SB(); } while (0)
#define WAITV(N) do { SB(); asm volatile("s_waitcnt vmcnt(" #N ")" ::: "memory"); SB(); } while (0)

struct Params {
    const u16* x; const u16* wqkv; const u16* wo; const u16* wg; const u16* wu; const u16* wd; u16* out; char* ws;
};

constexpr int wslot(int s) { return (s == 0 ? 4 : s == 1 ? 14 : s == 2 ? 7 : s == 3 ? 0 : s == 4 ? 7 : s == 5 ? 0 : s == 6 ? 10 : s == 7 ? 3 : s == 8 ? 6 : s == 9 ? 9 : s == 10 ? 0 : 3) * GR; }
constexpr int xslot(int c) { return (c == 0 ? 0 : c == 1 ? 2 : c == 2 ? 10 : c == 3 ? 12 : c == 4 ? 3 : c == 5 ? 5 : c == 6 ? 13 : 15) * GR; }
FI constexpr int dloc0(int u) { return (u == 0 ? 12 : u < 4 ? 6 + (u - 1) : u < 7 ? 9 + (u - 4) : u < 9 ? u - 7 : 2 + (u - 9)) * GR; }
FI constexpr int dloc(int u) { return u < 13 ? dloc0(u) : u < 19 ? L_KV + (u - 13) * GR : dloc0(u - 19); }

struct Ctx {
    const Params* p;
    unsigned lds0;
    lp lds;
    int lane, w, tm, g, rb, cg, tb, nb;
    unsigned vq, vf0, vf1, vf2, vx, vlin, vd;
    const char *px, *pq, *pf, *pwd, *pwo;
};

FI void issue_x1(const Ctx& c, int chunk, int k) {
    dma16(k ? c.vx ^ 64u : c.vx, c.px + chunk * 128 + k * 8 * 1024, c.lds0 + xslot(chunk) + 16 * c.w * 128 + k * 8 * 128);
}
FI void issue_x(const Ctx& c, int chunk) { issue_x1(c, chunk, 0); issue_x1(c, chunk, 1); }
template <int S> FI void issue_w1(const Ctx& c, int k) {
    if constexpr (S >= 4) {
        const char* base = c.pf + (size_t)(S - 4) * (64 * 2752);
        dma16(k == 0 ? c.vf0 : k == 1 ? c.vf1 : c.vf2, base, c.lds0 + wslot(S) + c.g * 12288 + c.tm * 3072 + k * 1024);
    } else {
        const char* base = c.pq + (size_t)S * (128 * 3072);
        dma16(c.vq, base + (k == 0 ? 1024 : k == 1 ? 2048 : 0), c.lds0 + wslot(S) + c.w * 1024 + k * 8192);
    }
}
template <int S> FI void issue_w(const Ctx& c) { issue_w1<S>(c, 0); issue_w1<S>(c, 1); issue_w1<S>(c, 2); }
FI void issue_d(const Ctx& c, int u) {
    const char* base;
    if (u < 21) base = c.pwd + (size_t)u * 65536;
    else if (u == 21) base = c.pwd + (size_t)21 * 65536 - (c.w >= 4 ? 32768 : 0);
    else base = c.pwo + (size_t)(u - 22) * 65536;
    dma16(c.vd, base, c.lds0 + dloc(u) + c.w * 1024);
}

FI float blo(unsigned d) { return __uint_as_float(d << 16); }
FI float bhi(unsigned d) { return __uint_as_float(d & 0xffff0000u); }
template <int CTRL> FI float dppadd(float x) { return x + __builtin_bit_cast(float, __builtin_amdgcn_update_dpp(0, __builtin_bit_cast(int, x), CTRL, 0xf, 0xf, true)); }
FI void swap32add(float& u, float v) { auto rr = __builtin_amdgcn_permlane32_swap(__float_as_uint(u), __float_as_uint(v), false, false); u = __uint_as_float(rr[0]) + __uint_as_float(rr[1]); }
FI void swap16add(float& u, float v) { auto rr = __builtin_amdgcn_permlane16_swap(__float_as_uint(u), __float_as_uint(v), false, false); u = __uint_as_float(rr[0]) + __uint_as_float(rr[1]); }

struct G1State {
    bf16x8 xf[32];
    bf16x8 wq[4];
    f32x16 a0, a1, b0, b1;
    f32x16 qa, qb;
    unsigned wbf[4], wbq[4];
    unsigned xb[4];
};
constexpr int fkind(int G, int f) { return G == 0 ? (f < 8 ? (f & 3) : (f & 1)) : (f < 4 ? (f & 1) : f < 6 ? (f & 1) : f < 8 ? 2 + (f & 1) : f < 10 ? (f & 1) : 2 + (f & 1)); }
constexpr int fpos(int G, int f) { return G == 0 ? (f < 8 ? (f >> 2) : 2 + ((f - 8) >> 1)) : (f < 4 ? (f >> 1) : f < 8 ? 2 : 3); }
constexpr int qkind(int G, int f) { return G == 0 ? ((f == 1 || f == 3 || f == 7 || f == 9) ? 1 : 0) : ((f == 3 || f == 5 || f == 9 || f == 11) ? 1 : 0); }
constexpr int qpos(int G, int f) {
    return G == 0 ? (f == 0 ? 0 : f == 1 ? 0 : f == 2 ? 1 : f == 3 ? 1 : f == 4 ? 2 : f == 5 ? 3 : f == 6 ? 4 : f == 7 ? 4 : f == 8 ? 5 : f == 9 ? 5 : f == 10 ? 6 : 7)
                  : (f == 0 ? 0 : f == 1 ? 1 : f == 2 ? 2 : f == 3 ? 2 : f == 4 ? 3 : f == 5 ? 3 : f == 6 ? 4 : f == 7 ? 5 : f == 8 ? 6 : f == 9 ? 6 : f == 10 ? 7 : 7);
}
template <int G, int S> FI bf16x8 rdfrag(const G1State& st, int f) {
    if constexpr (S >= 4) { const int k = fkind(G, f); return trfrag<768>((lcp)(uintptr_t)(st.wbf[fpos(G, f)] + (k & 1) * 12288 + (k >= 2 ? 64 : (G == 0 ? 0 : 128)))); }
    else { const int e = qpos(G, f); return trfrag<256>((lcp)(uintptr_t)(st.wbq[e & 3] + (e >> 2) * 4096 + (qkind(G, f) ? 16384 : G * 8192))); }
}
template <int C> FI void rdx(G1State& st) {
#pragma unroll
    for (int e = 0; e < 4; ++e) { st.xf[4 * C + e] = ldsf((lcp)(uintptr_t)st.xb[e]); if (C < 7) st.xb[e] = (unsigned)opaque((int)(st.xb[e] + (xslot(C + 1) - xslot(C)))); }
}
template <int G, int S, bool FRESH, bool XF, int XP, int NX, class F>
FI void stage(G1State& st, F&& hook) {
    if constexpr (XF) rdx<(XF ? 2 * S : 0)>(st);
    if constexpr (FRESH) {
#pragma unroll
        for (int f = 0; f < 4; ++f) st.wq[f] = rdfrag<G, S>(st, f);
    }
    if constexpr (XF) rdx<(XF ? 2 * S + 1 : 0)>(st);
    SB();
#pragma unroll
    for (int f = 0; f < 12; ++f) {
        const bf16x8 w = st.wq[f & 3];
        if constexpr (S >= 4) {
            const int k = fkind(G, f); const bf16x8 x = st.xf[4 * (S - 4) + fpos(G, f)];
            if (k == 0) st.a0 = mfma(w, x, st.a0); else if (k == 1) st.a1 = mfma(w, x, st.a1); else if (k == 2) st.b0 = mfma(w, x, st.b0); else st.b1 = mfma(w, x, st.b1);
        } else {
            const bf16x8 x = st.xf[8 * S + qpos(G, f)];
            if (qkind(G, f)) st.qb = mfma(w, x, st.qb); else if (G == 0) st.qa = mfma(w, x, st.qa); else st.qa = mfma(x, w, st.qa);
        }
        SB();
        if (f < 8) st.wq[f & 3] = rdfrag<G, S>(st, f + 4);
        else {
            if (f == 8) {
                if constexpr (S < 3) { _Pragma("unroll") for (int j = 0; j < 4; ++j) st.wbq[j] = (unsigned)opaque((int)(st.wbq[j] + (wslot(S + 1) - wslot(S)))); }
                if constexpr (S >= 4 && S < 11) { _Pragma("unroll") for (int j = 0; j < 4; ++j) st.wbf[j] = (unsigned)opaque((int)(st.wbf[j] + (wslot(S + 1) - wslot(S)))); }
            }
            if constexpr (NX >= 0) st.wq[f & 3] = rdfrag<G, (NX >= 0 ? NX : 0)>(st, f - 8);
        }
        if constexpr (XP >= 0) { if (f == 2) rdx<(XP >= 0 ? XP : 0)>(st); if (f == 6) rdx<(XP >= 0 ? XP + 1 : 0)>(st); }
        hook(f);
        SB();
    }
}

FI u64 mix64(u64 z) { z += 0x9E3779B97F4A7C15ull; z = (z ^ (z >> 30)) * 0xBF58476D1CE4E5B9ull; z = (z ^ (z >> 27)) * 0x94D049BB133111EBull; return z ^ (z >> 31); }

constexpr unsigned SPIN_CAP = 1u << 18;
FI void set_timeout(char* ws, int lane) { if (lane == 0) __hip_atomic_store((GAS unsigned*)(ws + WS_TMO), 1u, __ATOMIC_RELAXED, __HIP_MEMORY_SCOPE_AGENT); }
FI void poll_flags(const u64* flags, int n, u64 tag, int lane, char* ws) {
    const GAS u64* pp = (const GAS u64*)flags + (lane < n ? lane : 0);
    for (unsigned spins = 0;; ++spins) {
        const u64 v = __hip_atomic_load(pp, __ATOMIC_RELAXED, __HIP_MEMORY_SCOPE_AGENT);
        if (__all(v == tag)) return;
        if (spins > SPIN_CAP) { set_timeout(ws, lane); return; }
        __builtin_amdgcn_s_sleep(2);
    }
}
FI void store_flag(char* ws, size_t off, u64 tag) { __hip_atomic_store((GAS u64*)(ws + off), tag, __ATOMIC_RELAXED, __HIP_MEMORY_SCOPE_AGENT); }

__global__ __launch_bounds__(512, 2) void fused_kernel(Params P) {
    extern __shared__ __attribute__((aligned(16))) char smem[];
    Ctx c; c.p = &P;
    const int tid = threadIdx.x; c.lane = tid & 63;
    const int w = __builtin_amdgcn_readfirstlane(tid >> 6);
    c.w = w; c.tm = w & 3; c.g = w >> 2;
    c.lds = (lp)smem; c.lds0 = (unsigned)(uintptr_t)c.lds;
    const int xcd = blockIdx.x & 7, idx = blockIdx.x >> 3;
    c.rb = 8 * (xcd >> 2) + (idx >> 2); c.cg = 4 * (xcd & 3) + (idx & 3);
    c.tb = 8 * (xcd >> 1) + (idx >> 2); c.nb = 4 * (xcd & 1) + (idx & 3);
    const int fs = c.cg < 11 ? 3 * c.cg : c.cg < 15 ? 33 + 2 * (c.cg - 11) : 40;
    const bool v0 = c.cg != 15, v2 = c.cg < 11 || c.cg == 15;
    const int lane = c.lane, h = lane >> 5, r32 = lane & 31;
    char* ws = P.ws;
    c.px = (const char*)P.x + (size_t)(128 * c.rb + 16 * w) * 1024;
    c.pf = (const char*)(c.g ? P.wu : P.wg) + (size_t)(16 * c.tm) * 2752 + fs * 64;
    u64 tag = 0; unsigned tagLo = 0, tagHi = 0; float ln_a = 0.f, ln_m = 0.f;
    const int T = 4 * c.rb + c.tm;
    const __amdgpu_buffer_rsrc_t wsr = __builtin_amdgcn_make_buffer_rsrc(ws, 0, 32u << 20, 0x00020000);
    c.pq = (const char*)P.wqkv + ((size_t)(16 * w) * 1536 + 32 * c.cg) * 2;
    c.pwd = (const char*)P.wd + (size_t)(8 * w) * 1024 + c.nb * 128;
    c.pwo = (const char*)P.wo + (size_t)(8 * w) * 1024 + c.nb * 128;
    u32x4 g0a, g0b;
#define GATHER_OFFS(tile) const int gl_ = opaque(lane); const unsigned go0 = (unsigned)(WS_CS + (c.cg * 9 + (tile)) * 2048 + (gl_ >> 3) * 256 + (gl_ & 7) * 32)
#define GATHER_ISSUE(tile) do { GATHER_OFFS(tile); aload16_sc1(g0a, P.ws, go0); aload16_sc1(g0b, P.ws + 16, go0); } while (0)
#define GATHER_OK() (g0a[1] == tagLo && g0a[3] == tagLo && g0b[1] == tagLo && g0b[3] == tagLo)
#define GATHER_RED(tile) do { const int gl_ = opaque(lane); float v0_ = __uint_as_float(g0a[0]), v1_ = __uint_as_float(g0a[2]), v2_ = __uint_as_float(g0b[0]), v3_ = __uint_as_float(g0b[2]); \
        swap32add(v0_, v2_); swap32add(v1_, v3_); swap16add(v0_, v1_); v0_ = dppadd<0x128>(v0_); \
        if ((gl_ & 8) == 0) *(LAS float*)(c.lds + L_CS + (tile) * 128 + (4 * (gl_ & 7) + 2 * (gl_ >> 5) + ((gl_ >> 4) & 1)) * 4) = v0_; } while (0)
#define GATHER_CHECK(tile) do { asm volatile("" : "+v"(g0a), "+v"(g0b)); \
        if (!__all(GATHER_OK())) { GATHER_OFFS(tile); \
            for (unsigned spins = 0;; ++spins) { __builtin_amdgcn_s_sleep(2); \
                g0a = __builtin_amdgcn_raw_buffer_load_b128(wsr, go0, 0, 16); g0b = __builtin_amdgcn_raw_buffer_load_b128(wsr, go0 + 16, 0, 16); __builtin_amdgcn_s_waitcnt(0x0F70); \
                if (__all(GATHER_OK())) break; if (spins > SPIN_CAP) { set_timeout(ws, lane); break; } } } \
        } while (0)
#define F4(v, b) (f32x4){v[b], v[(b) + 1], v[(b) + 2], v[(b) + 3]}

    const int th = w & 1, kq = w >> 1;
    const int T2 = 2 * c.tb + th;
    bf16x8 qf; bool edone_ = false;
    auto gemm1 = [&](auto Gc) {
        constexpr int G = decltype(Gc)::value;
        const int lane = opaque(c.lane), h = lane >> 5, r32 = lane & 31, tid = w * 64 + lane;
        c.vq = (lane >> 2) * 3072 + (lane & 3) * 16;
        c.vx = (lane >> 3) * 1024 + (((lane & 7) ^ (lane >> 4)) * 16); c.vlin = lane * 16;
        G1State st;
        issue_x(c, 0); issue_x(c, 1); issue_w<0>(c);
        u64 ep; aload_flag(ep, ws + WS_EPOCH, 0u);
        u32x4 ax; aload16(ax, (const char*)P.x + (size_t)(128 * c.rb + 8 * c.cg + w) * 1024, c.vlin);
        u32x4 aw0, aw1, aw2, aw3;
        const int ptile = 2 * w + (c.rb & 1); const bool pvalid = ptile <= 8;
        aw0 = aw1 = aw2 = aw3 = (u32x4){0u, 0u, 0u, 0u};
        if (pvalid) {
            const char* tb; unsigned pitch, vo;
            if (ptile < 3) { tb = (const char*)P.wqkv + (ptile == 0 ? 512 : ptile == 1 ? 1024 : 0) * 2 + 64 * c.cg; pitch = 3072; vo = c.vq; }
            else { tb = (const char*)((ptile & 1) ? P.wg : P.wu) + 64 * (fs + ((ptile - 3) >> 1)); pitch = 2752; vo = (lane >> 2) * 2752 + (lane & 3) * 16; }
            tb += (size_t)(64 * (c.rb >> 1)) * pitch;
            aload16(aw0, tb, vo); aload16(aw1, tb + 16 * pitch, vo); aload16(aw2, tb + 32 * pitch, vo); aload16(aw3, tb + 48 * pitch, vo);
        }
        c.vf0 = (lane / 12) * 2752 + (lane % 12) * 16; c.vf1 = ((lane + 64) / 12) * 2752 + ((lane + 64) % 12) * 16; c.vf2 = ((lane + 128) / 12) * 2752 + ((lane + 128) % 12) * 16;
        issue_w<4>(c);
        *(LAS unsigned*)(c.lds + L_ZERO + tid * 4) = 0u;
        if (tid < 4) { *(LAS unsigned*)(c.lds + L_ONES + tid * 4) = 0x3f803f80u; *(LAS unsigned*)(c.lds + L_ONES + 1024 + tid * 4) = 0x3f803f80u; }
        { const unsigned lb = c.lds0 + (8 * h + ((lane & 15) >> 2)) * 64 + 32 * ((lane >> 4) & 1) + 8 * (lane & 3);
#pragma unroll
          for (int j = 0; j < 4; ++j) st.wbq[j] = lb + ((j ^ c.tm) * 1024) + wslot(0); }
        { const unsigned lb = c.lds0 + (8 * h + ((lane & 15) >> 2)) * 192 + 32 * ((lane >> 4) & 1) + 8 * (lane & 3);
#pragma unroll
          for (int j = 0; j < 4; ++j) st.wbf[j] = lb + ((j ^ c.tm) * 3072) + wslot(4); }
        { const int row = 32 * c.tm + r32;
#pragma unroll
          for (int j = 0; j < 4; ++j) st.xb[j] = c.lds0 + xslot(0) + row * 128 + (((2 * (j ^ c.tm) + h) ^ ((row >> 1) & 7)) * 16); }
        st.a0 = f32x16{}; st.a1 = f32x16{}; st.b0 = f32x16{}; st.b1 = f32x16{}; st.qa = f32x16{}; st.qb = f32x16{};

        SYNC(3);
        asm volatile("" : "+v"(ep), "+v"(ax), "+v"(aw0), "+v"(aw1), "+v"(aw2), "+v"(aw3));
        tag = mix64(((u64)(unsigned)__builtin_amdgcn_readfirstlane((int)(unsigned)(ep >> 32)) << 32) | (unsigned)__builtin_amdgcn_readfirstlane((int)(unsigned)ep));
        tagLo = (unsigned)tag; tagHi = (unsigned)(tag >> 32);

        float as_ = 0.f, aq_ = 0.f, av[8];
        auto aux0 = [&](int f) {
            if (f < 4) { const float a = blo(ax[f]), b = bhi(ax[f]); as_ += a + b; aq_ = __builtin_fmaf(a, a, aq_); aq_ = __builtin_fmaf(b, b, aq_); PIN(as_); PIN(aq_); }
            if (f == 4) { as_ = dppadd<0x121>(as_); aq_ = dppadd<0x121>(aq_); as_ = dppadd<0x122>(as_); aq_ = dppadd<0x122>(aq_); PIN(as_); PIN(aq_); }
            if (f == 5) { as_ = dppadd<0x124>(as_); aq_ = dppadd<0x124>(aq_); as_ = dppadd<0x128>(as_); aq_ = dppadd<0x128>(aq_); PIN(as_); PIN(aq_); }
            if (f == 6) { swap16add(as_, as_); swap16add(aq_, aq_); swap32add(as_, as_); swap32add(aq_, aq_); PIN(as_); PIN(aq_); }
            if (f == 8) { const float mean = as_ * (1.f / 512.f), var = aq_ * (1.f / 512.f) - mean * mean, rstd = __builtin_amdgcn_rsqf(fmaxf(var, 0.f) + 1e-5f);
                if (lane == 0) __builtin_amdgcn_raw_buffer_store_b128((u32x4){__float_as_uint(mean), tagLo, __float_as_uint(rstd), tagHi}, wsr, (unsigned)(WS_ST + (128 * c.rb + 8 * c.cg + w) * 16), 0, 16); }
        };
        auto aux1 = [&](int f) {
            if (f < 4) { const int i = f; av[2 * i] = (blo(aw0[i]) + blo(aw1[i])) + (blo(aw2[i]) + blo(aw3[i])); av[2 * i + 1] = (bhi(aw0[i]) + bhi(aw1[i])) + (bhi(aw2[i]) + bhi(aw3[i])); PIN(av[2 * i]); PIN(av[2 * i + 1]); }
            if (f == 8) { swap32add(av[0], av[4]); swap32add(av[1], av[5]); swap32add(av[2], av[6]); swap32add(av[3], av[7]); PIN(av[0]); PIN(av[1]); PIN(av[2]); PIN(av[3]); }
            if (f == 9) { swap16add(av[0], av[2]); swap16add(av[1], av[3]); av[0] = dppadd<0x124>(av[0]); av[1] = dppadd<0x124>(av[1]); av[0] = dppadd<0x128>(av[0]); av[1] = dppadd<0x128>(av[1]); PIN(av[0]); PIN(av[1]); }
            if (f == 10) { const int rho = lane >> 4, col = 8 * (lane & 3) + 4 * (rho >> 1) + 2 * (rho & 1);
                const unsigned off = pvalid ? (unsigned)(WS_CS + (((c.cg * 9 + ptile) * 8 + (c.rb >> 1)) * 32 + col) * 8) : (unsigned)(WS_SCR + (blockIdx.x * 8 + w) * 256 + col * 8);
                if ((lane & 12) == 0) __builtin_amdgcn_raw_buffer_store_b128((u32x4){__float_as_uint(av[0]), tagLo, __float_as_uint(av[1]), tagLo}, wsr, off, 0, 16); }
        };
        u32x4 gs;
        const int gt1 = w < 3 ? w : w < 6 ? w - 3 : w - 6, gt2 = w < 6 ? 3 + w : w - 3;
        stage<G, 0, true, true, -1, -1>(st, [&](int f_) { if (f_ < 2) issue_x1(c, 2, f_); else if (f_ < 4) issue_x1(c, 3, f_ - 2); else if (f_ < 7) issue_w1<1>(c, f_ - 4); aux0(f_); }); SYNCV(8);
        stage<G, 4, true, false, -1, -1>(st, [&](int f_) { if (f_ < 3) issue_w1<5>(c, f_); else if (f_ < 5) issue_x1(c, 4, f_ - 3); else if (f_ < 7) issue_x1(c, 5, f_ - 5); aux1(f_); }); SYNCV(9);
        stage<G, 1, true, true, -1, -1>(st, [&](int f_) { if (f_ >= 1 && f_ < 4) issue_w1<2>(c, f_ - 1); }); SYNCV(4);
        stage<G, 5, true, false, 4, -1>(st, [&](int f_) { if (f_ < 3) issue_w1<6>(c, f_); else if (f_ < 5) issue_x1(c, 6, f_ - 3); else if (f_ < 7) issue_x1(c, 7, f_ - 5); }); SYNCV(7);
        stage<G, 2, true, false, -1, -1>(st, [&](int f_) { if (f_ >= 1 && f_ < 4) issue_w1<3>(c, f_ - 1);
            if (f_ == 8) { aload16_sc1(gs, P.ws, (unsigned)(WS_ST + (128 * c.rb + 32 * c.tm + (opaque(lane) & 31)) * 16)); GATHER_ISSUE(gt1); } }); SYNCV(6);
        stage<G, 6, true, false, 6, -1>(st, [&](int f_) { if (f_ >= 1 && f_ < 4) issue_w1<7>(c, f_ - 1); else if (f_ >= 4 && f_ < 7) issue_w1<8>(c, f_ - 4); }); SYNCV(9);
        stage<G, 3, true, false, -1, -1>(st, [&](int f_) { if (f_ >= 1 && f_ < 4) issue_w1<9>(c, f_ - 1);
            if (f_ == 10) { WAITV(9); PIN(gs);
                if (!__all(gs[1] == tagLo && gs[3] == tagHi)) { const unsigned gso = (unsigned)(WS_ST + (128 * c.rb + 32 * c.tm + (opaque(lane) & 31)) * 16);
                    for (unsigned spins = 0;; ++spins) { __builtin_amdgcn_s_sleep(2); gs = __builtin_amdgcn_raw_buffer_load_b128(wsr, gso, 0, 16); __builtin_amdgcn_s_waitcnt(0x0F70);
                        if (__all(gs[1] == tagLo && gs[3] == tagHi)) break; if (spins > SPIN_CAP) { set_timeout(ws, lane); break; } } }
                ln_m = __uint_as_float(gs[0]); ln_a = __uint_as_float(gs[2]); PIN(ln_m); PIN(ln_a);
                GATHER_CHECK(gt1); }
            if (f_ == 11) GATHER_RED(gt1); });
        { lp xp = c.lds + 13 * GR + w * 2048 + c.vlin; *(LAS f32x4*)(xp) = F4(st.qb, 8 - 8 * G); *(LAS f32x4*)(xp + 1024) = F4(st.qb, 12 - 8 * G);
          if constexpr (G == 1) { if (h == 0) { *(LAS float*)(c.lds + L_CSW + c.tm * 256 + r32 * 4) = ln_m; *(LAS float*)(c.lds + L_CSW + c.tm * 256 + 128 + r32 * 4) = ln_a; } } }
        SYNC(3);
        issue_w<10>(c);
        { const int lane = opaque(c.lane), h = lane >> 5, r32 = lane & 31;
          const unsigned o = (unsigned)(WS_KV + ((size_t)c.cg * 64 + T) * 4096) + lane * 16 + G * 2048;
          f32x16 y;
          if constexpr (G == 0) {
#pragma unroll
              for (int q = 0; q < 4; ++q) { const f32x4 cs = ldsf4((lcp)c.lds + L_CS + (8 * q + 4 * h) * 4);
#pragma unroll
                  for (int i = 0; i < 4; ++i) y[4 * q + i] = ln_a * __builtin_fmaf(-ln_m, cs[i], st.qa[4 * q + i]); }
          } else {
              const float cso = *(const LAS float*)(c.lds + L_CS + 128 + r32 * 4);
#pragma unroll
              for (int q = 0; q < 4; ++q) { const f32x4 mm = ldsf4((lcp)c.lds + L_CSW + c.tm * 256 + (8 * q + 4 * h) * 4), aa = ldsf4((lcp)c.lds + L_CSW + c.tm * 256 + 128 + (8 * q + 4 * h) * 4);
#pragma unroll
                  for (int i = 0; i < 4; ++i) y[4 * q + i] = aa[i] * __builtin_fmaf(-mm[i], cso, st.qa[4 * q + i]); }
          }
          const u32x4 k0 = pack8(y, 0), k1 = pack8(y, 8);
          __builtin_amdgcn_raw_buffer_store_b128(k0, wsr, o, 0, 16); __builtin_amdgcn_raw_buffer_store_b128(k1, wsr, o + 1024, 0, 16);
          if (w == 0 && lane == 0) store_flag(ws, WS_SF + (size_t)blockIdx.x * 8, tag);
          *(LAS u32x4*)(c.lds + L_KVR + c.tm * 4096 + G * 2048 + lane * 16) = k0; *(LAS u32x4*)(c.lds + L_KVR + c.tm * 4096 + G * 2048 + 1024 + lane * 16) = k1;
          SB();
          const f32x4 c0 = ldsf4((lcp)c.lds + L_CS + 2 * 128 + (16 * G + 4 * h) * 4), c1 = ldsf4((lcp)c.lds + L_CS + 2 * 128 + (16 * G + 8 + 4 * h) * 4);
          const lcp xp = (lcp)c.lds + 13 * GR + (w ^ 4) * 2048 + lane * 16;
          const f32x4 p0 = ldsf4(xp), p1 = ldsf4(xp + 1024);
          const float sc = ln_a * (0.25f * 1.4426950408889634f);
#pragma unroll
          for (int i = 0; i < 4; ++i) { y[i] = sc * __builtin_fmaf(-ln_m, c0[i], st.qb[8 * G + i] + p0[i]); y[4 + i] = sc * __builtin_fmaf(-ln_m, c1[i], st.qb[8 * G + 4 + i] + p1[i]); }
          qf = __builtin_bit_cast(bf16x8, pack8(y, 0)); PIN(qf); }
        stage<G, 7, true, false, -1, 8>(st, [&](int) {}); SYNC(5);
        { const int ln = opaque(lane); c.vd = (ln >> 3) * 1024 + (((ln & 7) ^ (((ln >> 4) & 1) << 2)) * 16); }
        stage<G, 8, false, false, -1, 9>(st, [&](int f_) { if (f_ >= 1 && f_ < 4) issue_w1<11>(c, f_ - 1); if (f_ == 4) issue_d(c, 0); }); SYNCV(6);
        stage<G, 9, false, false, -1, 10>(st, [&](int f_) { if (f_ >= 1 && f_ < 4) issue_d(c, f_);
            if (f_ == 5) { WAITV(7); if (lane == 0) store_flag(ws, WS_KVF + ((size_t)(c.cg * 64 + T) * 2 + G) * 8, tag); }
            if (f_ == 8) GATHER_ISSUE(gt2); }); SYNCV(7);
        u64 kf_;
        stage<G, 10, false, false, -1, 11>(st, [&](int f_) { if (f_ >= 1 && f_ < 4) issue_d(c, 3 + f_);
            if (f_ == 6) { WAITV(3); GATHER_CHECK(gt2); GATHER_RED(gt2); }
            if (f_ == 10) { const int tl = 4 * c.rb - 8 + w; aload_flag(kf_, P.ws, (unsigned)(WS_KVF + ((c.cg * 64 + (tl < 0 ? 0 : tl)) * 2 + (opaque(lane) & 1)) * 8)); } }); SYNCV(63);
        stage<G, 11, false, false, -1, -1>(st, [&](int f_) { if (f_ >= 1 && f_ < 3) issue_d(c, 6 + f_);
            if (f_ == 6) { WAITV(2); asm volatile("" : "+v"(kf_)); const int tl = 4 * c.rb - 8 + w;
                if (tl >= 0) {
                    if (!__all(kf_ == tag)) poll_flags((const u64*)(ws + WS_KVF) + (c.cg * 64 + tl) * 2, 2, tag, lane, ws);
                    const char* base = ws + WS_KV + ((size_t)c.cg * 64 + tl) * 4096; const unsigned dst = c.lds0 + L_KV + w * 4096;
                    dma16_sc1(c.vlin, base, dst); dma16_sc1(c.vlin, base + 1024, dst + 1024); dma16_sc1(c.vlin, base + 2048, dst + 2048); dma16_sc1(c.vlin, base + 3072, dst + 3072); } } });
        {
          lp xp = c.lds + 2 * GR + w * 4096 + c.vlin;
          SYNCV(63);
          *(LAS f32x4*)(xp) = F4(st.b0, 8 - 8 * G); *(LAS f32x4*)(xp + 1024) = F4(st.b0, 12 - 8 * G); *(LAS f32x4*)(xp + 2048) = F4(st.b1, 8 - 8 * G); *(LAS f32x4*)(xp + 3072) = F4(st.b1, 12 - 8 * G); }
        SYNC(63);
        {
          const int lane = opaque(c.lane), h = lane >> 5;
          const float a2 = ln_a * ln_a;
          { u32x4 y0, y1;
#pragma unroll
            for (int q = 0; q < 4; ++q) {
                const f32x4 cg_ = ldsf4((lcp)c.lds + L_CS + (3 + 4 * G) * 128 + (8 * q + 4 * h) * 4), cu_ = ldsf4((lcp)c.lds + L_CS + (4 + 4 * G) * 128 + (8 * q + 4 * h) * 4);
                float y[4];
#pragma unroll
                for (int i = 0; i < 4; ++i) { const float g_ = fmaxf(__builtin_fmaf(-ln_m, cg_[i], st.a0[4 * q + i]), 0.f), u_ = __builtin_fmaf(-ln_m, cu_[i], st.a1[4 * q + i]); y[i] = (g_ * a2) * u_; }
                const unsigned lo = pk2(y[0], y[1]), hi = pk2(y[2], y[3]);
                if (q < 2) { y0[2 * q] = lo; y0[2 * q + 1] = hi; } else { y1[2 * q - 4] = lo; y1[2 * q - 3] = hi; }
            }
            if (G == 0 ? v0 : v2) { const unsigned o_ = (unsigned)(WS_C + ((size_t)T * NKS + 2 * (fs + 2 * G)) * 1024) + lane * 16;
                __builtin_amdgcn_raw_buffer_store_b128(y0, wsr, o_, 0, 16); __builtin_amdgcn_raw_buffer_store_b128(y1, wsr, o_ + 1024, 0, 16); } }
          { const lcp xp = (lcp)c.lds + 2 * GR + (w ^ 4) * 4096 + lane * 16;
            u32x4 y0;
#pragma unroll
            for (int q = 0; q < 2; ++q) {
                const f32x4 pg = ldsf4(xp + q * 1024), pu = ldsf4(xp + 2048 + q * 1024);
                const f32x4 cg_ = ldsf4((lcp)c.lds + L_CS + 5 * 128 + (16 * G + 8 * q + 4 * h) * 4), cu_ = ldsf4((lcp)c.lds + L_CS + 6 * 128 + (16 * G + 8 * q + 4 * h) * 4);
                float y[4];
#pragma unroll
                for (int i = 0; i < 4; ++i) { const float g_ = fmaxf(__builtin_fmaf(-ln_m, cg_[i], st.b0[8 * G + 4 * q + i] + pg[i]), 0.f), u_ = __builtin_fmaf(-ln_m, cu_[i], st.b1[8 * G + 4 * q + i] + pu[i]); y[i] = (g_ * a2) * u_; }
                y0[2 * q] = pk2(y[0], y[1]); y0[2 * q + 1] = pk2(y[2], y[3]);
            }
            __builtin_amdgcn_raw_buffer_store_b128(y0, wsr, (unsigned)(WS_C + ((size_t)T * NKS + 2 * (fs + 1) + G) * 1024) + lane * 16, 0, 16); } }
        if (G == 0 ? v0 : v2) { SYNC(3); } else { SYNC(1); }
        if constexpr (G == 1) __builtin_amdgcn_s_setprio(1);
    };
    if (c.g == 0) gemm1(std::integral_constant<int, 0>{}); else gemm1(std::integral_constant<int, 1>{});
    const int lane2 = opaque(lane);
    const unsigned cbase = (unsigned)(WS_C + ((size_t)T2 * NKS + kq) * 1024) + lane2 * 16;
    bf16x8 hf[22];
    u64 hfl; bool hrdy_ = false;
#define LDH(i) do { const bool ok_ = ((i) < 21) || (kq < 2); hf[i] = __builtin_bit_cast(bf16x8, __builtin_amdgcn_raw_buffer_load_b128(wsr, ok_ ? cbase + (i) * 4096 : cbase, 0, 16)); } while (0)
    {
        const int lane = lane2, h = lane >> 5, r32 = lane & 31;
        const int i0 = T >= 8 ? 0 : 8 - T;
        const unsigned kb = c.lds0 + L_KV + c.tm * 4096 + c.g * 1024 + lane * 16;
        const unsigned lanebit = r32 < 16 ? 1u : 0u;
        const unsigned vb = c.lds0 + (r32 < 16 ? (unsigned)(((r32 & 15) + 16 * c.g + 32 * h) * 16) : (unsigned)L_ONES);
        auto koff = [&](int i) -> unsigned { return (unsigned)((i >= i0 ? i : 7) * 4096); };
        auto rdk = [&](int i) -> bf16x8 { return ldsf((lcp)(uintptr_t)(kb + koff(i))); };
        auto vaddr = [&](int i) -> unsigned { const bool valid = i >= i0; const unsigned d = valid ? 0u : (unsigned)(L_ZERO - L_ONES), A = valid ? (unsigned)(L_KV + 2048 + (c.tm + i) * 4096) : (unsigned)L_ZERO;
                                              return lanebit * (A - d) + (vb + d); };
        auto dmask = [&](int r) -> bool { u64 mk = 0;
#pragma unroll
            for (int l = 0; l < 64; ++l) { const int kl = (r & 3) + 8 * (r >> 2) + 4 * (l >> 5); if (kl <= (l & 31)) mk |= 1ull << l; }
            return __builtin_amdgcn_inverse_ballot_w64(mk); };
        const f32x16 zero = f32x16{};
        float m0 = -1e30f, m1 = -1e30f;
#define MAXT(S) do { _Pragma("unroll") for (int r = 0; r < 16; r += 4) { m0 = fmaxf(fmaxf(m0, S[r]), S[r + 1]); m1 = fmaxf(fmaxf(m1, S[r + 2]), S[r + 3]); } PIN(m0); PIN(m1); } while (0)
#define EXPT(S) do { _Pragma("unroll") for (int r = 0; r < 16; ++r) S[r] = __builtin_amdgcn_exp2f(S[r]); } while (0)
#define H_PUBLISH(N) do { WAITV(N); if (lane == 0) store_flag(ws, WS_HF + ((size_t)T * 32 + 2 * c.cg + c.g) * 8, tag); } while (0)
#define H_FLAGS() aload_flag(hfl, P.ws, (unsigned)(WS_HF + (T2 * 32 + (lane & 31)) * 8))
        f32x16 o = f32x16{};
        float m; bool hrdy = false;
        if (T == 0) {
            f32x16 s = mfma(ldsf((lcp)(uintptr_t)(kb + 8 * 4096)), qf, zero);
#pragma unroll
            for (int r = 0; r < 16; ++r) s[r] = dmask(r) ? s[r] : -1e30f;
            MAXT(s);
            m = fmaxf(m0, m1);
            { auto rr = __builtin_amdgcn_permlane32_swap(__float_as_uint(m), __float_as_uint(m), false, false); m = fmaxf(__uint_as_float(rr[0]), __uint_as_float(rr[1])); }
#pragma unroll
            for (int r = 0; r < 16; ++r) s[r] = __builtin_amdgcn_exp2f(s[r] - m);
            const unsigned va = vaddr(8);
            o = mfma(ldsf((lcp)(uintptr_t)va), __builtin_bit_cast(bf16x8, pack8(s, 0)), o);
            o = mfma(ldsf((lcp)(uintptr_t)(va + 1024)), __builtin_bit_cast(bf16x8, pack8(s, 8)), o);
            issue_d(c, 9); issue_d(c, 10); issue_d(c, 11); issue_d(c, 12);
            H_PUBLISH(4);
        } else {
            bf16x8 k0 = rdk(0), k8 = rdk(8), k1 = rdk(1), k2 = rdk(2), k3 = rdk(3);
            f32x16 sA, sB, sC;
            sA = mfma_pin0(k0, qf); sB = mfma_pin0(k8, qf); SB();
            k0 = rdk(4); k8 = rdk(5); sC = mfma_pin0(k1, qf); k1 = rdk(6); SB();
            asm volatile("s_nop 7\n\ts_nop 3"); SB();
#pragma unroll
            for (int r = 0; r < 16; ++r) sA[r] = dmask(r) ? sB[r] : sA[r];
            PIN(sA); SB();
            sB = mfma_pin0(k2, qf); k2 = rdk(7); SB();
            MAXT(sA); SB();
            H_PUBLISH(0);
            unsigned pfw = 0;
            if (w < 3) { const int id = w * 64 + lane; int row = 832 + 8 * id + (idx >> 2); if (row > 1887) row = 1887;
                aprefetch(pfw, (row < 1376 ? (const char*)P.wd + (size_t)row * 1024 : (const char*)P.wo + (size_t)(row - 1376) * 1024) + c.nb * 128); }
            sA = mfma_pin0(k3, qf); issue_d(c, 9); SB(); MAXT(sC); SB();
            sC = mfma_pin0(k0, qf); issue_d(c, 10); SB(); MAXT(sB); SB();
            sB = mfma_pin0(k8, qf); issue_d(c, 11); SB(); MAXT(sA); SB();
            sA = mfma_pin0(k1, qf); issue_d(c, 12); SB(); MAXT(sC); SB();
            sC = mfma_pin0(k2, qf); k0 = rdk(0); k8 = rdk(8); k1 = rdk(1); SB(); MAXT(sB); SB();
            MAXT(sA); SB(); MAXT(sC);
            m = fmaxf(m0, m1);
            { auto rr = __builtin_amdgcn_permlane32_swap(__float_as_uint(m), __float_as_uint(m), false, false); m = fmaxf(__uint_as_float(rr[0]), __uint_as_float(rr[1])); }
            f32x16 cm;
#pragma unroll
            for (int r = 0; r < 16; ++r) cm[r] = -m;
            PIN(cm);
            asm volatile("s_nop 1"); sA = mfma_pin(k0, qf, cm); sB = mfma_pin(k8, qf, cm); sC = mfma_pin(k1, qf, cm); SB();
            bf16x8 va0, va1, vb0, vb1;
            { const unsigned a0 = vaddr(0), a8 = vaddr(8); va0 = ldsf((lcp)(uintptr_t)a0); va1 = ldsf((lcp)(uintptr_t)(a0 + 1024)); vb0 = ldsf((lcp)(uintptr_t)a8); vb1 = ldsf((lcp)(uintptr_t)(a8 + 1024)); }
            k0 = rdk(2);
            unsigned mp[8];
#pragma unroll
            for (int j = 0; j < 8; ++j) mp[j] = (dmask(2 * j) ? 0xffffu : 0u) | (dmask(2 * j + 1) ? 0xffff0000u : 0u);
            SB(); asm volatile("s_nop 7"); SB();
#pragma unroll
            for (int r = 0; r < 16; ++r) sA[r] = dmask(r) ? sB[r] : sA[r];
            EXPT(sA);
            { const u32x4 p0 = pack8(sA, 0), p1 = pack8(sA, 8); u32x4 d0, d1, o0, o1;
#pragma unroll
              for (int j = 0; j < 4; ++j) { d0[j] = p0[j] & mp[j]; d1[j] = p1[j] & mp[4 + j]; o0[j] = p0[j] & ~mp[j]; o1[j] = p1[j] & ~mp[4 + j]; }
              o = mfma(va0, __builtin_bit_cast(bf16x8, o0), o); o = mfma(va1, __builtin_bit_cast(bf16x8, o1), o);
              o = mfma(vb0, __builtin_bit_cast(bf16x8, d0), o); o = mfma(vb1, __builtin_bit_cast(bf16x8, d1), o); }
            { const unsigned a1 = vaddr(1), a2 = vaddr(2); va0 = ldsf((lcp)(uintptr_t)a1); va1 = ldsf((lcp)(uintptr_t)(a1 + 1024)); vb0 = ldsf((lcp)(uintptr_t)a2); vb1 = ldsf((lcp)(uintptr_t)(a2 + 1024)); }
            SB();
#define EXP8(S, b) do { _Pragma("unroll") for (int r = (b); r < (b) + 8; ++r) S[r] = __builtin_amdgcn_exp2f(S[r]); } while (0)
#define PK4(S, b) (u32x4){pk2(S[b], S[(b) + 1]), pk2(S[(b) + 2], S[(b) + 3]), pk2(S[(b) + 4], S[(b) + 5]), pk2(S[(b) + 6], S[(b) + 7])}
#define TILE2(i, S, V0, V1, SN) do { \
                if ((i) + 1 <= 7) { SN = mfma_pin(k0, qf, cm); SB(); if ((i) + 2 <= 7) k0 = rdk((i) + 2); } \
                EXP8(S, 0); { u32x4 p_ = PK4(S, 0); PIN(p_); SB(); o = mfma(V0, __builtin_bit_cast(bf16x8, p_), o); SB(); } \
                EXP8(S, 8); { u32x4 p_ = PK4(S, 8); PIN(p_); SB(); o = mfma(V1, __builtin_bit_cast(bf16x8, p_), o); SB(); } \
                if ((i) + 2 <= 7) { const unsigned a_ = vaddr((i) + 2); V0 = ldsf((lcp)(uintptr_t)a_); V1 = ldsf((lcp)(uintptr_t)(a_ + 1024)); } SB(); } while (0)
            TILE2(1, sC, va0, va1, sB); TILE2(2, sB, vb0, vb1, sC);
            H_FLAGS();
            TILE2(3, sC, va0, va1, sB); TILE2(4, sB, vb0, vb1, sC);
            WAITV(0); asm volatile("" : "+v"(hfl), "+v"(pfw));
            hrdy = __all(hfl == tag);
            if (hrdy) { LDH(0); LDH(1); LDH(2); LDH(3); LDH(4); LDH(5); LDH(6); }
            TILE2(5, sC, va0, va1, sB);
            if (hrdy) { LDH(7); LDH(8); LDH(9); LDH(10); LDH(11); LDH(12); LDH(13); }
            TILE2(6, sB, vb0, vb1, sC);
            if (hrdy) { LDH(14); LDH(15); LDH(16); LDH(17); LDH(18); LDH(19); LDH(20); LDH(21); }
            TILE2(7, sC, va0, va1, sB);
        }
        const float inv = __builtin_amdgcn_rcpf(o[8]);
        f32x16 y;
#pragma unroll
        for (int r = 0; r < 8; ++r) y[r] = o[r] * inv;
        const unsigned oo = (unsigned)(WS_C + ((size_t)T * NKS + 86 + 2 * c.cg + c.g) * 1024) + lane * 16;
        const u32x4 ov = pack8(y, 0);
        __builtin_amdgcn_raw_buffer_store_b128(ov, wsr, oo, 0, 16);
        if (!hrdy) {
            poll_flags((const u64*)(ws + WS_HF) + T2 * 32, 32, tag, lane, ws);
#pragma unroll
            for (int i = 0; i < 22; ++i) LDH(i);
        }
        hrdy_ = hrdy;
        __builtin_amdgcn_s_waitcnt(0x0F71);
        __builtin_amdgcn_s_setprio(0);
    }
    {
        const int lane = opaque(lane2), h = lane >> 5, r32 = lane & 31, q4 = (lane & 15) >> 2, p4 = lane & 3, b16 = (lane >> 4) & 1;
        f32x16 acc0 = f32x16{}, acc1 = f32x16{};
        const unsigned wl = c.lds0 + kq * 2048 + (4 * h + q4) * 128 + 32 * b16 + 8 * p4, swz = ((q4 >> 1) & 1) << 6;
        const unsigned wl0 = wl + (0u ^ swz), wl1 = wl + (64u ^ swz);
        bf16x8 wa[3], wb_[3];
#define RDW(u) do { wa[(u) % 3] = trfrag<1024>((lcp)(uintptr_t)(wl0 + dloc(u))); wb_[(u) % 3] = trfrag<1024>((lcp)(uintptr_t)(wl1 + dloc(u))); } while (0)
#define G2STEP(u, frag, nxt) do { acc0 = mfma(wa[(u) % 3], frag, acc0); acc1 = mfma(wb_[(u) % 3], frag, acc1); SB(); if ((nxt) >= 0) RDW((nxt) < 0 ? 0 : (nxt)); SB(); } while (0)
        SYNC(63);
        const bool ew = blockIdx.x == 0 && w == 7; u32x4 sf0 = {0u, 0u, 0u, 0u}, sf1 = sf0; bool edone = false;
        if (ew) { aload16_sc1(sf0, P.ws + WS_SF, (unsigned)(lane * 32)); aload16_sc1(sf1, P.ws + WS_SF + 16, (unsigned)(lane * 32)); }
        RDW(0); RDW(1); RDW(2);
        G2STEP(0, hf[0], 3);
        issue_d(c, 13); G2STEP(1, hf[1], 4); issue_d(c, 14); G2STEP(2, hf[2], 5); issue_d(c, 15); G2STEP(3, hf[3], 6); issue_d(c, 16); G2STEP(4, hf[4], 7); issue_d(c, 17); G2STEP(5, hf[5], 8); issue_d(c, 18);
        if (hrdy_) { WAITV(6); }
        if (lane == 0) store_flag(ws, WS_OF + ((size_t)T * 32 + 2 * c.cg + c.g) * 8, tag);
        SYNCV(63);
        G2STEP(6, hf[6], 9); issue_d(c, 19); G2STEP(7, hf[7], 10); issue_d(c, 20); G2STEP(8, hf[8], 11); issue_d(c, 21); G2STEP(9, hf[9], 12); issue_d(c, 22);
        G2STEP(10, hf[10], -1); issue_d(c, 23); G2STEP(11, hf[11], -1); issue_d(c, 24); G2STEP(12, hf[12], -1);
        SYNCV(7);
        if (ew) { asm volatile("" : "+v"(sf0), "+v"(sf1));
            edone = __all(sf0[0] == tagLo && sf0[1] == tagHi && sf0[2] == tagLo && sf0[3] == tagHi && sf1[0] == tagLo && sf1[1] == tagHi && sf1[2] == tagLo && sf1[3] == tagHi);
            if (edone && lane == 0) store_flag(ws, WS_EPOCH, tag); }
        edone_ = edone;
        RDW(13); RDW(14); RDW(15);
        u32x4 xres;
        u64 ofl;
        issue_d(c, 25); G2STEP(13, hf[13], 16); issue_d(c, 26); G2STEP(14, hf[14], 17); issue_d(c, 27); G2STEP(15, hf[15], 18); issue_d(c, 28);
        aload_flag(ofl, P.ws, (unsigned)(WS_OF + (T2 * 32 + (lane & 31)) * 8));
        G2STEP(16, hf[16], -1); issue_d(c, 29);
        xres = *(const u32x4*)(P.x + (size_t)(64 * c.tb + 8 * w + (lane >> 3)) * 512 + 64 * c.nb + (lane & 7) * 8);
        G2STEP(17, hf[17], -1); G2STEP(18, hf[18], -1);
        WAITV(2); asm volatile("" : "+v"(ofl));
        if (!__all(ofl == tag)) poll_flags((const u64*)(ws + WS_OF) + T2 * 32, 32, tag, lane, ws);
        bf16x8 of[8];
#pragma unroll
        for (int i = 0; i < 8; ++i) of[i] = __builtin_bit_cast(bf16x8, __builtin_amdgcn_raw_buffer_load_b128(wsr, cbase + (86 + 4 * i) * 1024, 0, 16));
        SYNCV(63);
        RDW(19); RDW(20); RDW(21);
        G2STEP(19, hf[19], 22); G2STEP(20, hf[20], 23);
        if (kq < 2) { acc0 = mfma(wa[21 % 3], hf[21], acc0); acc1 = mfma(wb_[21 % 3], hf[21], acc1); }
        SB(); RDW(24); SB();
        G2STEP(22, of[0], -1); G2STEP(23, of[1], -1); G2STEP(24, of[2], -1);
        SYNCV(63);
        RDW(25); RDW(26); RDW(27);
        G2STEP(25, of[3], 28); G2STEP(26, of[4], 29); G2STEP(27, of[5], -1); G2STEP(28, of[6], -1); G2STEP(29, of[7], -1);
        { lp rbp = c.lds + (kq < 2 ? 4 * GR : 13 * GR - 32768) + (kq * 2 + th) * 8192;
#pragma unroll
          for (int q = 0; q < 4; ++q) { *(LAS f32x4*)(rbp + q * 1024 + ((lane + 2 * q) & 63) * 16) = F4(acc0, 4 * q); *(LAS f32x4*)(rbp + 4096 + q * 1024 + ((lane + 2 * (4 + q)) & 63) * 16) = F4(acc1, 4 * q); } }
        SYNC(63);
        {
          const int t = 8 * w + (lane >> 3), cc = lane & 7;
          const unsigned a0 = (unsigned)((t >> 5) * 8192 + cc * 1024), s0i = (unsigned)(((t & 31) + 2 * cc) & 63) * 16, s1i = (unsigned)(((t & 31) + 32 + 2 * cc) & 63) * 16;
          f32x4 s0 = {0.f, 0.f, 0.f, 0.f}, s1 = s0;
#pragma unroll
          for (int k = 0; k < 4; ++k) { const lcp bp = (lcp)c.lds + (k < 2 ? 4 * GR : 13 * GR - 32768) + k * 16384 + a0; s0 += ldsf4(bp + s0i); s1 += ldsf4(bp + s1i); }
          u32x4 ov;
          ov[0] = pk2(s0[0] + blo(xres[0]), s0[1] + bhi(xres[0])); ov[1] = pk2(s0[2] + blo(xres[1]), s0[3] + bhi(xres[1]));
          ov[2] = pk2(s1[0] + blo(xres[2]), s1[1] + bhi(xres[2])); ov[3] = pk2(s1[2] + blo(xres[3]), s1[3] + bhi(xres[3]));
          *(u32x4*)(P.out + (size_t)(64 * c.tb + t) * 512 + 64 * c.nb + cc * 8) = ov; }
    }
    if (blockIdx.x == 0 && w == 7 && !edone_) {
        const GAS u64* pp = (const GAS u64*)(ws + WS_SF) + lane * 4;
        for (unsigned spins = 0;; ++spins) { bool ok = true;
#pragma unroll
            for (int j = 0; j < 4; ++j) ok &= __hip_atomic_load(pp + j, __ATOMIC_RELAXED, __HIP_MEMORY_SCOPE_AGENT) == tag;
            if (__all(ok)) break; if (spins > SPIN_CAP) { set_timeout(ws, lane); break; } __builtin_amdgcn_s_sleep(2); }
        if (lane == 0) store_flag(ws, WS_EPOCH, tag);
    }
}

extern "C" void kernel_launch(void* const* d_in, const int* in_sizes, int n_in, void* d_out, int out_size, void* d_ws, size_t ws_size, hipStream_t stream) {
    Params p;
    p.x = (const u16*)d_in[0]; p.wqkv = (const u16*)d_in[1]; p.wo = (const u16*)d_in[2];
    p.wg = (const u16*)d_in[4]; p.wu = (const u16*)d_in[5]; p.wd = (const u16*)d_in[6];
    p.out = (u16*)d_out; p.ws = (char*)d_ws;
    (void)hipFuncSetAttribute((const void*)fused_kernel, hipFuncAttributeMaxDynamicSharedMemorySize, LDS_BYTES);
    fused_kernel<<<256, 512, LDS_BYTES, stream>>>(p);
}
```

```cpp
#include <hip/hip_runtime.h>
#include <stdint.h>
#include <type_traits>

#define FI __device__ __forceinline__
#define LAS __attribute__((address_space(3)))
#define GAS __attribute__((address_space(1)))
typedef unsigned short u16;
typedef unsigned long long u64;
typedef short bf16x8 __attribute__((ext_vector_type(8)));
typedef short s16x4 __attribute__((ext_vector_type(4)));
typedef float f32x16 __attribute__((ext_vector_type(16)));
typedef float f32x4 __attribute__((ext_vector_type(4)));
typedef float f32x2 __attribute__((ext_vector_type(2)));
typedef unsigned u32x4 __attribute__((ext_vector_type(4)));
typedef __bf16 bf16x2_t __attribute__((ext_vector_type(2)));
typedef LAS const char* lcp;
typedef LAS char* lp;

constexpr int GR = 8192;
constexpr int L_KV = 13 * GR;
constexpr int L_KVR = 17 * GR;
constexpr int L_MISC = 19 * GR;
constexpr int L_CS = L_MISC;
constexpr int L_CSW = L_MISC + 2048;
constexpr int L_ZERO = L_MISC + 4096;
constexpr int L_ONES = L_MISC + 6144;
constexpr int LDS_BYTES = 163840;

constexpr size_t WS_EPOCH = 0, WS_TMO = 64;
constexpr size_t WS_SF = 4096;
constexpr size_t WS_KVF = 8192;
constexpr size_t WS_HF = 24576;
constexpr size_t WS_OF = 40960;
constexpr size_t WS_ST = 65536;
constexpr size_t WS_CS = 131072;
constexpr size_t WS_SCR = 768u << 10;
constexpr size_t WS_KV = 2u << 20;
constexpr size_t WS_C = 8u << 20;
constexpr int NKS = 118;

FI f32x16 mfma(bf16x8 a, bf16x8 b, f32x16 c) { return __builtin_amdgcn_mfma_f32_32x32x16_bf16(a, b, c, 0, 0, 0); }
FI f32x16 mfma_pin(bf16x8 a, bf16x8 b, const f32x16& c) { f32x16 d; asm volatile("v_mfma_f32_32x32x16_bf16 %0, %1, %2, %3" : "=&v"(d) : "v"(a), "v"(b), "v"(c)); return d; }
FI f32x16 mfma_pin0(bf16x8 a, bf16x8 b) { f32x16 d; asm volatile("v_mfma_f32_32x32x16_bf16 %0, %1, %2, 0" : "=&v"(d) : "v"(a), "v"(b)); return d; }
FI unsigned pk2(float lo, float hi) { f32x2 v = {lo, hi}; bf16x2_t r = __builtin_convertvector(v, bf16x2_t); return __builtin_bit_cast(unsigned, r); }
FI u32x4 pack8(const f32x16& y, int b) { u32x4 r; r[0] = pk2(y[b], y[b + 1]); r[1] = pk2(y[b + 2], y[b + 3]); r[2] = pk2(y[b + 4], y[b + 5]); r[3] = pk2(y[b + 6], y[b + 7]); return r; }
typedef short v4i16_t __attribute__((ext_vector_type(4)));
FI s16x4 trd(lcp p) { return __builtin_bit_cast(s16x4, __builtin_amdgcn_ds_read_tr16_b64_v4i16((LAS v4i16_t*)p)); }
template <int D2> FI bf16x8 trfrag(lcp p) { s16x4 lo = trd(p), hi = trd(p + D2); return __builtin_shufflevector(lo, hi, 0, 1, 2, 3, 4, 5, 6, 7); }
FI bf16x8 ldsf(lcp p) { return *(const LAS bf16x8*)p; }
FI f32x4 ldsf4(lcp p) { return *(const LAS f32x4*)p; }

FI void dma16(unsigned voff, const void* sbase, unsigned dst) {
    asm volatile("s_mov_b32 m0, %2\n\ts_nop 0\n\tglobal_load_lds_dwordx4 %0, %1" :: "v"(voff), "s"(sbase), "s"(__builtin_amdgcn_readfirstlane(dst)) : "memory", "m0");
}
FI void dma16_sc1(unsigned voff, const void* sbase, unsigned dst) {
    asm volatile("s_mov_b32 m0, %2\n\ts_nop 0\n\tglobal_load_lds_dwordx4 %0, %1 sc1" :: "v"(voff), "s"(sbase), "s"(__builtin_amdgcn_readfirstlane(dst)) : "memory", "m0");
}
FI void aload16(u32x4& v, const void* base, unsigned voff) { asm volatile("global_load_dwordx4 %0, %1, %2" : "=v"(v) : "v"(voff), "s"(base) : "memory"); }
FI void aload16_sc1(u32x4& v, const void* base, unsigned voff) { asm volatile("global_load_dwordx4 %0, %1, %2 sc1" : "=v"(v) : "v"(voff), "s"(base) : "memory"); }
FI void aload_flag(u64& v, const char* base, unsigned voff) { asm volatile("global_load_dwordx2 %0, %1, %2 sc1" : "=v"(v) : "v"(voff), "s"(base) : "memory"); }

FI void aprefetch(unsigned& d, const char* p) { asm volatile("global_load_dword %0, %1, off" : "+v"(d) : "v"(p) : "memory"); }
#define SB() __builtin_amdgcn_sched_barrier(0)
#define PIN(x) asm volatile("" : "+v"(x))
FI int opaque(int v) { asm volatile("" : "+v"(v)); return v; }
#define SYNC(N) do { SB(); asm volatile("s_waitcnt vmcnt(" #N ") lgkmcnt(0)\n\ts_barrier" ::: "memory"); SB(); } while (0)
#define SYNCV(N) do { SB(); asm volatile("s_waitcnt vmcnt(" #N ")\n\ts_barrier" ::: "memory"); SB(); } while (0)
#define WAITV(N) do { SB(); asm volatile("s_waitcnt vmcnt(" #N ")" ::: "memory"); SB(); } while (0)

struct Params {
    const u16* x; const u16* wqkv; const u16* wo; const u16* wg; const u16* wu; const u16* wd; u16* out; char* ws;
};

constexpr int wslot(int s) { return (s == 0 ? 4 : s == 1 ? 14 : s == 2 ? 7 : s == 3 ? 0 : s == 4 ? 7 : s == 5 ? 0 : s == 6 ? 10 : s == 7 ? 3 : s == 8 ? 6 : s == 9 ? 9 : s == 10 ? 0 : 3) * GR; }
constexpr int xslot(int c) { return (c == 0 ? 0 : c == 1 ? 2 : c == 2 ? 10 : c == 3 ? 12 : c == 4 ? 3 : c == 5 ? 5 : c == 6 ? 13 : 15) * GR; }
FI constexpr int dloc0(int u) { return (u == 0 ? 12 : u < 4 ? 6 + (u - 1) : u < 7 ? 9 + (u - 4) : u < 9 ? u - 7 : 2 + (u - 9)) * GR; }
FI constexpr int dloc(int u) { return u < 13 ? dloc0(u) : u < 19 ? L_KV + (u - 13) * GR : dloc0(u - 19); }

struct Ctx {
    const Params* p;
    unsigned lds0;
    lp lds;
    int lane, w, tm, g, rb, cg, tb, nb;
    unsigned vq, vf0, vf1, vf2, vx, vlin, vd;
    const char *px, *pq, *pf, *pwd, *pwo;
};

FI void issue_x1(const Ctx& c, int chunk, int k) {
    dma16(k ? c.vx ^ 64u : c.vx, c.px + chunk * 128 + k * 8 * 1024, c.lds0 + xslot(chunk) + 16 * c.w * 128 + k * 8 * 128);
}
FI void issue_x(const Ctx& c, int chunk) { issue_x1(c, chunk, 0); issue_x1(c, chunk, 1); }
template <int S> FI void issue_w1(const Ctx& c, int k) {
    if constexpr (S >= 4) {
        const char* base = c.pf + (size_t)(S - 4) * (64 * 2752);
        dma16(k == 0 ? c.vf0 : k == 1 ? c.vf1 : c.vf2, base, c.lds0 + wslot(S) + c.g * 12288 + c.tm * 3072 + k * 1024);
    } else {
        const char* base = c.pq + (size_t)S * (128 * 3072);
        dma16(c.vq, base + (k == 0 ? 1024 : k == 1 ? 2048 : 0), c.lds0 + wslot(S) + c.w * 1024 + k * 8192);
    }
}
template <int S> FI void issue_w(const Ctx& c) { issue_w1<S>(c, 0); issue_w1<S>(c, 1); issue_w1<S>(c, 2); }
FI void issue_d(const Ctx& c, int u) {
    const char* base;
    if (u < 21) base = c.pwd + (size_t)u * 65536;
    else if (u == 21) base = c.pwd + (size_t)21 * 65536 - (c.w >= 4 ? 32768 : 0);
    else base = c.pwo + (size_t)(u - 22) * 65536;
    dma16(c.vd, base, c.lds0 + dloc(u) + c.w * 1024);
}

FI float blo(unsigned d) { return __uint_as_float(d << 16); }
FI float bhi(unsigned d) { return __uint_as_float(d & 0xffff0000u); }
template <int CTRL> FI float dppadd(float x) { return x + __builtin_bit_cast(float, __builtin_amdgcn_update_dpp(0, __builtin_bit_cast(int, x), CTRL, 0xf, 0xf, true)); }
FI void swap32add(float& u, float v) { auto rr = __builtin_amdgcn_permlane32_swap(__float_as_uint(u), __float_as_uint(v), false, false); u = __uint_as_float(rr[0]) + __uint_as_float(rr[1]); }
FI void swap16add(float& u, float v) { auto rr = __builtin_amdgcn_permlane16_swap(__float_as_uint(u), __float_as_uint(v), false, false); u = __uint_as_float(rr[0]) + __uint_as_float(rr[1]); }

struct G1State {
    bf16x8 xf[32];
    bf16x8 wq[4];
    f32x16 a0, a1, b0, b1;
    f32x16 qa, qb;
    unsigned wbf[4], wbq[4];
    unsigned xb[4];
};
constexpr int fkind(int G, int f) { return G == 0 ? (f < 8 ? (f & 3) : (f & 1)) : (f < 4 ? (f & 1) : f < 6 ? (f & 1) : f < 8 ? 2 + (f & 1) : f < 10 ? (f & 1) : 2 + (f & 1)); }
constexpr int fpos(int G, int f) { return G == 0 ? (f < 8 ? (f >> 2) : 2 + ((f - 8) >> 1)) : (f < 4 ? (f >> 1) : f < 8 ? 2 : 3); }
constexpr int qkind(int G, int f) { return G == 0 ? ((f == 1 || f == 3 || f == 7 || f == 9) ? 1 : 0) : ((f == 3 || f == 5 || f == 9 || f == 11) ? 1 : 0); }
constexpr int qpos(int G, int f) {
    return G == 0 ? (f == 0 ? 0 : f == 1 ? 0 : f == 2 ? 1 : f == 3 ? 1 : f == 4 ? 2 : f == 5 ? 3 : f == 6 ? 4 : f == 7 ? 4 : f == 8 ? 5 : f == 9 ? 5 : f == 10 ? 6 : 7)
                  : (f == 0 ? 0 : f == 1 ? 1 : f == 2 ? 2 : f == 3 ? 2 : f == 4 ? 3 : f == 5 ? 3 : f == 6 ? 4 : f == 7 ? 5 : f == 8 ? 6 : f == 9 ? 6 : f == 10 ? 7 : 7);
}
template <int G, int S> FI bf16x8 rdfrag(const G1State& st, int f) {
    if constexpr (S >= 4) { const int k = fkind(G, f); return trfrag<768>((lcp)(uintptr_t)(st.wbf[fpos(G, f)] + (k & 1) * 12288 + (k >= 2 ? 64 : (G == 0 ? 0 : 128)))); }
    else { const int e = qpos(G, f); return trfrag<256>((lcp)(uintptr_t)(st.wbq[e & 3] + (e >> 2) * 4096 + (qkind(G, f) ? 16384 : G * 8192))); }
}
template <int C> FI void rdx(G1State& st) {
#pragma unroll
    for (int e = 0; e < 4; ++e) { st.xf[4 * C + e] = ldsf((lcp)(uintptr_t)st.xb[e]); if (C < 7) st.xb[e] = (unsigned)opaque((int)(st.xb[e] + (xslot(C + 1) - xslot(C)))); }
}
template <int G, int S, bool FRESH, bool XF, int XP, int NX, class F>
FI void stage(G1State& st, F&& hook) {
    if constexpr (XF) rdx<(XF ? 2 * S : 0)>(st);
    if constexpr (FRESH) {
#pragma unroll
        for (int f = 0; f < 4; ++f) st.wq[f] = rdfrag<G, S>(st, f);
    }
    if constexpr (XF) rdx<(XF ? 2 * S + 1 : 0)>(st);
    SB();
#pragma unroll
    for (int f = 0; f < 12; ++f) {
        const bf16x8 w = st.wq[f & 3];
        if constexpr (S >= 4) {
            const int k = fkind(G, f); const bf16x8 x = st.xf[4 * (S - 4) + fpos(G, f)];
            if (k == 0) st.a0 = mfma(w, x, st.a0); else if (k == 1) st.a1 = mfma(w, x, st.a1); else if (k == 2) st.b0 = mfma(w, x, st.b0); else st.b1 = mfma(w, x, st.b1);
        } else {
            const bf16x8 x = st.xf[8 * S + qpos(G, f)];
            if (qkind(G, f)) st.qb = mfma(w, x, st.qb); else if (G == 0) st.qa = mfma(w, x, st.qa); else st.qa = mfma(x, w, st.qa);
        }
        SB();
        if (f < 8) st.wq[f & 3] = rdfrag<G, S>(st, f + 4);
        else {
            if (f == 8) {
                if constexpr (S < 3) { _Pragma("unroll") for (int j = 0; j < 4; ++j) st.wbq[j] = (unsigned)opaque((int)(st.wbq[j] + (wslot(S + 1) - wslot(S)))); }
                if constexpr (S >= 4 && S < 11) { _Pragma("unroll") for (int j = 0; j < 4; ++j) st.wbf[j] = (unsigned)opaque((int)(st.wbf[j] + (wslot(S + 1) - wslot(S)))); }
            }
            if constexpr (NX >= 0) st.wq[f & 3] = rdfrag<G, (NX >= 0 ? NX : 0)>(st, f - 8);
        }
        if constexpr (XP >= 0) { if (f == 2) rdx<(XP >= 0 ? XP : 0)>(st); if (f == 6) rdx<(XP >= 0 ? XP + 1 : 0)>(st); }
        hook(f);
        SB();
    }
}

FI u64 mix64(u64 z) { z += 0x9E3779B97F4A7C15ull; z = (z ^ (z >> 30)) * 0xBF58476D1CE4E5B9ull; z = (z ^ (z >> 27)) * 0x94D049BB133111EBull; return z ^ (z >> 31); }

constexpr unsigned SPIN_CAP = 1u << 18;
FI void set_timeout(char* ws, int lane) { if (lane == 0) __hip_atomic_store((GAS unsigned*)(ws + WS_TMO), 1u, __ATOMIC_RELAXED, __HIP_MEMORY_SCOPE_AGENT); }
FI void poll_flags(const u64* flags, int n, u64 tag, int lane, char* ws) {
    const GAS u64* pp = (const GAS u64*)flags + (lane < n ? lane : 0);
    for (unsigned spins = 0;; ++spins) {
        const u64 v = __hip_atomic_load(pp, __ATOMIC_RELAXED, __HIP_MEMORY_SCOPE_AGENT);
        if (__all(v == tag)) return;
        if (spins > SPIN_CAP) { set_timeout(ws, lane); return; }
        __builtin_amdgcn_s_sleep(2);
    }
}
FI void store_flag(char* ws, size_t off, u64 tag) { __hip_atomic_store((GAS u64*)(ws + off), tag, __ATOMIC_RELAXED, __HIP_MEMORY_SCOPE_AGENT); }

__global__ __launch_bounds__(512, 2) void fused_kernel(Params P) {
    extern __shared__ __attribute__((aligned(16))) char smem[];
    Ctx c; c.p = &P;
    const int tid = threadIdx.x; c.lane = tid & 63;
    const int w = __builtin_amdgcn_readfirstlane(tid >> 6);
    c.w = w; c.tm = w & 3; c.g = w >> 2;
    c.lds = (lp)smem; c.lds0 = (unsigned)(uintptr_t)c.lds;
    const int xcd = blockIdx.x & 7, idx = blockIdx.x >> 3;
    c.rb = 8 * (xcd >> 2) + (idx >> 2); c.cg = 4 * (xcd & 3) + (idx & 3);
    c.tb = 8 * (xcd >> 1) + (idx >> 2); c.nb = 4 * (xcd & 1) + (idx & 3);
    const int fs = c.cg < 11 ? 3 * c.cg : c.cg < 15 ? 33 + 2 * (c.cg - 11) : 40;
    const bool v0 = c.cg != 15, v2 = c.cg < 11 || c.cg == 15;
    const int lane = c.lane, h = lane >> 5, r32 = lane & 31;
    char* ws = P.ws;
    c.px = (const char*)P.x + (size_t)(128 * c.rb + 16 * w) * 1024;
    c.pf = (const char*)(c.g ? P.wu : P.wg) + (size_t)(16 * c.tm) * 2752 + fs * 64;
    u64 tag = 0; unsigned tagLo = 0, tagHi = 0; float ln_a = 0.f, ln_m = 0.f;
    const int T = 4 * c.rb + c.tm;
    const __amdgpu_buffer_rsrc_t wsr = __builtin_amdgcn_make_buffer_rsrc(ws, 0, 32u << 20, 0x00020000);
    c.pq = (const char*)P.wqkv + ((size_t)(16 * w) * 1536 + 32 * c.cg) * 2;
    c.pwd = (const char*)P.wd + (size_t)(8 * w) * 1024 + c.nb * 128;
    c.pwo = (const char*)P.wo + (size_t)(8 * w) * 1024 + c.nb * 128;
    u32x4 g0a, g0b;
#define GATHER_OFFS(tile) const int gl_ = opaque(lane); const unsigned go0 = (unsigned)(WS_CS + (c.cg * 9 + (tile)) * 2048 + (gl_ >> 3) * 256 + (gl_ & 7) * 32)
#define GATHER_ISSUE(tile) do { GATHER_OFFS(tile); aload16_sc1(g0a, P.ws, go0); aload16_sc1(g0b, P.ws + 16, go0); } while (0)
#define GATHER_OK() (g0a[1] == tagLo && g0a[3] == tagLo && g0b[1] == tagLo && g0b[3] == tagLo)
#define GATHER_RED(tile) do { const int gl_ = opaque(lane); float v0_ = __uint_as_float(g0a[0]), v1_ = __uint_as_float(g0a[2]), v2_ = __uint_as_float(g0b[0]), v3_ = __uint_as_float(g0b[2]); \
        swap32add(v0_, v2_); swap32add(v1_, v3_); swap16add(v0_, v1_); v0_ = dppadd<0x128>(v0_); \
        if ((gl_ & 8) == 0) *(LAS float*)(c.lds + L_CS + (tile) * 128 + (4 * (gl_ & 7) + 2 * (gl_ >> 5) + ((gl_ >> 4) & 1)) * 4) = v0_; } while (0)
#define GATHER_CHECK(tile) do { asm volatile("" : "+v"(g0a), "+v"(g0b)); \
        if (!__all(GATHER_OK())) { GATHER_OFFS(tile); \
            for (unsigned spins = 0;; ++spins) { __builtin_amdgcn_s_sleep(2); \
                g0a = __builtin_amdgcn_raw_buffer_load_b128(wsr, go0, 0, 16); g0b = __builtin_amdgcn_raw_buffer_load_b128(wsr, go0 + 16, 0, 16); __builtin_amdgcn_s_waitcnt(0x0F70); \
                if (__all(GATHER_OK())) break; if (spins > SPIN_CAP) { set_timeout(ws, lane); break; } } } \
        } while (0)
#define F4(v, b) (f32x4){v[b], v[(b) + 1], v[(b) + 2], v[(b) + 3]}

    const int th = w & 1, kq = w >> 1;
    const int T2 = 2 * c.tb + th;
    bf16x8 qf; bool edone_ = false;
    auto gemm1 = [&](auto Gc) {
        constexpr int G = decltype(Gc)::value;
        const int lane = opaque(c.lane), h = lane >> 5, r32 = lane & 31, tid = w * 64 + lane;
        c.vq = (lane >> 2) * 3072 + (lane & 3) * 16;
        c.vx = (lane >> 3) * 1024 + (((lane & 7) ^ (lane >> 4)) * 16); c.vlin = lane * 16;
        G1State st;
        issue_x(c, 0); issue_x(c, 1); issue_w<0>(c);
        u64 ep; aload_flag(ep, ws + WS_EPOCH, 0u);
        u32x4 ax; aload16(ax, (const char*)P.x + (size_t)(128 * c.rb + 8 * c.cg + w) * 1024, c.vlin);
        u32x4 aw0, aw1, aw2, aw3;
        const int ptile = 2 * w + (c.rb & 1); const bool pvalid = ptile <= 8;
        aw0 = aw1 = aw2 = aw3 = (u32x4){0u, 0u, 0u, 0u};
        if (pvalid) {
            const char* tb; unsigned pitch, vo;
            if (ptile < 3) { tb = (const char*)P.wqkv + (ptile == 0 ? 512 : ptile == 1 ? 1024 : 0) * 2 + 64 * c.cg; pitch = 3072; vo = c.vq; }
            else { tb = (const char*)((ptile & 1) ? P.wg : P.wu) + 64 * (fs + ((ptile - 3) >> 1)); pitch = 2752; vo = (lane >> 2) * 2752 + (lane & 3) * 16; }
            tb += (size_t)(64 * (c.rb >> 1)) * pitch;
            aload16(aw0, tb, vo); aload16(aw1, tb + 16 * pitch, vo); aload16(aw2, tb + 32 * pitch, vo); aload16(aw3, tb + 48 * pitch, vo);
        }
        c.vf0 = (lane / 12) * 2752 + (lane % 12) * 16; c.vf1 = ((lane + 64) / 12) * 2752 + ((lane + 64) % 12) * 16; c.vf2 = ((lane + 128) / 12) * 2752 + ((lane + 128) % 12) * 16;
        issue_w<4>(c);
        *(LAS unsigned*)(c.lds + L_ZERO + tid * 4) = 0u;
        if (tid < 4) { *(LAS unsigned*)(c.lds + L_ONES + tid * 4) = 0x3f803f80u; *(LAS unsigned*)(c.lds + L_ONES + 1024 + tid * 4) = 0x3f803f80u; }
        { const unsigned lb = c.lds0 + (8 * h + ((lane & 15) >> 2)) * 64 + 32 * ((lane >> 4) & 1) + 8 * (lane & 3);
#pragma unroll
          for (int j = 0; j < 4; ++j) st.wbq[j] = lb + ((j ^ c.tm) * 1024) + wslot(0); }
        { const unsigned lb = c.lds0 + (8 * h + ((lane & 15) >> 2)) * 192 + 32 * ((lane >> 4) & 1) + 8 * (lane & 3);
#pragma unroll
          for (int j = 0; j < 4; ++j) st.wbf[j] = lb + ((j ^ c.tm) * 3072) + wslot(4); }
        { const int row = 32 * c.tm + r32;
#pragma unroll
          for (int j = 0; j < 4; ++j) st.xb[j] = c.lds0 + xslot(0) + row * 128 + (((2 * (j ^ c.tm) + h) ^ ((row >> 1) & 7)) * 16); }
        st.a0 = f32x16{}; st.a1 = f32x16{}; st.b0 = f32x16{}; st.b1 = f32x16{}; st.qa = f32x16{}; st.qb = f32x16{};

        SYNC(3);
        asm volatile("" : "+v"(ep), "+v"(ax), "+v"(aw0), "+v"(aw1), "+v"(aw2), "+v"(aw3));
        tag = mix64(((u64)(unsigned)__builtin_amdgcn_readfirstlane((int)(unsigned)(ep >> 32)) << 32) | (unsigned)__builtin_amdgcn_readfirstlane((int)(unsigned)ep));
        tagLo = (unsigned)tag; tagHi = (unsigned)(tag >> 32);

        float as_ = 0.f, aq_ = 0.f, av[8];
        auto aux0 = [&](int f) {
            if (f < 4) { const float a = blo(ax[f]), b = bhi(ax[f]); as_ += a + b; aq_ = __builtin_fmaf(a, a, aq_); aq_ = __builtin_fmaf(b, b, aq_); PIN(as_); PIN(aq_); }
            if (f == 4) { as_ = dppadd<0x121>(as_); aq_ = dppadd<0x121>(aq_); as_ = dppadd<0x122>(as_); aq_ = dppadd<0x122>(aq_); PIN(as_); PIN(aq_); }
            if (f == 5) { as_ = dppadd<0x124>(as_); aq_ = dppadd<0x124>(aq_); as_ = dppadd<0x128>(as_); aq_ = dppadd<0x128>(aq_); PIN(as_); PIN(aq_); }
            if (f == 6) { swap16add(as_, as_); swap16add(aq_, aq_); swap32add(as_, as_); swap32add(aq_, aq_); PIN(as_); PIN(aq_); }
            if (f == 8) { const float mean = as_ * (1.f / 512.f), var = aq_ * (1.f / 512.f) - mean * mean, rstd = __builtin_amdgcn_rsqf(fmaxf(var, 0.f) + 1e-5f);
                if (lane == 0) __builtin_amdgcn_raw_buffer_store_b128((u32x4){__float_as_uint(mean), tagLo, __float_as_uint(rstd), tagHi}, wsr, (unsigned)(WS_ST + (128 * c.rb + 8 * c.cg + w) * 16), 0, 16); }
        };
        auto aux1 = [&](int f) {
            if (f < 4) { const int i = f; av[2 * i] = (blo(aw0[i]) + blo(aw1[i])) + (blo(aw2[i]) + blo(aw3[i])); av[2 * i + 1] = (bhi(aw0[i]) + bhi(aw1[i])) + (bhi(aw2[i]) + bhi(aw3[i])); PIN(av[2 * i]); PIN(av[2 * i + 1]); }
            if (f == 8) { swap32add(av[0], av[4]); swap32add(av[1], av[5]); swap32add(av[2], av[6]); swap32add(av[3], av[7]); PIN(av[0]); PIN(av[1]); PIN(av[2]); PIN(av[3]); }
            if (f == 9) { swap16add(av[0], av[2]); swap16add(av[1], av[3]); av[0] = dppadd<0x124>(av[0]); av[1] = dppadd<0x124>(av[1]); av[0] = dppadd<0x128>(av[0]); av[1] = dppadd<0x128>(av[1]); PIN(av[0]); PIN(av[1]); }
            if (f == 10) { const int rho = lane >> 4, col = 8 * (lane & 3) + 4 * (rho >> 1) + 2 * (rho & 1);
                const unsigned off = pvalid ? (unsigned)(WS_CS + (((c.cg * 9 + ptile) * 8 + (c.rb >> 1)) * 32 + col) * 8) : (unsigned)(WS_SCR + (blockIdx.x * 8 + w) * 256 + col * 8);
                if ((lane & 12) == 0) __builtin_amdgcn_raw_buffer_store_b128((u32x4){__float_as_uint(av[0]), tagLo, __float_as_uint(av[1]), tagLo}, wsr, off, 0, 16); }
        };
        u32x4 gs;
        const int gt1 = w < 3 ? w : w < 6 ? w - 3 : w - 6, gt2 = w < 6 ? 3 + w : w - 3;
        stage<G, 0, true, true, -1, -1>(st, [&](int f_) { if (f_ < 2) issue_x1(c, 2, f_); else if (f_ < 4) issue_x1(c, 3, f_ - 2); else if (f_ < 7) issue_w1<1>(c, f_ - 4); aux0(f_); }); SYNCV(8);
        stage<G, 4, true, false, -1, -1>(st, [&](int f_) { if (f_ < 3) issue_w1<5>(c, f_); else if (f_ < 5) issue_x1(c, 4, f_ - 3); else if (f_ < 7) issue_x1(c, 5, f_ - 5); aux1(f_); }); SYNCV(9);
        unsigned pf2 = 0;
        auto warm2 = [&]() { const int l = opaque(lane) & 15; size_t off; const int tl = 4 * c.rb - 8 + w;
            if (l == 0) off = WS_KVF + ((size_t)(c.cg * 64 + T) * 2) * 8;
            else if (l == 1) off = WS_KVF + ((size_t)(c.cg * 64 + (tl < 0 ? 0 : tl)) * 2) * 8;
            else if (l < 4) off = WS_HF + (size_t)T * 256 + (l & 1) * 128;
            else if (l < 6) off = WS_HF + (size_t)T2 * 256 + (l & 1) * 128;
            else if (l < 8) off = WS_OF + (size_t)T * 256 + (l & 1) * 128;
            else if (l < 10) off = WS_OF + (size_t)T2 * 256 + (l & 1) * 128;
            else off = WS_SF + (size_t)blockIdx.x * 8;
            aprefetch(pf2, ws + off); };
        stage<G, 1, true, true, -1, -1>(st, [&](int f_) { if (f_ >= 1 && f_ < 4) issue_w1<2>(c, f_ - 1); if (f_ == 8) warm2(); }); SYNCV(5);
        stage<G, 5, true, false, 4, -1>(st, [&](int f_) { if (f_ < 3) issue_w1<6>(c, f_); else if (f_ < 5) issue_x1(c, 6, f_ - 3); else if (f_ < 7) issue_x1(c, 7, f_ - 5); }); SYNCV(8);
        stage<G, 2, true, false, -1, -1>(st, [&](int f_) { if (f_ >= 1 && f_ < 4) issue_w1<3>(c, f_ - 1);
            if (f_ == 8) { aload16_sc1(gs, P.ws, (unsigned)(WS_ST + (128 * c.rb + 32 * c.tm + (opaque(lane) & 31)) * 16)); GATHER_ISSUE(gt1); } }); SYNCV(6);
        PIN(pf2);
        stage<G, 6, true, false, 6, -1>(st, [&](int f_) { if (f_ >= 1 && f_ < 4) issue_w1<7>(c, f_ - 1); else if (f_ >= 4 && f_ < 7) issue_w1<8>(c, f_ - 4); }); SYNCV(9);
        stage<G, 3, true, false, -1, -1>(st, [&](int f_) { if (f_ >= 1 && f_ < 4) issue_w1<9>(c, f_ - 1);
            if (f_ == 10) { WAITV(9); PIN(gs);
                if (!__all(gs[1] == tagLo && gs[3] == tagHi)) { const unsigned gso = (unsigned)(WS_ST + (128 * c.rb + 32 * c.tm + (opaque(lane) & 31)) * 16);
                    for (unsigned spins = 0;; ++spins) { __builtin_amdgcn_s_sleep(2); gs = __builtin_amdgcn_raw_buffer_load_b128(wsr, gso, 0, 16); __builtin_amdgcn_s_waitcnt(0x0F70);
                        if (__all(gs[1] == tagLo && gs[3] == tagHi)) break; if (spins > SPIN_CAP) { set_timeout(ws, lane); break; } } }
                ln_m = __uint_as_float(gs[0]); ln_a = __uint_as_float(gs[2]); PIN(ln_m); PIN(ln_a);
                GATHER_CHECK(gt1); }
            if (f_ == 11) GATHER_RED(gt1); });
        { lp xp = c.lds + 13 * GR + w * 2048 + c.vlin; *(LAS f32x4*)(xp) = F4(st.qb, 8 - 8 * G); *(LAS f32x4*)(xp + 1024) = F4(st.qb, 12 - 8 * G);
          if constexpr (G == 1) { if (h == 0) { *(LAS float*)(c.lds + L_CSW + c.tm * 256 + r32 * 4) = ln_m; *(LAS float*)(c.lds + L_CSW + c.tm * 256 + 128 + r32 * 4) = ln_a; } } }
        SYNC(3);
        issue_w<10>(c);
        { const int lane = opaque(c.lane), h = lane >> 5, r32 = lane & 31;
          const unsigned o = (unsigned)(WS_KV + ((size_t)c.cg * 64 + T) * 4096) + lane * 16 + G * 2048;
          f32x16 y;
          if constexpr (G == 0) {
#pragma unroll
              for (int q = 0; q < 4; ++q) { const f32x4 cs = ldsf4((lcp)c.lds + L_CS + (8 * q + 4 * h) * 4);
#pragma unroll
                  for (int i = 0; i < 4; ++i) y[4 * q + i] = ln_a * __builtin_fmaf(-ln_m, cs[i], st.qa[4 * q + i]); }
          } else {
              const float cso = *(const LAS float*)(c.lds + L_CS + 128 + r32 * 4);
#pragma unroll
              for (int q = 0; q < 4; ++q) { const f32x4 mm = ldsf4((lcp)c.lds + L_CSW + c.tm * 256 + (8 * q + 4 * h) * 4), aa = ldsf4((lcp)c.lds + L_CSW + c.tm * 256 + 128 + (8 * q + 4 * h) * 4);
#pragma unroll
                  for (int i = 0; i < 4; ++i) y[4 * q + i] = aa[i] * __builtin_fmaf(-mm[i], cso, st.qa[4 * q + i]); }
          }
          const u32x4 k0 = pack8(y, 0), k1 = pack8(y, 8);
          __builtin_amdgcn_raw_buffer_store_b128(k0, wsr, o, 0, 16); __builtin_amdgcn_raw_buffer_store_b128(k1, wsr, o + 1024, 0, 16);
          if (w == 0 && lane == 0) store_flag(ws, WS_SF + (size_t)blockIdx.x * 8, tag);
          *(LAS u32x4*)(c.lds + L_KVR + c.tm * 4096 + G * 2048 + lane * 16) = k0; *(LAS u32x4*)(c.lds + L_KVR + c.tm * 4096 + G * 2048 + 1024 + lane * 16) = k1;
          SB();
          const f32x4 c0 = ldsf4((lcp)c.lds + L_CS + 2 * 128 + (16 * G + 4 * h) * 4), c1 = ldsf4((lcp)c.lds + L_CS + 2 * 128 + (16 * G + 8 + 4 * h) * 4);
          const lcp xp = (lcp)c.lds + 13 * GR + (w ^ 4) * 2048 + lane * 16;
          const f32x4 p0 = ldsf4(xp), p1 = ldsf4(xp + 1024);
          const float sc = ln_a * (0.25f * 1.4426950408889634f);
#pragma unroll
          for (int i = 0; i < 4; ++i) { y[i] = sc * __builtin_fmaf(-ln_m, c0[i], st.qb[8 * G + i] + p0[i]); y[4 + i] = sc * __builtin_fmaf(-ln_m, c1[i], st.qb[8 * G + 4 + i] + p1[i]); }
          qf = __builtin_bit_cast(bf16x8, pack8(y, 0)); PIN(qf); }
        stage<G, 7, true, false, -1, 8>(st, [&](int) {}); SYNC(5);
        { const int ln = opaque(lane); c.vd = (ln >> 3) * 1024 + (((ln & 7) ^ (((ln >> 4) & 1) << 2)) * 16); }
        stage<G, 8, false, false, -1, 9>(st, [&](int f_) { if (f_ >= 1 && f_ < 4) issue_w1<11>(c, f_ - 1); if (f_ == 4) issue_d(c, 0); }); SYNCV(6);
        stage<G, 9, false, false, -1, 10>(st, [&](int f_) { if (f_ >= 1 && f_ < 4) issue_d(c, f_);
            if (f_ == 5) { WAITV(7); if (lane == 0) store_flag(ws, WS_KVF + ((size_t)(c.cg * 64 + T) * 2 + G) * 8, tag); }
            if (f_ == 8) GATHER_ISSUE(gt2); }); SYNCV(7);
        u64 kf_;
        stage<G, 10, false, false, -1, 11>(st, [&](int f_) { if (f_ >= 1 && f_ < 4) issue_d(c, 3 + f_);
            if (f_ == 6) { WAITV(3); GATHER_CHECK(gt2); GATHER_RED(gt2); }
            if (f_ == 10) { const int tl = 4 * c.rb - 8 + w; aload_flag(kf_, P.ws, (unsigned)(WS_KVF + ((c.cg * 64 + (tl < 0 ? 0 : tl)) * 2 + (opaque(lane) & 1)) * 8)); } }); SYNCV(63);
        stage<G, 11, false, false, -1, -1>(st, [&](int f_) { if (f_ >= 1 && f_ < 3) issue_d(c, 6 + f_);
            if (f_ == 6) { WAITV(2); asm volatile("" : "+v"(kf_)); const int tl = 4 * c.rb - 8 + w;
                if (tl >= 0) {
                    if (!__all(kf_ == tag)) poll_flags((const u64*)(ws + WS_KVF) + (c.cg * 64 + tl) * 2, 2, tag, lane, ws);
                    const char* base = ws + WS_KV + ((size_t)c.cg * 64 + tl) * 4096; const unsigned dst = c.lds0 + L_KV + w * 4096;
                    dma16_sc1(c.vlin, base, dst); dma16_sc1(c.vlin, base + 1024, dst + 1024); dma16_sc1(c.vlin, base + 2048, dst + 2048); dma16_sc1(c.vlin, base + 3072, dst + 3072); } } });
        {
          lp xp = c.lds + 2 * GR + w * 4096 + c.vlin;
          SYNCV(63);
          *(LAS f32x4*)(xp) = F4(st.b0, 8 - 8 * G); *(LAS f32x4*)(xp + 1024) = F4(st.b0, 12 - 8 * G); *(LAS f32x4*)(xp + 2048) = F4(st.b1, 8 - 8 * G); *(LAS f32x4*)(xp + 3072) = F4(st.b1, 12 - 8 * G); }
        SYNC(63);
        {
          const int lane = opaque(c.lane), h = lane >> 5;
          const float a2 = ln_a * ln_a;
          { u32x4 y0, y1;
#pragma unroll
            for (int q = 0; q < 4; ++q) {
                const f32x4 cg_ = ldsf4((lcp)c.lds + L_CS + (3 + 4 * G) * 128 + (8 * q + 4 * h) * 4), cu_ = ldsf4((lcp)c.lds + L_CS + (4 + 4 * G) * 128 + (8 * q + 4 * h) * 4);
                float y[4];
#pragma unroll
                for (int i = 0; i < 4; ++i) { const float g_ = fmaxf(__builtin_fmaf(-ln_m, cg_[i], st.a0[4 * q + i]), 0.f), u_ = __builtin_fmaf(-ln_m, cu_[i], st.a1[4 * q + i]); y[i] = (g_ * a2) * u_; }
                const unsigned lo = pk2(y[0], y[1]), hi = pk2(y[2], y[3]);
                if (q < 2) { y0[2 * q] = lo; y0[2 * q + 1] = hi; } else { y1[2 * q - 4] = lo; y1[2 * q - 3] = hi; }
            }
            if (G == 0 ? v0 : v2) { const unsigned o_ = (unsigned)(WS_C + ((size_t)T * NKS + 2 * (fs + 2 * G)) * 1024) + lane * 16;
                __builtin_amdgcn_raw_buffer_store_b128(y0, wsr, o_, 0, 16); __builtin_amdgcn_raw_buffer_store_b128(y1, wsr, o_ + 1024, 0, 16); } }
          { const lcp xp = (lcp)c.lds + 2 * GR + (w ^ 4) * 4096 + lane * 16;
            u32x4 y0;
#pragma unroll
            for (int q = 0; q < 2; ++q) {
                const f32x4 pg = ldsf4(xp + q * 1024), pu = ldsf4(xp + 2048 + q * 1024);
                const f32x4 cg_ = ldsf4((lcp)c.lds + L_CS + 5 * 128 + (16 * G + 8 * q + 4 * h) * 4), cu_ = ldsf4((lcp)c.lds + L_CS + 6 * 128 + (16 * G + 8 * q + 4 * h) * 4);
                float y[4];
#pragma unroll
                for (int i = 0; i < 4; ++i) { const float g_ = fmaxf(__builtin_fmaf(-ln_m, cg_[i], st.b0[8 * G + 4 * q + i] + pg[i]), 0.f), u_ = __builtin_fmaf(-ln_m, cu_[i], st.b1[8 * G + 4 * q + i] + pu[i]); y[i] = (g_ * a2) * u_; }
                y0[2 * q] = pk2(y[0], y[1]); y0[2 * q + 1] = pk2(y[2], y[3]);
            }
            __builtin_amdgcn_raw_buffer_store_b128(y0, wsr, (unsigned)(WS_C + ((size_t)T * NKS + 2 * (fs + 1) + G) * 1024) + lane * 16, 0, 16); } }
        if (G == 0 ? v0 : v2) { SYNC(3); } else { SYNC(1); }
        if constexpr (G == 1) __builtin_amdgcn_s_setprio(1);
    };
    if (c.g == 0) gemm1(std::integral_constant<int, 0>{}); else gemm1(std::integral_constant<int, 1>{});
    const int lane2 = opaque(lane);
    const unsigned cbase = (unsigned)(WS_C + ((size_t)T2 * NKS + kq) * 1024) + lane2 * 16;
    bf16x8 hf[22];
    u64 hfl; bool hrdy_ = false;
#define LDH(i) do { const bool ok_ = ((i) < 21) || (kq < 2); hf[i] = __builtin_bit_cast(bf16x8, __builtin_amdgcn_raw_buffer_load_b128(wsr, ok_ ? cbase + (i) * 4096 : cbase, 0, 16)); } while (0)
    {
        const int lane = lane2, h = lane >> 5, r32 = lane & 31;
        const int i0 = T >= 8 ? 0 : 8 - T;
        const unsigned kb = c.lds0 + L_KV + c.tm * 4096 + c.g * 1024 + lane * 16;
        const unsigned lanebit = r32 < 16 ? 1u : 0u;
        const unsigned vb = c.lds0 + (r32 < 16 ? (unsigned)(((r32 & 15) + 16 * c.g + 32 * h) * 16) : (unsigned)L_ONES);
        auto koff = [&](int i) -> unsigned { return (unsigned)((i >= i0 ? i : 7) * 4096); };
        auto rdk = [&](int i) -> bf16x8 { return ldsf((lcp)(uintptr_t)(kb + koff(i))); };
        auto vaddr = [&](int i) -> unsigned { const bool valid = i >= i0; const unsigned d = valid ? 0u : (unsigned)(L_ZERO - L_ONES), A = valid ? (unsigned)(L_KV + 2048 + (c.tm + i) * 4096) : (unsigned)L_ZERO;
                                              return lanebit * (A - d) + (vb + d); };
        auto dmask = [&](int r) -> bool { u64 mk = 0;
#pragma unroll
            for (int l = 0; l < 64; ++l) { const int kl = (r & 3) + 8 * (r >> 2) + 4 * (l >> 5); if (kl <= (l & 31)) mk |= 1ull << l; }
            return __builtin_amdgcn_inverse_ballot_w64(mk); };
        const f32x16 zero = f32x16{};
        float m0 = -1e30f, m1 = -1e30f;
#define MAXT(S) do { _Pragma("unroll") for (int r = 0; r < 16; r += 4) { m0 = fmaxf(fmaxf(m0, S[r]), S[r + 1]); m1 = fmaxf(fmaxf(m1, S[r + 2]), S[r + 3]); } PIN(m0); PIN(m1); } while (0)
#define EXPT(S) do { _Pragma("unroll") for (int r = 0; r < 16; ++r) S[r] = __builtin_amdgcn_exp2f(S[r]); } while (0)
#define H_PUBLISH(N) do { WAITV(N); if (lane == 0) store_flag(ws, WS_HF + ((size_t)T * 32 + 2 * c.cg + c.g) * 8, tag); } while (0)
#define H_FLAGS() aload_flag(hfl, P.ws, (unsigned)(WS_HF + (T2 * 32 + (lane & 31)) * 8))
        f32x16 o = f32x16{};
        float m; bool hrdy = false;
        if (T == 0) {
            f32x16 s = mfma(ldsf((lcp)(uintptr_t)(kb + 8 * 4096)), qf, zero);
#pragma unroll
            for (int r = 0; r < 16; ++r) s[r] = dmask(r) ? s[r] : -1e30f;
            MAXT(s);
            m = fmaxf(m0, m1);
            { auto rr = __builtin_amdgcn_permlane32_swap(__float_as_uint(m), __float_as_uint(m), false, false); m = fmaxf(__uint_as_float(rr[0]), __uint_as_float(rr[1])); }
#pragma unroll
            for (int r = 0; r < 16; ++r) s[r] = __builtin_amdgcn_exp2f(s[r] - m);
            const unsigned va = vaddr(8);
            o = mfma(ldsf((lcp)(uintptr_t)va), __builtin_bit_cast(bf16x8, pack8(s, 0)), o);
            o = mfma(ldsf((lcp)(uintptr_t)(va + 1024)), __builtin_bit_cast(bf16x8, pack8(s, 8)), o);
            issue_d(c, 9); issue_d(c, 10); issue_d(c, 11); issue_d(c, 12);
            H_PUBLISH(4);
        } else {
            bf16x8 k0 = rdk(0), k8 = rdk(8), k1 = rdk(1), k2 = rdk(2), k3 = rdk(3);
            f32x16 sA, sB, sC;
            sA = mfma_pin0(k0, qf); sB = mfma_pin0(k8, qf); SB();
            k0 = rdk(4); k8 = rdk(5); sC = mfma_pin0(k1, qf); k1 = rdk(6); SB();
            asm volatile("s_nop 7\n\ts_nop 3"); SB();
#pragma unroll
            for (int r = 0; r < 16; ++r) sA[r] = dmask(r) ? sB[r] : sA[r];
            PIN(sA); SB();
            sB = mfma_pin0(k2, qf); k2 = rdk(7); SB();
            MAXT(sA); SB();
            H_PUBLISH(0);
            unsigned pfw = 0;
            if (w < 3) { const int id = w * 64 + lane; int row = 832 + 8 * id + (idx >> 2); if (row > 1887) row = 1887;
                aprefetch(pfw, (row < 1376 ? (const char*)P.wd + (size_t)row * 1024 : (const char*)P.wo + (size_t)(row - 1376) * 1024) + c.nb * 128); }
            sA = mfma_pin0(k3, qf); issue_d(c, 9); SB(); MAXT(sC); SB();
            sC = mfma_pin0(k0, qf); issue_d(c, 10); SB(); MAXT(sB); SB();
            sB = mfma_pin0(k8, qf); issue_d(c, 11); SB(); MAXT(sA); SB();
            sA = mfma_pin0(k1, qf); issue_d(c, 12); SB(); MAXT(sC); SB();
            sC = mfma_pin0(k2, qf); k0 = rdk(0); k8 = rdk(8); k1 = rdk(1); SB(); MAXT(sB); SB();
            MAXT(sA); SB(); MAXT(sC);
            m = fmaxf(m0, m1);
            { auto rr = __builtin_amdgcn_permlane32_swap(__float_as_uint(m), __float_as_uint(m), false, false); m = fmaxf(__uint_as_float(rr[0]), __uint_as_float(rr[1])); }
            f32x16 cm;
#pragma unroll
            for (int r = 0; r < 16; ++r) cm[r] = -m;
            PIN(cm);
            asm volatile("s_nop 1"); sA = mfma_pin(k0, qf, cm); sB = mfma_pin(k8, qf, cm); sC = mfma_pin(k1, qf, cm); SB();
            bf16x8 va0, va1, vb0, vb1;
            { const unsigned a0 = vaddr(0), a8 = vaddr(8); va0 = ldsf((lcp)(uintptr_t)a0); va1 = ldsf((lcp)(uintptr_t)(a0 + 1024)); vb0 = ldsf((lcp)(uintptr_t)a8); vb1 = ldsf((lcp)(uintptr_t)(a8 + 1024)); }
            k0 = rdk(2);
            unsigned mp[8];
#pragma unroll
            for (int j = 0; j < 8; ++j) mp[j] = (dmask(2 * j) ? 0xffffu : 0u) | (dmask(2 * j + 1) ? 0xffff0000u : 0u);
            SB(); asm volatile("s_nop 7"); SB();
#pragma unroll
            for (int r = 0; r < 16; ++r) sA[r] = dmask(r) ? sB[r] : sA[r];
            EXPT(sA);
            { const u32x4 p0 = pack8(sA, 0), p1 = pack8(sA, 8); u32x4 d0, d1, o0, o1;
#pragma unroll
              for (int j = 0; j < 4; ++j) { d0[j] = p0[j] & mp[j]; d1[j] = p1[j] & mp[4 + j]; o0[j] = p0[j] & ~mp[j]; o1[j] = p1[j] & ~mp[4 + j]; }
              o = mfma(va0, __builtin_bit_cast(bf16x8, o0), o); o = mfma(va1, __builtin_bit_cast(bf16x8, o1), o);
              o = mfma(vb0, __builtin_bit_cast(bf16x8, d0), o); o = mfma(vb1, __builtin_bit_cast(bf16x8, d1), o); }
            { const unsigned a1 = vaddr(1), a2 = vaddr(2); va0 = ldsf((lcp)(uintptr_t)a1); va1 = ldsf((lcp)(uintptr_t)(a1 + 1024)); vb0 = ldsf((lcp)(uintptr_t)a2); vb1 = ldsf((lcp)(uintptr_t)(a2 + 1024)); }
            SB();
#define EXP8(S, b) do { _Pragma("unroll") for (int r = (b); r < (b) + 8; ++r) S[r] = __builtin_amdgcn_exp2f(S[r]); } while (0)
#define PK4(S, b) (u32x4){pk2(S[b], S[(b) + 1]), pk2(S[(b) + 2], S[(b) + 3]), pk2(S[(b) + 4], S[(b) + 5]), pk2(S[(b) + 6], S[(b) + 7])}
#define TILE2(i, S, V0, V1, SN) do { \
                if ((i) + 1 <= 7) { SN = mfma_pin(k0, qf, cm); SB(); if ((i) + 2 <= 7) k0 = rdk((i) + 2); } \
                EXP8(S, 0); { u32x4 p_ = PK4(S, 0); PIN(p_); SB(); o = mfma(V0, __builtin_bit_cast(bf16x8, p_), o); SB(); } \
                EXP8(S, 8); { u32x4 p_ = PK4(S, 8); PIN(p_); SB(); o = mfma(V1, __builtin_bit_cast(bf16x8, p_), o); SB(); } \
                if ((i) + 2 <= 7) { const unsigned a_ = vaddr((i) + 2); V0 = ldsf((lcp)(uintptr_t)a_); V1 = ldsf((lcp)(uintptr_t)(a_ + 1024)); } SB(); } while (0)
            TILE2(1, sC, va0, va1, sB); TILE2(2, sB, vb0, vb1, sC);
            H_FLAGS();
            TILE2(3, sC, va0, va1, sB); TILE2(4, sB, vb0, vb1, sC);
            WAITV(0); asm volatile("" : "+v"(hfl), "+v"(pfw));
            hrdy = __all(hfl == tag);
            if (hrdy) { LDH(0); LDH(1); LDH(2); LDH(3); LDH(4); LDH(5); LDH(6); }
            TILE2(5, sC, va0, va1, sB);
            if (hrdy) { LDH(7); LDH(8); LDH(9); LDH(10); LDH(11); LDH(12); LDH(13); }
            TILE2(6, sB, vb0, vb1, sC);
            if (hrdy) { LDH(14); LDH(15); LDH(16); LDH(17); LDH(18); LDH(19); LDH(20); LDH(21); }
            TILE2(7, sC, va0, va1, sB);
        }
        const float inv = __builtin_amdgcn_rcpf(o[8]);
        f32x16 y;
#pragma unroll
        for (int r = 0; r < 8; ++r) y[r] = o[r] * inv;
        const unsigned oo = (unsigned)(WS_C + ((size_t)T * NKS + 86 + 2 * c.cg + c.g) * 1024) + lane * 16;
        const u32x4 ov = pack8(y, 0);
        __builtin_amdgcn_raw_buffer_store_b128(ov, wsr, oo, 0, 16);
        if (!hrdy) {
            poll_flags((const u64*)(ws + WS_HF) + T2 * 32, 32, tag, lane, ws);
#pragma unroll
            for (int i = 0; i < 22; ++i) LDH(i);
        }
        hrdy_ = hrdy;
        __builtin_amdgcn_s_waitcnt(0x0F71);
        __builtin_amdgcn_s_setprio(0);
    }
    {
        const int lane = opaque(lane2), h = lane >> 5, r32 = lane & 31, q4 = (lane & 15) >> 2, p4 = lane & 3, b16 = (lane >> 4) & 1;
        f32x16 acc0 = f32x16{}, acc1 = f32x16{};
        const unsigned wl = c.lds0 + kq * 2048 + (4 * h + q4) * 128 + 32 * b16 + 8 * p4, swz = ((q4 >> 1) & 1) << 6;
        const unsigned wl0 = wl + (0u ^ swz), wl1 = wl + (64u ^ swz);
        bf16x8 wa[3], wb_[3];
#define RDW(u) do { wa[(u) % 3] = trfrag<1024>((lcp)(uintptr_t)(wl0 + dloc(u))); wb_[(u) % 3] = trfrag<1024>((lcp)(uintptr_t)(wl1 + dloc(u))); } while (0)
#define G2STEP(u, frag, nxt) do { acc0 = mfma(wa[(u) % 3], frag, acc0); acc1 = mfma(wb_[(u) % 3], frag, acc1); SB(); if ((nxt) >= 0) RDW((nxt) < 0 ? 0 : (nxt)); SB(); } while (0)
        SYNC(63);
        const bool ew = blockIdx.x == 0 && w == 7; u32x4 sf0 = {0u, 0u, 0u, 0u}, sf1 = sf0; bool edone = false;
        if (ew) { aload16_sc1(sf0, P.ws + WS_SF, (unsigned)(lane * 32)); aload16_sc1(sf1, P.ws + WS_SF + 16, (unsigned)(lane * 32)); }
        RDW(0); RDW(1); RDW(2);
        G2STEP(0, hf[0], 3);
        issue_d(c, 13); G2STEP(1, hf[1], 4); issue_d(c, 14); G2STEP(2, hf[2], 5); issue_d(c, 15); G2STEP(3, hf[3], 6); issue_d(c, 16); G2STEP(4, hf[4], 7); issue_d(c, 17); G2STEP(5, hf[5], 8); issue_d(c, 18);
        if (hrdy_) { WAITV(6); }
        if (lane == 0) store_flag(ws, WS_OF + ((size_t)T * 32 + 2 * c.cg + c.g) * 8, tag);
        SYNCV(63);
        G2STEP(6, hf[6], 9); issue_d(c, 19); G2STEP(7, hf[7], 10); issue_d(c, 20); G2STEP(8, hf[8], 11); issue_d(c, 21); G2STEP(9, hf[9], 12); issue_d(c, 22);
        G2STEP(10, hf[10], -1); issue_d(c, 23); G2STEP(11, hf[11], -1); issue_d(c, 24); G2STEP(12, hf[12], -1);
        SYNCV(7);
        if (ew) { asm volatile("" : "+v"(sf0), "+v"(sf1));
            edone = __all(sf0[0] == tagLo && sf0[1] == tagHi && sf0[2] == tagLo && sf0[3] == tagHi && sf1[0] == tagLo && sf1[1] == tagHi && sf1[2] == tagLo && sf1[3] == tagHi);
            if (edone && lane == 0) store_flag(ws, WS_EPOCH, tag); }
        edone_ = edone;
        RDW(13); RDW(14); RDW(15);
        u32x4 xres;
        u64 ofl;
        issue_d(c, 25); G2STEP(13, hf[13], 16); issue_d(c, 26); G2STEP(14, hf[14], 17); issue_d(c, 27); G2STEP(15, hf[15], 18); issue_d(c, 28);
        aload_flag(ofl, P.ws, (unsigned)(WS_OF + (T2 * 32 + (lane & 31)) * 8));
        G2STEP(16, hf[16], -1); issue_d(c, 29);
        xres = *(const u32x4*)(P.x + (size_t)(64 * c.tb + 8 * w + (lane >> 3)) * 512 + 64 * c.nb + (lane & 7) * 8);
        G2STEP(17, hf[17], -1); G2STEP(18, hf[18], -1);
        WAITV(2); asm volatile("" : "+v"(ofl));
        if (!__all(ofl == tag)) poll_flags((const u64*)(ws + WS_OF) + T2 * 32, 32, tag, lane, ws);
        bf16x8 of[8];
#pragma unroll
        for (int i = 0; i < 8; ++i) of[i] = __builtin_bit_cast(bf16x8, __builtin_amdgcn_raw_buffer_load_b128(wsr, cbase + (86 + 4 * i) * 1024, 0, 16));
        SYNCV(63);
        RDW(19); RDW(20); RDW(21);
        G2STEP(19, hf[19], 22); G2STEP(20, hf[20], 23);
        if (kq < 2) { acc0 = mfma(wa[21 % 3], hf[21], acc0); acc1 = mfma(wb_[21 % 3], hf[21], acc1); }
        SB(); RDW(24); SB();
        G2STEP(22, of[0], -1); G2STEP(23, of[1], -1); G2STEP(24, of[2], -1);
        SYNCV(63);
        RDW(25); RDW(26); RDW(27);
        G2STEP(25, of[3], 28); G2STEP(26, of[4], 29); G2STEP(27, of[5], -1); G2STEP(28, of[6], -1); G2STEP(29, of[7], -1);
        { lp rbp = c.lds + (kq < 2 ? 4 * GR : 13 * GR - 32768) + (kq * 2 + th) * 8192;
#pragma unroll
          for (int q = 0; q < 4; ++q) { *(LAS f32x4*)(rbp + q * 1024 + ((lane + 2 * q) & 63) * 16) = F4(acc0, 4 * q); *(LAS f32x4*)(rbp + 4096 + q * 1024 + ((lane + 2 * (4 + q)) & 63) * 16) = F4(acc1, 4 * q); } }
        SYNC(63);
        {
          const int t = 8 * w + (lane >> 3), cc = lane & 7;
          const unsigned a0 = (unsigned)((t >> 5) * 8192 + cc * 1024), s0i = (unsigned)(((t & 31) + 2 * cc) & 63) * 16, s1i = (unsigned)(((t & 31) + 32 + 2 * cc) & 63) * 16;
          f32x4 s0 = {0.f, 0.f, 0.f, 0.f}, s1 = s0;
#pragma unroll
          for (int k = 0; k < 4; ++k) { const lcp bp = (lcp)c.lds + (k < 2 ? 4 * GR : 13 * GR - 32768) + k * 16384 + a0; s0 += ldsf4(bp + s0i); s1 += ldsf4(bp + s1i); }
          u32x4 ov;
          ov[0] = pk2(s0[0] + blo(xres[0]), s0[1] + bhi(xres[0])); ov[1] = pk2(s0[2] + blo(xres[1]), s0[3] + bhi(xres[1]));
          ov[2] = pk2(s1[0] + blo(xres[2]), s1[1] + bhi(xres[2])); ov[3] = pk2(s1[2] + blo(xres[3]), s1[3] + bhi(xres[3]));
          *(u32x4*)(P.out + (size_t)(64 * c.tb + t) * 512 + 64 * c.nb + cc * 8) = ov; }
    }
    if (blockIdx.x == 0 && w == 7 && !edone_) {
        const GAS u64* pp = (const GAS u64*)(ws + WS_SF) + lane * 4;
        for (unsigned spins = 0;; ++spins) { bool ok = true;
#pragma unroll
            for (int j = 0; j < 4; ++j) ok &= __hip_atomic_load(pp + j, __ATOMIC_RELAXED, __HIP_MEMORY_SCOPE_AGENT) == tag;
            if (__all(ok)) break; if (spins > SPIN_CAP) { set_timeout(ws, lane); break; } __builtin_amdgcn_s_sleep(2); }
        if (lane == 0) store_flag(ws, WS_EPOCH, tag);
    }
}

extern "C" void kernel_launch(void* const* d_in, const int* in_sizes, int n_in, void* d_out, int out_size, void* d_ws, size_t ws_size, hipStream_t stream) {
    Params p;
    p.x = (const u16*)d_in[0]; p.wqkv = (const u16*)d_in[1]; p.wo = (const u16*)d_in[2];
    p.wg = (const u16*)d_in[4]; p.wu = (const u16*)d_in[5]; p.wd = (const u16*)d_in[6];
    p.out = (u16*)d_out; p.ws = (char*)d_ws;
    (void)hipFuncSetAttribute((const void*)fused_kernel, hipFuncAttributeMaxDynamicSharedMemorySize, LDS_BYTES);
    fused_kernel<<<256, 512, LDS_BYTES, stream>>>(p);
}
```

```cpp
#include <hip/hip_runtime.h>
#include <stdint.h>
#include <type_traits>

#define FI __device__ __forceinline__
#define LAS __attribute__((address_space(3)))
#define GAS __attribute__((address_space(1)))
typedef unsigned short u16;
typedef unsigned long long u64;
typedef short bf16x8 __attribute__((ext_vector_type(8)));
typedef short s16x4 __attribute__((ext_vector_type(4)));
typedef float f32x16 __attribute__((ext_vector_type(16)));
typedef float f32x4 __attribute__((ext_vector_type(4)));
typedef float f32x2 __attribute__((ext_vector_type(2)));
typedef unsigned u32x4 __attribute__((ext_vector_type(4)));
typedef __bf16 bf16x2_t __attribute__((ext_vector_type(2)));
typedef LAS const char* lcp;
typedef LAS char* lp;

constexpr int GR = 8192;
constexpr int L_KV = 13 * GR;
constexpr int L_KVR = 17 * GR;
constexpr int L_MISC = 19 * GR;
constexpr int L_CS = L_MISC;
constexpr int L_CSW = L_MISC + 2048;
constexpr int L_ZERO = L_MISC + 4096;
constexpr int L_ONES = L_MISC + 6144;
constexpr int LDS_BYTES = 163840;

constexpr size_t WS_EPOCH = 0, WS_TMO = 64;
constexpr size_t WS_SF = 4096;
constexpr size_t WS_KVF = 8192;
constexpr size_t WS_HF = 24576;
constexpr size_t WS_OF = 40960;
constexpr size_t WS_ST = 65536;
constexpr size_t WS_CS = 131072;
constexpr size_t WS_SCR = 768u << 10;
constexpr size_t WS_KV = 2u << 20;
constexpr size_t WS_C = 8u << 20;
constexpr int NKS = 118;

FI f32x16 mfma(bf16x8 a, bf16x8 b, f32x16 c) { return __builtin_amdgcn_mfma_f32_32x32x16_bf16(a, b, c, 0, 0, 0); }
FI f32x16 mfma_pin(bf16x8 a, bf16x8 b, const f32x16& c) { f32x16 d; asm volatile("v_mfma_f32_32x32x16_bf16 %0, %1, %2, %3" : "=&v"(d) : "v"(a), "v"(b), "v"(c)); return d; }
FI f32x16 mfma_pin0(bf16x8 a, bf16x8 b) { f32x16 d; asm volatile("v_mfma_f32_32x32x16_bf16 %0, %1, %2, 0" : "=&v"(d) : "v"(a), "v"(b)); return d; }
FI unsigned pk2(float lo, float hi) { f32x2 v = {lo, hi}; bf16x2_t r = __builtin_convertvector(v, bf16x2_t); return __builtin_bit_cast(unsigned, r); }
FI u32x4 pack8(const f32x16& y, int b) { u32x4 r; r[0] = pk2(y[b], y[b + 1]); r[1] = pk2(y[b + 2], y[b + 3]); r[2] = pk2(y[b + 4], y[b + 5]); r[3] = pk2(y[b + 6], y[b + 7]); return r; }
typedef short v4i16_t __attribute__((ext_vector_type(4)));
FI s16x4 trd(lcp p) { return __builtin_bit_cast(s16x4, __builtin_amdgcn_ds_read_tr16_b64_v4i16((LAS v4i16_t*)p)); }
template <int D2> FI bf16x8 trfrag(lcp p) { s16x4 lo = trd(p), hi = trd(p + D2); return __builtin_shufflevector(lo, hi, 0, 1, 2, 3, 4, 5, 6, 7); }
FI bf16x8 ldsf(lcp p) { return *(const LAS bf16x8*)p; }
FI f32x4 ldsf4(lcp p) { return *(const LAS f32x4*)p; }

FI void dma16(unsigned voff, const void* sbase, unsigned dst) {
    asm volatile("s_mov_b32 m0, %2\n\ts_nop 0\n\tglobal_load_lds_dwordx4 %0, %1" :: "v"(voff), "s"(sbase), "s"(__builtin_amdgcn_readfirstlane(dst)) : "memory", "m0");
}
FI void dma16_sc1(unsigned voff, const void* sbase, unsigned dst) {
    asm volatile("s_mov_b32 m0, %2\n\ts_nop 0\n\tglobal_load_lds_dwordx4 %0, %1 sc1" :: "v"(voff), "s"(sbase), "s"(__builtin_amdgcn_readfirstlane(dst)) : "memory", "m0");
}
FI void aload16(u32x4& v, const void* base, unsigned voff) { asm volatile("global_load_dwordx4 %0, %1, %2" : "=v"(v) : "v"(voff), "s"(base) : "memory"); }
FI void aload16_sc1(u32x4& v, const void* base, unsigned voff) { asm volatile("global_load_dwordx4 %0, %1, %2 sc1" : "=v"(v) : "v"(voff), "s"(base) : "memory"); }
FI void aload_flag(u64& v, const char* base, unsigned voff) { asm volatile("global_load_dwordx2 %0, %1, %2 sc1" : "=v"(v) : "v"(voff), "s"(base) : "memory"); }

FI void aprefetch(unsigned& d, const char* p) { asm volatile("global_load_dword %0, %1, off" : "+v"(d) : "v"(p) : "memory"); }
#define SB() __builtin_amdgcn_sched_barrier(0)
#define PIN(x) asm volatile("" : "+v"(x))
FI int opaque(int v) { asm volatile("" : "+v"(v)); return v; }
#define SYNC(N) do { SB(); asm volatile("s_waitcnt vmcnt(" #N ") lgkmcnt(0)\n\ts_barrier" ::: "memory"); SB(); } while (0)
#define SYNCV(N) do { SB(); asm volatile("s_waitcnt vmcnt(" #N ")\n\ts_barrier" ::: "memory"); SB(); } while (0)
#define WAITV(N) do { SB(); asm volatile("s_waitcnt vmcnt(" #N ")" ::: "memory"); SB(); } while (0)

struct Params {
    const u16* x; const u16* wqkv; const u16* wo; const u16* wg; const u16* wu; const u16* wd; u16* out; char* ws;
};

constexpr int wslot(int s) { return (s == 0 ? 4 : s == 1 ? 14 : s == 2 ? 7 : s == 3 ? 0 : s == 4 ? 7 : s == 5 ? 0 : s == 6 ? 10 : s == 7 ? 3 : s == 8 ? 6 : s == 9 ? 9 : s == 10 ? 0 : 3) * GR; }
constexpr int xslot(int c) { return (c == 0 ? 0 : c == 1 ? 2 : c == 2 ? 10 : c == 3 ? 12 : c == 4 ? 3 : c == 5 ? 5 : c == 6 ? 13 : 15) * GR; }
FI constexpr int dloc0(int u) { return (u == 0 ? 12 : u < 4 ? 6 + (u - 1) : u < 7 ? 9 + (u - 4) : u < 9 ? u - 7 : 2 + (u - 9)) * GR; }
FI constexpr int dloc(int u) { return u < 13 ? dloc0(u) : u < 19 ? L_KV + (u - 13) * GR : dloc0(u - 19); }

struct Ctx {
    const Params* p;
    unsigned lds0;
    lp lds;
    int lane, w, tm, g, rb, cg, tb, nb;
    unsigned vq, vf0, vf1, vf2, vx, vlin, vd;
    const char *px, *pq, *pf, *pwd, *pwo;
};

FI void issue_x1(const Ctx& c, int chunk, int k) {
    dma16(k ? c.vx ^ 64u : c.vx, c.px + chunk * 128 + k * 8 * 1024, c.lds0 + xslot(chunk) + 16 * c.w * 128 + k * 8 * 128);
}
FI void issue_x(const Ctx& c, int chunk) { issue_x1(c, chunk, 0); issue_x1(c, chunk, 1); }
template <int S> FI void issue_w1(const Ctx& c, int k) {
    if constexpr (S >= 4) {
        const char* base = c.pf + (size_t)(S - 4) * (64 * 2752);
        dma16(k == 0 ? c.vf0 : k == 1 ? c.vf1 : c.vf2, base, c.lds0 + wslot(S) + c.g * 12288 + c.tm * 3072 + k * 1024);
    } else {
        const char* base = c.pq + (size_t)S * (128 * 3072);
        dma16(c.vq, base + (k == 0 ? 1024 : k == 1 ? 2048 : 0), c.lds0 + wslot(S) + c.w * 1024 + k * 8192);
    }
}
template <int S> FI void issue_w(const Ctx& c) { issue_w1<S>(c, 0); issue_w1<S>(c, 1); issue_w1<S>(c, 2); }
FI void issue_d(const Ctx& c, int u) {
    const char* base;
    if (u < 21) base = c.pwd + (size_t)u * 65536;
    else if (u == 21) base = c.pwd + (size_t)21 * 65536 - (c.w >= 4 ? 32768 : 0);
    else base = c.pwo + (size_t)(u - 22) * 65536;
    dma16(c.vd, base, c.lds0 + dloc(u) + c.w * 1024);
}

FI float blo(unsigned d) { return __uint_as_float(d << 16); }
FI float bhi(unsigned d) { return __uint_as_float(d & 0xffff0000u); }
template <int CTRL> FI float dppadd(float x) { return x + __builtin_bit_cast(float, __builtin_amdgcn_update_dpp(0, __builtin_bit_cast(int, x), CTRL, 0xf, 0xf, true)); }
FI void swap32add(float& u, float v) { auto rr = __builtin_amdgcn_permlane32_swap(__float_as_uint(u), __float_as_uint(v), false, false); u = __uint_as_float(rr[0]) + __uint_as_float(rr[1]); }
FI void swap16add(float& u, float v) { auto rr = __builtin_amdgcn_permlane16_swap(__float_as_uint(u), __float_as_uint(v), false, false); u = __uint_as_float(rr[0]) + __uint_as_float(rr[1]); }

struct G1State {
    bf16x8 xf[32];
    bf16x8 wq[4];
    f32x16 a0, a1, b0, b1;
    f32x16 qa, qb;
    unsigned wbf[4], wbq[4];
    unsigned xb[4];
};
constexpr int fkind(int G, int f) { return G == 0 ? (f < 8 ? (f & 3) : (f & 1)) : (f < 4 ? (f & 1) : f < 6 ? (f & 1) : f < 8 ? 2 + (f & 1) : f < 10 ? (f & 1) : 2 + (f & 1)); }
constexpr int fpos(int G, int f) { return G == 0 ? (f < 8 ? (f >> 2) : 2 + ((f - 8) >> 1)) : (f < 4 ? (f >> 1) : f < 8 ? 2 : 3); }
constexpr int qkind(int G, int f) { return G == 0 ? ((f == 1 || f == 3 || f == 7 || f == 9) ? 1 : 0) : ((f == 3 || f == 5 || f == 9 || f == 11) ? 1 : 0); }
constexpr int qpos(int G, int f) {
    return G == 0 ? (f == 0 ? 0 : f == 1 ? 0 : f == 2 ? 1 : f == 3 ? 1 : f == 4 ? 2 : f == 5 ? 3 : f == 6 ? 4 : f == 7 ? 4 : f == 8 ? 5 : f == 9 ? 5 : f == 10 ? 6 : 7)
                  : (f == 0 ? 0 : f == 1 ? 1 : f == 2 ? 2 : f == 3 ? 2 : f == 4 ? 3 : f == 5 ? 3 : f == 6 ? 4 : f == 7 ? 5 : f == 8 ? 6 : f == 9 ? 6 : f == 10 ? 7 : 7);
}
template <int G, int S> FI bf16x8 rdfrag(const G1State& st, int f) {
    if constexpr (S >= 4) { const int k = fkind(G, f); return trfrag<768>((lcp)(uintptr_t)(st.wbf[fpos(G, f)] + (k & 1) * 12288 + (k >= 2 ? 64 : (G == 0 ? 0 : 128)))); }
    else { const int e = qpos(G, f); return trfrag<256>((lcp)(uintptr_t)(st.wbq[e & 3] + (e >> 2) * 4096 + (qkind(G, f) ? 16384 : G * 8192))); }
}
template <int C> FI void rdx(G1State& st) {
#pragma unroll
    for (int e = 0; e < 4; ++e) { st.xf[4 * C + e] = ldsf((lcp)(uintptr_t)st.xb[e]); if (C < 7) st.xb[e] = (unsigned)opaque((int)(st.xb[e] + (xslot(C + 1) - xslot(C)))); }
}
template <int G, int S, bool FRESH, bool XF, int XP, int NX, class F>
FI void stage(G1State& st, F&& hook) {
    if constexpr (XF) rdx<(XF ? 2 * S : 0)>(st);
    if constexpr (FRESH) {
#pragma unroll
        for (int f = 0; f < 4; ++f) st.wq[f] = rdfrag<G, S>(st, f);
    }
    if constexpr (XF) rdx<(XF ? 2 * S + 1 : 0)>(st);
    SB();
#pragma unroll
    for (int f = 0; f < 12; ++f) {
        const bf16x8 w = st.wq[f & 3];
        if constexpr (S >= 4) {
            const int k = fkind(G, f); const bf16x8 x = st.xf[4 * (S - 4) + fpos(G, f)];
            if (k == 0) st.a0 = mfma(w, x, st.a0); else if (k == 1) st.a1 = mfma(w, x, st.a1); else if (k == 2) st.b0 = mfma(w, x, st.b0); else st.b1 = mfma(w, x, st.b1);
        } else {
            const bf16x8 x = st.xf[8 * S + qpos(G, f)];
            if (qkind(G, f)) st.qb = mfma(w, x, st.qb); else if (G == 0) st.qa = mfma(w, x, st.qa); else st.qa = mfma(x, w, st.qa);
        }
        SB();
        if (f < 8) st.wq[f & 3] = rdfrag<G, S>(st, f + 4);
        else {
            if (f == 8) {
                if constexpr (S < 3) { _Pragma("unroll") for (int j = 0; j < 4; ++j) st.wbq[j] = (unsigned)opaque((int)(st.wbq[j] + (wslot(S + 1) - wslot(S)))); }
                if constexpr (S >= 4 && S < 11) { _Pragma("unroll") for (int j = 0; j < 4; ++j) st.wbf[j] = (unsigned)opaque((int)(st.wbf[j] + (wslot(S + 1) - wslot(S)))); }
            }
            if constexpr (NX >= 0) st.wq[f & 3] = rdfrag<G, (NX >= 0 ? NX : 0)>(st, f - 8);
        }
        if constexpr (XP >= 0) { if (f == 2) rdx<(XP >= 0 ? XP : 0)>(st); if (f == 6) rdx<(XP >= 0 ? XP + 1 : 0)>(st); }
        hook(f);
        SB();
    }
}

FI u64 mix64(u64 z) { z += 0x9E3779B97F4A7C15ull; z = (z ^ (z >> 30)) * 0xBF58476D1CE4E5B9ull; z = (z ^ (z >> 27)) * 0x94D049BB133111EBull; return z ^ (z >> 31); }

constexpr unsigned SPIN_CAP = 1u << 18;
FI void set_timeout(char* ws, int lane) { if (lane == 0) __hip_atomic_store((GAS unsigned*)(ws + WS_TMO), 1u, __ATOMIC_RELAXED, __HIP_MEMORY_SCOPE_AGENT); }
FI void poll_flags(const u64* flags, int n, u64 tag, int lane, char* ws) {
    const GAS u64* pp = (const GAS u64*)flags + (lane < n ? lane : 0);
    for (unsigned spins = 0;; ++spins) {
        const u64 v = __hip_atomic_load(pp, __ATOMIC_RELAXED, __HIP_MEMORY_SCOPE_AGENT);
        if (__all(v == tag)) return;
        if (spins > SPIN_CAP) { set_timeout(ws, lane); return; }
        __builtin_amdgcn_s_sleep(2);
    }
}
FI void store_flag(char* ws, size_t off, u64 tag) { __hip_atomic_store((GAS u64*)(ws + off), tag, __ATOMIC_RELAXED, __HIP_MEMORY_SCOPE_AGENT); }

__global__ __launch_bounds__(512, 2) void fused_kernel(Params P) {
    extern __shared__ __attribute__((aligned(16))) char smem[];
    Ctx c; c.p = &P;
    const int tid = threadIdx.x; c.lane = tid & 63;
    const int w = __builtin_amdgcn_readfirstlane(tid >> 6);
    c.w = w; c.tm = w & 3; c.g = w >> 2;
    c.lds = (lp)smem; c.lds0 = (unsigned)(uintptr_t)c.lds;
    const int xcd = blockIdx.x & 7, idx = blockIdx.x >> 3;
    c.rb = 8 * (xcd >> 2) + (idx >> 2); c.cg = 4 * (xcd & 3) + (idx & 3);
    c.tb = 8 * (xcd >> 1) + (idx >> 2); c.nb = 4 * (xcd & 1) + (idx & 3);
    const int fs = c.cg < 11 ? 3 * c.cg : c.cg < 15 ? 33 + 2 * (c.cg - 11) : 40;
    const bool v0 = c.cg != 15, v2 = c.cg < 11 || c.cg == 15;
    const int lane = c.lane, h = lane >> 5, r32 = lane & 31;
    char* ws = P.ws;
    c.px = (const char*)P.x + (size_t)(128 * c.rb + 16 * w) * 1024;
    c.pf = (const char*)(c.g ? P.wu : P.wg) + (size_t)(16 * c.tm) * 2752 + fs * 64;
    u64 tag = 0; unsigned tagLo = 0, tagHi = 0; float ln_a = 0.f, ln_m = 0.f;
    const int T = 4 * c.rb + c.tm;
    const __amdgpu_buffer_rsrc_t wsr = __builtin_amdgcn_make_buffer_rsrc(ws, 0, 32u << 20, 0x00020000);
    c.pq = (const char*)P.wqkv + ((size_t)(16 * w) * 1536 + 32 * c.cg) * 2;
    c.pwd = (const char*)P.wd + (size_t)(8 * w) * 1024 + c.nb * 128;
    c.pwo = (const char*)P.wo + (size_t)(8 * w) * 1024 + c.nb * 128;
    u32x4 g0a, g0b;
#define GATHER_OFFS(tile) const int gl_ = opaque(lane); const unsigned go0 = (unsigned)(WS_CS + (c.cg * 9 + (tile)) * 2048 + (gl_ >> 3) * 256 + (gl_ & 7) * 32)
#define GATHER_ISSUE(tile) do { GATHER_OFFS(tile); aload16_sc1(g0a, P.ws, go0); aload16_sc1(g0b, P.ws + 16, go0); } while (0)
#define GATHER_OK() (g0a[1] == tagLo && g0a[3] == tagLo && g0b[1] == tagLo && g0b[3] == tagLo)
#define GATHER_RED(tile) do { const int gl_ = opaque(lane); float v0_ = __uint_as_float(g0a[0]), v1_ = __uint_as_float(g0a[2]), v2_ = __uint_as_float(g0b[0]), v3_ = __uint_as_float(g0b[2]); \
        swap32add(v0_, v2_); swap32add(v1_, v3_); swap16add(v0_, v1_); v0_ = dppadd<0x128>(v0_); \
        if ((gl_ & 8) == 0) *(LAS float*)(c.lds + L_CS + (tile) * 128 + (4 * (gl_ & 7) + 2 * (gl_ >> 5) + ((gl_ >> 4) & 1)) * 4) = v0_; } while (0)
#define GATHER_CHECK(tile) do { asm volatile("" : "+v"(g0a), "+v"(g0b)); \
        if (!__all(GATHER_OK())) { GATHER_OFFS(tile); \
            for (unsigned spins = 0;; ++spins) { __builtin_amdgcn_s_sleep(2); \
                g0a = __builtin_amdgcn_raw_buffer_load_b128(wsr, go0, 0, 16); g0b = __builtin_amdgcn_raw_buffer_load_b128(wsr, go0 + 16, 0, 16); __builtin_amdgcn_s_waitcnt(0x0F70); \
                if (__all(GATHER_OK())) break; if (spins > SPIN_CAP) { set_timeout(ws, lane); break; } } } \
        } while (0)
#define F4(v, b) (f32x4){v[b], v[(b) + 1], v[(b) + 2], v[(b) + 3]}

    const int th = w & 1, kq = w >> 1;
    const int T2 = 2 * c.tb + th;
    bf16x8 qf; bool edone_ = false;
    auto gemm1 = [&](auto Gc) {
        constexpr int G = decltype(Gc)::value;
        const int lane = opaque(c.lane), h = lane >> 5, r32 = lane & 31, tid = w * 64 + lane;
        c.vq = (lane >> 2) * 3072 + (lane & 3) * 16;
        c.vx = (lane >> 3) * 1024 + (((lane & 7) ^ (lane >> 4)) * 16); c.vlin = lane * 16;
        G1State st;
        issue_x(c, 0); issue_x(c, 1); issue_w<0>(c);
        u64 ep; aload_flag(ep, ws + WS_EPOCH, 0u);
        u32x4 ax; aload16(ax, (const char*)P.x + (size_t)(128 * c.rb + 8 * c.cg + w) * 1024, c.vlin);
        u32x4 aw0, aw1, aw2, aw3;
        const int ptile = 2 * w + (c.rb & 1); const bool pvalid = ptile <= 8;
        aw0 = aw1 = aw2 = aw3 = (u32x4){0u, 0u, 0u, 0u};
        if (pvalid) {
            const char* tb; unsigned pitch, vo;
            if (ptile < 3) { tb = (const char*)P.wqkv + (ptile == 0 ? 512 : ptile == 1 ? 1024 : 0) * 2 + 64 * c.cg; pitch = 3072; vo = c.vq; }
            else { tb = (const char*)((ptile & 1) ? P.wg : P.wu) + 64 * (fs + ((ptile - 3) >> 1)); pitch = 2752; vo = (lane >> 2) * 2752 + (lane & 3) * 16; }
            tb += (size_t)(64 * (c.rb >> 1)) * pitch;
            aload16(aw0, tb, vo); aload16(aw1, tb + 16 * pitch, vo); aload16(aw2, tb + 32 * pitch, vo); aload16(aw3, tb + 48 * pitch, vo);
        }
        c.vf0 = (lane / 12) * 2752 + (lane % 12) * 16; c.vf1 = ((lane + 64) / 12) * 2752 + ((lane + 64) % 12) * 16; c.vf2 = ((lane + 128) / 12) * 2752 + ((lane + 128) % 12) * 16;
        issue_w<4>(c);
        *(LAS unsigned*)(c.lds + L_ZERO + tid * 4) = 0u;
        if (tid < 4) { *(LAS unsigned*)(c.lds + L_ONES + tid * 4) = 0x3f803f80u; *(LAS unsigned*)(c.lds + L_ONES + 1024 + tid * 4) = 0x3f803f80u; }
        { const unsigned lb = c.lds0 + (8 * h + ((lane & 15) >> 2)) * 64 + 32 * ((lane >> 4) & 1) + 8 * (lane & 3);
#pragma unroll
          for (int j = 0; j < 4; ++j) st.wbq[j] = lb + ((j ^ c.tm) * 1024) + wslot(0); }
        { const unsigned lb = c.lds0 + (8 * h + ((lane & 15) >> 2)) * 192 + 32 * ((lane >> 4) & 1) + 8 * (lane & 3);
#pragma unroll
          for (int j = 0; j < 4; ++j) st.wbf[j] = lb + ((j ^ c.tm) * 3072) + wslot(4); }
        { const int row = 32 * c.tm + r32;
#pragma unroll
          for (int j = 0; j < 4; ++j) st.xb[j] = c.lds0 + xslot(0) + row * 128 + (((2 * (j ^ c.tm) + h) ^ ((row >> 1) & 7)) * 16); }
        st.a0 = f32x16{}; st.a1 = f32x16{}; st.b0 = f32x16{}; st.b1 = f32x16{}; st.qa = f32x16{}; st.qb = f32x16{};

        SYNC(3);
        asm volatile("" : "+v"(ep), "+v"(ax), "+v"(aw0), "+v"(aw1), "+v"(aw2), "+v"(aw3));
        tag = mix64(((u64)(unsigned)__builtin_amdgcn_readfirstlane((int)(unsigned)(ep >> 32)) << 32) | (unsigned)__builtin_amdgcn_readfirstlane((int)(unsigned)ep));
        tagLo = (unsigned)tag; tagHi = (unsigned)(tag >> 32);

        float as_ = 0.f, aq_ = 0.f, av[8];
        auto aux0 = [&](int f) {
            if (f < 4) { const float a = blo(ax[f]), b = bhi(ax[f]); as_ += a + b; aq_ = __builtin_fmaf(a, a, aq_); aq_ = __builtin_fmaf(b, b, aq_); PIN(as_); PIN(aq_); }
            if (f == 4) { as_ = dppadd<0x121>(as_); aq_ = dppadd<0x121>(aq_); as_ = dppadd<0x122>(as_); aq_ = dppadd<0x122>(aq_); PIN(as_); PIN(aq_); }
            if (f == 5) { as_ = dppadd<0x124>(as_); aq_ = dppadd<0x124>(aq_); as_ = dppadd<0x128>(as_); aq_ = dppadd<0x128>(aq_); PIN(as_); PIN(aq_); }
            if (f == 6) { swap16add(as_, as_); swap16add(aq_, aq_); swap32add(as_, as_); swap32add(aq_, aq_); PIN(as_); PIN(aq_); }
            if (f == 8) { const float mean = as_ * (1.f / 512.f), var = aq_ * (1.f / 512.f) - mean * mean, rstd = __builtin_amdgcn_rsqf(fmaxf(var, 0.f) + 1e-5f);
                if (lane == 0) __builtin_amdgcn_raw_buffer_store_b128((u32x4){__float_as_uint(mean), tagLo, __float_as_uint(rstd), tagHi}, wsr, (unsigned)(WS_ST + (128 * c.rb + 8 * c.cg + w) * 16), 0, 16); }
        };
        auto aux1 = [&](int f) {
            if (f < 4) { const int i = f; av[2 * i] = (blo(aw0[i]) + blo(aw1[i])) + (blo(aw2[i]) + blo(aw3[i])); av[2 * i + 1] = (bhi(aw0[i]) + bhi(aw1[i])) + (bhi(aw2[i]) + bhi(aw3[i])); PIN(av[2 * i]); PIN(av[2 * i + 1]); }
            if (f == 8) { swap32add(av[0], av[4]); swap32add(av[1], av[5]); swap32add(av[2], av[6]); swap32add(av[3], av[7]); PIN(av[0]); PIN(av[1]); PIN(av[2]); PIN(av[3]); }
            if (f == 9) { swap16add(av[0], av[2]); swap16add(av[1], av[3]); av[0] = dppadd<0x124>(av[0]); av[1] = dppadd<0x124>(av[1]); av[0] = dppadd<0x128>(av[0]); av[1] = dppadd<0x128>(av[1]); PIN(av[0]); PIN(av[1]); }
            if (f == 10) { const int rho = lane >> 4, col = 8 * (lane & 3) + 4 * (rho >> 1) + 2 * (rho & 1);
                const unsigned off = pvalid ? (unsigned)(WS_CS + (((c.cg * 9 + ptile) * 8 + (c.rb >> 1)) * 32 + col) * 8) : (unsigned)(WS_SCR + (blockIdx.x * 8 + w) * 256 + col * 8);
                if ((lane & 12) == 0) __builtin_amdgcn_raw_buffer_store_b128((u32x4){__float_as_uint(av[0]), tagLo, __float_as_uint(av[1]), tagLo}, wsr, off, 0, 16); }
        };
        u32x4 gs;
        const int gt1 = w < 3 ? w : w < 6 ? w - 3 : w - 6, gt2 = w < 6 ? 3 + w : w - 3;
        stage<G, 0, true, true, -1, -1>(st, [&](int f_) { if (f_ < 2) issue_x1(c, 2, f_); else if (f_ < 4) issue_x1(c, 3, f_ - 2); else if (f_ < 7) issue_w1<1>(c, f_ - 4); aux0(f_); }); SYNCV(8);
        unsigned pfw = 0;
        auto warmw = [&]() { int row = 8 * (w * 64 + opaque(lane)) + (idx >> 2); if (row > 831) row = 831;
            aprefetch(pfw, (row < 1376 ? (const char*)P.wd + (size_t)row * 1024 : (const char*)P.wo + (size_t)(row - 1376) * 1024) + c.nb * 128); };
        stage<G, 4, true, false, -1, -1>(st, [&](int f_) { if (f_ < 3) issue_w1<5>(c, f_); else if (f_ < 5) issue_x1(c, 4, f_ - 3); else if (f_ < 7) issue_x1(c, 5, f_ - 5); aux1(f_); if (f_ == 11) warmw(); }); SYNCV(10);
        stage<G, 1, true, true, -1, -1>(st, [&](int f_) { if (f_ >= 1 && f_ < 4) issue_w1<2>(c, f_ - 1); }); SYNCV(5);
        stage<G, 5, true, false, 4, -1>(st, [&](int f_) { if (f_ < 3) issue_w1<6>(c, f_); else if (f_ < 5) issue_x1(c, 6, f_ - 3); else if (f_ < 7) issue_x1(c, 7, f_ - 5); }); SYNCV(7);
        PIN(pfw);
        stage<G, 2, true, false, -1, -1>(st, [&](int f_) { if (f_ >= 1 && f_ < 4) issue_w1<3>(c, f_ - 1);
            if (f_ == 8) { aload16_sc1(gs, P.ws, (unsigned)(WS_ST + (128 * c.rb + 32 * c.tm + (opaque(lane) & 31)) * 16)); GATHER_ISSUE(gt1); } }); SYNCV(6);
        stage<G, 6, true, false, 6, -1>(st, [&](int f_) { if (f_ >= 1 && f_ < 4) issue_w1<7>(c, f_ - 1); else if (f_ >= 4 && f_ < 7) issue_w1<8>(c, f_ - 4); }); SYNCV(9);
        stage<G, 3, true, false, -1, -1>(st, [&](int f_) { if (f_ >= 1 && f_ < 4) issue_w1<9>(c, f_ - 1);
            if (f_ == 10) { WAITV(9); PIN(gs);
                if (!__all(gs[1] == tagLo && gs[3] == tagHi)) { const unsigned gso = (unsigned)(WS_ST + (128 * c.rb + 32 * c.tm + (opaque(lane) & 31)) * 16);
                    for (unsigned spins = 0;; ++spins) { __builtin_amdgcn_s_sleep(2); gs = __builtin_amdgcn_raw_buffer_load_b128(wsr, gso, 0, 16); __builtin_amdgcn_s_waitcnt(0x0F70);
                        if (__all(gs[1] == tagLo && gs[3] == tagHi)) break; if (spins > SPIN_CAP) { set_timeout(ws, lane); break; } } }
                ln_m = __uint_as_float(gs[0]); ln_a = __uint_as_float(gs[2]); PIN(ln_m); PIN(ln_a);
                GATHER_CHECK(gt1); }
            if (f_ == 11) GATHER_RED(gt1); });
        { lp xp = c.lds + 13 * GR + w * 2048 + c.vlin; *(LAS f32x4*)(xp) = F4(st.qb, 8 - 8 * G); *(LAS f32x4*)(xp + 1024) = F4(st.qb, 12 - 8 * G);
          if constexpr (G == 1) { if (h == 0) { *(LAS float*)(c.lds + L_CSW + c.tm * 256 + r32 * 4) = ln_m; *(LAS float*)(c.lds + L_CSW + c.tm * 256 + 128 + r32 * 4) = ln_a; } } }
        SYNC(3);
        issue_w<10>(c);
        { const int lane = opaque(c.lane), h = lane >> 5, r32 = lane & 31;
          const unsigned o = (unsigned)(WS_KV + ((size_t)c.cg * 64 + T) * 4096) + lane * 16 + G * 2048;
          f32x16 y;
          if constexpr (G == 0) {
#pragma unroll
              for (int q = 0; q < 4; ++q) { const f32x4 cs = ldsf4((lcp)c.lds + L_CS + (8 * q + 4 * h) * 4);
#pragma unroll
                  for (int i = 0; i < 4; ++i) y[4 * q + i] = ln_a * __builtin_fmaf(-ln_m, cs[i], st.qa[4 * q + i]); }
          } else {
              const float cso = *(const LAS float*)(c.lds + L_CS + 128 + r32 * 4);
#pragma unroll
              for (int q = 0; q < 4; ++q) { const f32x4 mm = ldsf4((lcp)c.lds + L_CSW + c.tm * 256 + (8 * q + 4 * h) * 4), aa = ldsf4((lcp)c.lds + L_CSW + c.tm * 256 + 128 + (8 * q + 4 * h) * 4);
#pragma unroll
                  for (int i = 0; i < 4; ++i) y[4 * q + i] = aa[i] * __builtin_fmaf(-mm[i], cso, st.qa[4 * q + i]); }
          }
          const u32x4 k0 = pack8(y, 0), k1 = pack8(y, 8);
          __builtin_amdgcn_raw_buffer_store_b128(k0, wsr, o, 0, 16); __builtin_amdgcn_raw_buffer_store_b128(k1, wsr, o + 1024, 0, 16);
          if (w == 0 && lane == 0) store_flag(ws, WS_SF + (size_t)blockIdx.x * 8, tag);
          *(LAS u32x4*)(c.lds + L_KVR + c.tm * 4096 + G * 2048 + lane * 16) = k0; *(LAS u32x4*)(c.lds + L_KVR + c.tm * 4096 + G * 2048 + 1024 + lane * 16) = k1;
          SB();
          const f32x4 c0 = ldsf4((lcp)c.lds + L_CS + 2 * 128 + (16 * G + 4 * h) * 4), c1 = ldsf4((lcp)c.lds + L_CS + 2 * 128 + (16 * G + 8 + 4 * h) * 4);
          const lcp xp = (lcp)c.lds + 13 * GR + (w ^ 4) * 2048 + lane * 16;
          const f32x4 p0 = ldsf4(xp), p1 = ldsf4(xp + 1024);
          const float sc = ln_a * (0.25f * 1.4426950408889634f);
#pragma unroll
          for (int i = 0; i < 4; ++i) { y[i] = sc * __builtin_fmaf(-ln_m, c0[i], st.qb[8 * G + i] + p0[i]); y[4 + i] = sc * __builtin_fmaf(-ln_m, c1[i], st.qb[8 * G + 4 + i] + p1[i]); }
          qf = __builtin_bit_cast(bf16x8, pack8(y, 0)); PIN(qf); }
        stage<G, 7, true, false, -1, 8>(st, [&](int) {}); SYNC(5);
        { const int ln = opaque(lane); c.vd = (ln >> 3) * 1024 + (((ln & 7) ^ (((ln >> 4) & 1) << 2)) * 16); }
        stage<G, 8, false, false, -1, 9>(st, [&](int f_) { if (f_ >= 1 && f_ < 4) issue_w1<11>(c, f_ - 1); if (f_ == 4) issue_d(c, 0); }); SYNCV(6);
        stage<G, 9, false, false, -1, 10>(st, [&](int f_) { if (f_ >= 1 && f_ < 4) issue_d(c, f_);
            if (f_ == 5) { WAITV(7); if (lane == 0) store_flag(ws, WS_KVF + ((size_t)(c.cg * 64 + T) * 2 + G) * 8, tag); }
            if (f_ == 8) GATHER_ISSUE(gt2); }); SYNCV(7);
        u64 kf_;
        stage<G, 10, false, false, -1, 11>(st, [&](int f_) { if (f_ >= 1 && f_ < 4) issue_d(c, 3 + f_);
            if (f_ == 6) { WAITV(3); GATHER_CHECK(gt2); GATHER_RED(gt2); }
            if (f_ == 10) { const int tl = 4 * c.rb - 8 + w; aload_flag(kf_, P.ws, (unsigned)(WS_KVF + ((c.cg * 64 + (tl < 0 ? 0 : tl)) * 2 + (opaque(lane) & 1)) * 8)); } }); SYNCV(63);
        stage<G, 11, false, false, -1, -1>(st, [&](int f_) { if (f_ >= 1 && f_ < 3) issue_d(c, 6 + f_);
            if (f_ == 6) { WAITV(2); asm volatile("" : "+v"(kf_)); const int tl = 4 * c.rb - 8 + w;
                if (tl >= 0) {
                    if (!__all(kf_ == tag)) poll_flags((const u64*)(ws + WS_KVF) + (c.cg * 64 + tl) * 2, 2, tag, lane, ws);
                    const char* base = ws + WS_KV + ((size_t)c.cg * 64 + tl) * 4096; const unsigned dst = c.lds0 + L_KV + w * 4096;
                    dma16_sc1(c.vlin, base, dst); dma16_sc1(c.vlin, base + 1024, dst + 1024); dma16_sc1(c.vlin, base + 2048, dst + 2048); dma16_sc1(c.vlin, base + 3072, dst + 3072); } } });
        {
          lp xp = c.lds + 2 * GR + w * 4096 + c.vlin;
          SYNCV(63);
          *(LAS f32x4*)(xp) = F4(st.b0, 8 - 8 * G); *(LAS f32x4*)(xp + 1024) = F4(st.b0, 12 - 8 * G); *(LAS f32x4*)(xp + 2048) = F4(st.b1, 8 - 8 * G); *(LAS f32x4*)(xp + 3072) = F4(st.b1, 12 - 8 * G); }
        SYNC(63);
        {
          const int lane = opaque(c.lane), h = lane >> 5;
          const float a2 = ln_a * ln_a;
          { u32x4 y0, y1;
#pragma unroll
            for (int q = 0; q < 4; ++q) {
                const f32x4 cg_ = ldsf4((lcp)c.lds + L_CS + (3 + 4 * G) * 128 + (8 * q + 4 * h) * 4), cu_ = ldsf4((lcp)c.lds + L_CS + (4 + 4 * G) * 128 + (8 * q + 4 * h) * 4);
                float y[4];
#pragma unroll
                for (int i = 0; i < 4; ++i) { const float g_ = fmaxf(__builtin_fmaf(-ln_m, cg_[i], st.a0[4 * q + i]), 0.f), u_ = __builtin_fmaf(-ln_m, cu_[i], st.a1[4 * q + i]); y[i] = (g_ * a2) * u_; }
                const unsigned lo = pk2(y[0], y[1]), hi = pk2(y[2], y[3]);
                if (q < 2) { y0[2 * q] = lo; y0[2 * q + 1] = hi; } else { y1[2 * q - 4] = lo; y1[2 * q - 3] = hi; }
            }
            if (G == 0 ? v0 : v2) { const unsigned o_ = (unsigned)(WS_C + ((size_t)T * NKS + 2 * (fs + 2 * G)) * 1024) + lane * 16;
                __builtin_amdgcn_raw_buffer_store_b128(y0, wsr, o_, 0, 16); __builtin_amdgcn_raw_buffer_store_b128(y1, wsr, o_ + 1024, 0, 16); } }
          { const lcp xp = (lcp)c.lds + 2 * GR + (w ^ 4) * 4096 + lane * 16;
            u32x4 y0;
#pragma unroll
            for (int q = 0; q < 2; ++q) {
                const f32x4 pg = ldsf4(xp + q * 1024), pu = ldsf4(xp + 2048 + q * 1024);
                const f32x4 cg_ = ldsf4((lcp)c.lds + L_CS + 5 * 128 + (16 * G + 8 * q + 4 * h) * 4), cu_ = ldsf4((lcp)c.lds + L_CS + 6 * 128 + (16 * G + 8 * q + 4 * h) * 4);
                float y[4];
#pragma unroll
                for (int i = 0; i < 4; ++i) { const float g_ = fmaxf(__builtin_fmaf(-ln_m, cg_[i], st.b0[8 * G + 4 * q + i] + pg[i]), 0.f), u_ = __builtin_fmaf(-ln_m, cu_[i], st.b1[8 * G + 4 * q + i] + pu[i]); y[i] = (g_ * a2) * u_; }
                y0[2 * q] = pk2(y[0], y[1]); y0[2 * q + 1] = pk2(y[2], y[3]);
            }
            __builtin_amdgcn_raw_buffer_store_b128(y0, wsr, (unsigned)(WS_C + ((size_t)T * NKS + 2 * (fs + 1) + G) * 1024) + lane * 16, 0, 16); } }
        if (G == 0 ? v0 : v2) { SYNC(3); } else { SYNC(1); }
        if constexpr (G == 1) __builtin_amdgcn_s_setprio(1);
    };
    if (c.g == 0) gemm1(std::integral_constant<int, 0>{}); else gemm1(std::integral_constant<int, 1>{});
    const int lane2 = opaque(lane);
    const unsigned cbase = (unsigned)(WS_C + ((size_t)T2 * NKS + kq) * 1024) + lane2 * 16;
    bf16x8 hf[22];
    u64 hfl; bool hrdy_ = false;
#define LDH(i) do { const bool ok_ = ((i) < 21) || (kq < 2); hf[i] = __builtin_bit_cast(bf16x8, __builtin_amdgcn_raw_buffer_load_b128(wsr, ok_ ? cbase + (i) * 4096 : cbase, 0, 16)); } while (0)
    {
        const int lane = lane2, h = lane >> 5, r32 = lane & 31;
        const int i0 = T >= 8 ? 0 : 8 - T;
        const unsigned kb = c.lds0 + L_KV + c.tm * 4096 + c.g * 1024 + lane * 16;
        const unsigned lanebit = r32 < 16 ? 1u : 0u;
        const unsigned vb = c.lds0 + (r32 < 16 ? (unsigned)(((r32 & 15) + 16 * c.g + 32 * h) * 16) : (unsigned)L_ONES);
        auto koff = [&](int i) -> unsigned { return (unsigned)((i >= i0 ? i : 7) * 4096); };
        auto rdk = [&](int i) -> bf16x8 { return ldsf((lcp)(uintptr_t)(kb + koff(i))); };
        auto vaddr = [&](int i) -> unsigned { const bool valid = i >= i0; const unsigned d = valid ? 0u : (unsigned)(L_ZERO - L_ONES), A = valid ? (unsigned)(L_KV + 2048 + (c.tm + i) * 4096) : (unsigned)L_ZERO;
                                              return lanebit * (A - d) + (vb + d); };
        auto dmask = [&](int r) -> bool { u64 mk = 0;
#pragma unroll
            for (int l = 0; l < 64; ++l) { const int kl = (r & 3) + 8 * (r >> 2) + 4 * (l >> 5); if (kl <= (l & 31)) mk |= 1ull << l; }
            return __builtin_amdgcn_inverse_ballot_w64(mk); };
        const f32x16 zero = f32x16{};
        float m0 = -1e30f, m1 = -1e30f;
#define MAXT(S) do { _Pragma("unroll") for (int r = 0; r < 16; r += 4) { m0 = fmaxf(fmaxf(m0, S[r]), S[r + 1]); m1 = fmaxf(fmaxf(m1, S[r + 2]), S[r + 3]); } PIN(m0); PIN(m1); } while (0)
#define EXPT(S) do { _Pragma("unroll") for (int r = 0; r < 16; ++r) S[r] = __builtin_amdgcn_exp2f(S[r]); } while (0)
#define H_PUBLISH(N) do { WAITV(N); if (lane == 0) store_flag(ws, WS_HF + ((size_t)T * 32 + 2 * c.cg + c.g) * 8, tag); } while (0)
#define H_FLAGS() aload_flag(hfl, P.ws, (unsigned)(WS_HF + (T2 * 32 + (lane & 31)) * 8))
        f32x16 o = f32x16{};
        float m; bool hrdy = false;
        if (T == 0) {
            f32x16 s = mfma(ldsf((lcp)(uintptr_t)(kb + 8 * 4096)), qf, zero);
#pragma unroll
            for (int r = 0; r < 16; ++r) s[r] = dmask(r) ? s[r] : -1e30f;
            MAXT(s);
            m = fmaxf(m0, m1);
            { auto rr = __builtin_amdgcn_permlane32_swap(__float_as_uint(m), __float_as_uint(m), false, false); m = fmaxf(__uint_as_float(rr[0]), __uint_as_float(rr[1])); }
#pragma unroll
            for (int r = 0; r < 16; ++r) s[r] = __builtin_amdgcn_exp2f(s[r] - m);
            const unsigned va = vaddr(8);
            o = mfma(ldsf((lcp)(uintptr_t)va), __builtin_bit_cast(bf16x8, pack8(s, 0)), o);
            o = mfma(ldsf((lcp)(uintptr_t)(va + 1024)), __builtin_bit_cast(bf16x8, pack8(s, 8)), o);
            issue_d(c, 9); issue_d(c, 10); issue_d(c, 11); issue_d(c, 12);
            H_PUBLISH(4);
        } else {
            bf16x8 k0 = rdk(0), k8 = rdk(8), k1 = rdk(1), k2 = rdk(2), k3 = rdk(3);
            f32x16 sA, sB, sC;
            sA = mfma_pin0(k0, qf); sB = mfma_pin0(k8, qf); SB();
            k0 = rdk(4); k8 = rdk(5); sC = mfma_pin0(k1, qf); k1 = rdk(6); SB();
            asm volatile("s_nop 7\n\ts_nop 3"); SB();
#pragma unroll
            for (int r = 0; r < 16; ++r) sA[r] = dmask(r) ? sB[r] : sA[r];
            PIN(sA); SB();
            sB = mfma_pin0(k2, qf); k2 = rdk(7); SB();
            MAXT(sA); SB();
            H_PUBLISH(0);
            unsigned pfz = 0;
            if (w < 3) { const int id = w * 64 + lane; int row = 832 + 8 * id + (idx >> 2); if (row > 1887) row = 1887;
                aprefetch(pfz, (row < 1376 ? (const char*)P.wd + (size_t)row * 1024 : (const char*)P.wo + (size_t)(row - 1376) * 1024) + c.nb * 128); }
            sA = mfma_pin0(k3, qf); issue_d(c, 9); SB(); MAXT(sC); SB();
            sC = mfma_pin0(k0, qf); issue_d(c, 10); SB(); MAXT(sB); SB();
            sB = mfma_pin0(k8, qf); issue_d(c, 11); SB(); MAXT(sA); SB();
            sA = mfma_pin0(k1, qf); issue_d(c, 12); SB(); MAXT(sC); SB();
            sC = mfma_pin0(k2, qf); k0 = rdk(0); k8 = rdk(8); k1 = rdk(1); SB(); MAXT(sB); SB();
            MAXT(sA); SB(); MAXT(sC);
            m = fmaxf(m0, m1);
            { auto rr = __builtin_amdgcn_permlane32_swap(__float_as_uint(m), __float_as_uint(m), false, false); m = fmaxf(__uint_as_float(rr[0]), __uint_as_float(rr[1])); }
            f32x16 cm;
#pragma unroll
            for (int r = 0; r < 16; ++r) cm[r] = -m;
            PIN(cm);
            asm volatile("s_nop 1"); sA = mfma_pin(k0, qf, cm); sB = mfma_pin(k8, qf, cm); sC = mfma_pin(k1, qf, cm); SB();
            bf16x8 va0, va1, vb0, vb1;
            { const unsigned a0 = vaddr(0), a8 = vaddr(8); va0 = ldsf((lcp)(uintptr_t)a0); va1 = ldsf((lcp)(uintptr_t)(a0 + 1024)); vb0 = ldsf((lcp)(uintptr_t)a8); vb1 = ldsf((lcp)(uintptr_t)(a8 + 1024)); }
            k0 = rdk(2);
            unsigned mp[8];
#pragma unroll
            for (int j = 0; j < 8; ++j) mp[j] = (dmask(2 * j) ? 0xffffu : 0u) | (dmask(2 * j + 1) ? 0xffff0000u : 0u);
            SB(); asm volatile("s_nop 7"); SB();
#pragma unroll
            for (int r = 0; r < 16; ++r) sA[r] = dmask(r) ? sB[r] : sA[r];
            EXPT(sA);
            { const u32x4 p0 = pack8(sA, 0), p1 = pack8(sA, 8); u32x4 d0, d1, o0, o1;
#pragma unroll
              for (int j = 0; j < 4; ++j) { d0[j] = p0[j] & mp[j]; d1[j] = p1[j] & mp[4 + j]; o0[j] = p0[j] & ~mp[j]; o1[j] = p1[j] & ~mp[4 + j]; }
              o = mfma(va0, __builtin_bit_cast(bf16x8, o0), o); o = mfma(va1, __builtin_bit_cast(bf16x8, o1), o);
              o = mfma(vb0, __builtin_bit_cast(bf16x8, d0), o); o = mfma(vb1, __builtin_bit_cast(bf16x8, d1), o); }
            { const unsigned a1 = vaddr(1), a2 = vaddr(2); va0 = ldsf((lcp)(uintptr_t)a1); va1 = ldsf((lcp)(uintptr_t)(a1 + 1024)); vb0 = ldsf((lcp)(uintptr_t)a2); vb1 = ldsf((lcp)(uintptr_t)(a2 + 1024)); }
            SB();
#define EXP8(S, b) do { _Pragma("unroll") for (int r = (b); r < (b) + 8; ++r) S[r] = __builtin_amdgcn_exp2f(S[r]); } while (0)
#define PK4(S, b) (u32x4){pk2(S[b], S[(b) + 1]), pk2(S[(b) + 2], S[(b) + 3]), pk2(S[(b) + 4], S[(b) + 5]), pk2(S[(b) + 6], S[(b) + 7])}
#define TILE2(i, S, V0, V1, SN) do { \
                if ((i) + 1 <= 7) { SN = mfma_pin(k0, qf, cm); SB(); if ((i) + 2 <= 7) k0 = rdk((i) + 2); } \
                EXP8(S, 0); { u32x4 p_ = PK4(S, 0); PIN(p_); SB(); o = mfma(V0, __builtin_bit_cast(bf16x8, p_), o); SB(); } \
                EXP8(S, 8); { u32x4 p_ = PK4(S, 8); PIN(p_); SB(); o = mfma(V1, __builtin_bit_cast(bf16x8, p_), o); SB(); } \
                if ((i) + 2 <= 7) { const unsigned a_ = vaddr((i) + 2); V0 = ldsf((lcp)(uintptr_t)a_); V1 = ldsf((lcp)(uintptr_t)(a_ + 1024)); } SB(); } while (0)
            TILE2(1, sC, va0, va1, sB); TILE2(2, sB, vb0, vb1, sC);
            H_FLAGS();
            TILE2(3, sC, va0, va1, sB); TILE2(4, sB, vb0, vb1, sC);
            WAITV(0); asm volatile("" : "+v"(hfl), "+v"(pfz));
            hrdy = __all(hfl == tag);
            if (hrdy) { LDH(0); LDH(1); LDH(2); LDH(3); LDH(4); LDH(5); LDH(6); }
            TILE2(5, sC, va0, va1, sB);
            if (hrdy) { LDH(7); LDH(8); LDH(9); LDH(10); LDH(11); LDH(12); LDH(13); }
            TILE2(6, sB, vb0, vb1, sC);
            if (hrdy) { LDH(14); LDH(15); LDH(16); LDH(17); LDH(18); LDH(19); LDH(20); LDH(21); }
            TILE2(7, sC, va0, va1, sB);
        }
        const float inv = __builtin_amdgcn_rcpf(o[8]);
        f32x16 y;
#pragma unroll
        for (int r = 0; r < 8; ++r) y[r] = o[r] * inv;
        const unsigned oo = (unsigned)(WS_C + ((size_t)T * NKS + 86 + 2 * c.cg + c.g) * 1024) + lane * 16;
        const u32x4 ov = pack8(y, 0);
        __builtin_amdgcn_raw_buffer_store_b128(ov, wsr, oo, 0, 16);
        if (!hrdy) {
            poll_flags((const u64*)(ws + WS_HF) + T2 * 32, 32, tag, lane, ws);
#pragma unroll
            for (int i = 0; i < 22; ++i) LDH(i);
        }
        hrdy_ = hrdy;
        __builtin_amdgcn_s_waitcnt(0x0F71);
        __builtin_amdgcn_s_setprio(0);
    }
    {
        const int lane = opaque(lane2), h = lane >> 5, r32 = lane & 31, q4 = (lane & 15) >> 2, p4 = lane & 3, b16 = (lane >> 4) & 1;
        f32x16 acc0 = f32x16{}, acc1 = f32x16{};
        const unsigned wl = c.lds0 + kq * 2048 + (4 * h + q4) * 128 + 32 * b16 + 8 * p4, swz = ((q4 >> 1) & 1) << 6;
        const unsigned wl0 = wl + (0u ^ swz), wl1 = wl + (64u ^ swz);
        bf16x8 wa[3], wb_[3];
#define RDW(u) do { wa[(u) % 3] = trfrag<1024>((lcp)(uintptr_t)(wl0 + dloc(u))); wb_[(u) % 3] = trfrag<1024>((lcp)(uintptr_t)(wl1 + dloc(u))); } while (0)
#define G2STEP(u, frag, nxt) do { acc0 = mfma(wa[(u) % 3], frag, acc0); acc1 = mfma(wb_[(u) % 3], frag, acc1); SB(); if ((nxt) >= 0) RDW((nxt) < 0 ? 0 : (nxt)); SB(); } while (0)
        SYNC(63);
        const bool ew = blockIdx.x == 0 && w == 7; u32x4 sf0 = {0u, 0u, 0u, 0u}, sf1 = sf0; bool edone = false;
        if (ew) { aload16_sc1(sf0, P.ws + WS_SF, (unsigned)(lane * 32)); aload16_sc1(sf1, P.ws + WS_SF + 16, (unsigned)(lane * 32)); }
        RDW(0); RDW(1); RDW(2);
        G2STEP(0, hf[0], 3);
        issue_d(c, 13); G2STEP(1, hf[1], 4); issue_d(c, 14); G2STEP(2, hf[2], 5); issue_d(c, 15); G2STEP(3, hf[3], 6); issue_d(c, 16); G2STEP(4, hf[4], 7); issue_d(c, 17); G2STEP(5, hf[5], 8); issue_d(c, 18);
        if (hrdy_) { WAITV(6); }
        if (lane == 0) store_flag(ws, WS_OF + ((size_t)T * 32 + 2 * c.cg + c.g) * 8, tag);
        SYNCV(63);
        G2STEP(6, hf[6], 9); issue_d(c, 19); G2STEP(7, hf[7], 10); issue_d(c, 20); G2STEP(8, hf[8], 11); issue_d(c, 21); G2STEP(9, hf[9], 12); issue_d(c, 22);
        G2STEP(10, hf[10], -1); issue_d(c, 23); G2STEP(11, hf[11], -1); issue_d(c, 24); G2STEP(12, hf[12], -1);
        SYNCV(7);
        if (ew) { asm volatile("" : "+v"(sf0), "+v"(sf1));
            edone = __all(sf0[0] == tagLo && sf0[1] == tagHi && sf0[2] == tagLo && sf0[3] == tagHi && sf1[0] == tagLo && sf1[1] == tagHi && sf1[2] == tagLo && sf1[3] == tagHi);
            if (edone && lane == 0) store_flag(ws, WS_EPOCH, tag); }
        edone_ = edone;
        RDW(13); RDW(14); RDW(15);
        u32x4 xres;
        u64 ofl;
        issue_d(c, 25); G2STEP(13, hf[13], 16); issue_d(c, 26); G2STEP(14, hf[14], 17); issue_d(c, 27); G2STEP(15, hf[15], 18); issue_d(c, 28);
        aload_flag(ofl, P.ws, (unsigned)(WS_OF + (T2 * 32 + (lane & 31)) * 8));
        G2STEP(16, hf[16], -1); issue_d(c, 29);
        xres = *(const u32x4*)(P.x + (size_t)(64 * c.tb + 8 * w + (lane >> 3)) * 512 + 64 * c.nb + (lane & 7) * 8);
        G2STEP(17, hf[17], -1); G2STEP(18, hf[18], -1);
        WAITV(2); asm volatile("" : "+v"(ofl));
        if (!__all(ofl == tag)) poll_flags((const u64*)(ws + WS_OF) + T2 * 32, 32, tag, lane, ws);
        bf16x8 of[8];
#pragma unroll
        for (int i = 0; i < 8; ++i) of[i] = __builtin_bit_cast(bf16x8, __builtin_amdgcn_raw_buffer_load_b128(wsr, cbase + (86 + 4 * i) * 1024, 0, 16));
        SYNCV(63);
        RDW(19); RDW(20); RDW(21);
        G2STEP(19, hf[19], 22); G2STEP(20, hf[20], 23);
        if (kq < 2) { acc0 = mfma(wa[21 % 3], hf[21], acc0); acc1 = mfma(wb_[21 % 3], hf[21], acc1); }
        SB(); RDW(24); SB();
        G2STEP(22, of[0], -1); G2STEP(23, of[1], -1); G2STEP(24, of[2], -1);
        SYNCV(63);
        RDW(25); RDW(26); RDW(27);
        G2STEP(25, of[3], 28); G2STEP(26, of[4], 29); G2STEP(27, of[5], -1); G2STEP(28, of[6], -1); G2STEP(29, of[7], -1);
        { lp rbp = c.lds + (kq < 2 ? 4 * GR : 13 * GR - 32768) + (kq * 2 + th) * 8192;
#pragma unroll
          for (int q = 0; q < 4; ++q) { *(LAS f32x4*)(rbp + q * 1024 + ((lane + 2 * q) & 63) * 16) = F4(acc0, 4 * q); *(LAS f32x4*)(rbp + 4096 + q * 1024 + ((lane + 2 * (4 + q)) & 63) * 16) = F4(acc1, 4 * q); } }
        SYNC(63);
        {
          const int t = 8 * w + (lane >> 3), cc = lane & 7;
          const unsigned a0 = (unsigned)((t >> 5) * 8192 + cc * 1024), s0i = (unsigned)(((t & 31) + 2 * cc) & 63) * 16, s1i = (unsigned)(((t & 31) + 32 + 2 * cc) & 63) * 16;
          f32x4 s0 = {0.f, 0.f, 0.f, 0.f}, s1 = s0;
#pragma unroll
          for (int k = 0; k < 4; ++k) { const lcp bp = (lcp)c.lds + (k < 2 ? 4 * GR : 13 * GR - 32768) + k * 16384 + a0; s0 += ldsf4(bp + s0i); s1 += ldsf4(bp + s1i); }
          u32x4 ov;
          ov[0] = pk2(s0[0] + blo(xres[0]), s0[1] + bhi(xres[0])); ov[1] = pk2(s0[2] + blo(xres[1]), s0[3] + bhi(xres[1]));
          ov[2] = pk2(s1[0] + blo(xres[2]), s1[1] + bhi(xres[2])); ov[3] = pk2(s1[2] + blo(xres[3]), s1[3] + bhi(xres[3]));
          *(u32x4*)(P.out + (size_t)(64 * c.tb + t) * 512 + 64 * c.nb + cc * 8) = ov; }
    }
    if (blockIdx.x == 0 && w == 7 && !edone_) {
        const GAS u64* pp = (const GAS u64*)(ws + WS_SF) + lane * 4;
        for (unsigned spins = 0;; ++spins) { bool ok = true;
#pragma unroll
            for (int j = 0; j < 4; ++j) ok &= __hip_atomic_load(pp + j, __ATOMIC_RELAXED, __HIP_MEMORY_SCOPE_AGENT) == tag;
            if (__all(ok)) break; if (spins > SPIN_CAP) { set_timeout(ws, lane); break; } __builtin_amdgcn_s_sleep(2); }
        if (lane == 0) store_flag(ws, WS_EPOCH, tag);
    }
}

extern "C" void kernel_launch(void* const* d_in, const int* in_sizes, int n_in, void* d_out, int out_size, void* d_ws, size_t ws_size, hipStream_t stream) {
    Params p;
    p.x = (const u16*)d_in[0]; p.wqkv = (const u16*)d_in[1]; p.wo = (const u16*)d_in[2];
    p.wg = (const u16*)d_in[4]; p.wu = (const u16*)d_in[5]; p.wd = (const u16*)d_in[6];
    p.out = (u16*)d_out; p.ws = (char*)d_ws;
    (void)hipFuncSetAttribute((const void*)fused_kernel, hipFuncAttributeMaxDynamicSharedMemorySize, LDS_BYTES);
    fused_kernel<<<256, 512, LDS_BYTES, stream>>>(p);
}
```

```cpp
#include <hip/hip_runtime.h>
#include <stdint.h>
#include <type_traits>

#define FI __device__ __forceinline__
#define LAS __attribute__((address_space(3)))
#define GAS __attribute__((address_space(1)))
typedef unsigned short u16;
typedef unsigned long long u64;
typedef short bf16x8 __attribute__((ext_vector_type(8)));
typedef short s16x4 __attribute__((ext_vector_type(4)));
typedef float f32x16 __attribute__((ext_vector_type(16)));
typedef float f32x4 __attribute__((ext_vector_type(4)));
typedef float f32x2 __attribute__((ext_vector_type(2)));
typedef unsigned u32x4 __attribute__((ext_vector_type(4)));
typedef __bf16 bf16x2_t __attribute__((ext_vector_type(2)));
typedef LAS const char* lcp;
typedef LAS char* lp;

constexpr int GR = 8192;
constexpr int L_KV = 13 * GR;
constexpr int L_KVR = 17 * GR;
constexpr int L_MISC = 19 * GR;
constexpr int L_CS = L_MISC;
constexpr int L_CSW = L_MISC + 2048;
constexpr int L_ZERO = L_MISC + 4096;
constexpr int L_ONES = L_MISC + 6144;
constexpr int LDS_BYTES = 163840;

constexpr size_t WS_EPOCH = 0, WS_TMO = 64;
constexpr size_t WS_SF = 4096;
constexpr size_t WS_KVF = 8192;
constexpr size_t WS_HF = 24576;
constexpr size_t WS_OF = 40960;
constexpr size_t WS_ST = 65536;
constexpr size_t WS_CS = 131072;
constexpr size_t WS_SCR = 768u << 10;
constexpr size_t WS_KV = 2u << 20;
constexpr size_t WS_C = 8u << 20;
constexpr int NKS = 118;

FI f32x16 mfma(bf16x8 a, bf16x8 b, f32x16 c) { return __builtin_amdgcn_mfma_f32_32x32x16_bf16(a, b, c, 0, 0, 0); }
FI f32x16 mfma_pin(bf16x8 a, bf16x8 b, const f32x16& c) { f32x16 d; asm volatile("v_mfma_f32_32x32x16_bf16 %0, %1, %2, %3" : "=&v"(d) : "v"(a), "v"(b), "v"(c)); return d; }
FI f32x16 mfma_pin0(bf16x8 a, bf16x8 b) { f32x16 d; asm volatile("v_mfma_f32_32x32x16_bf16 %0, %1, %2, 0" : "=&v"(d) : "v"(a), "v"(b)); return d; }
FI unsigned pk2(float lo, float hi) { f32x2 v = {lo, hi}; bf16x2_t r = __builtin_convertvector(v, bf16x2_t); return __builtin_bit_cast(unsigned, r); }
FI u32x4 pack8(const f32x16& y, int b) { u32x4 r; r[0] = pk2(y[b], y[b + 1]); r[1] = pk2(y[b + 2], y[b + 3]); r[2] = pk2(y[b + 4], y[b + 5]); r[3] = pk2(y[b + 6], y[b + 7]); return r; }
typedef short v4i16_t __attribute__((ext_vector_type(4)));
FI s16x4 trd(lcp p) { return __builtin_bit_cast(s16x4, __builtin_amdgcn_ds_read_tr16_b64_v4i16((LAS v4i16_t*)p)); }
template <int D2> FI bf16x8 trfrag(lcp p) { s16x4 lo = trd(p), hi = trd(p + D2); return __builtin_shufflevector(lo, hi, 0, 1, 2, 3, 4, 5, 6, 7); }
FI bf16x8 ldsf(lcp p) { return *(const LAS bf16x8*)p; }
FI f32x4 ldsf4(lcp p) { return *(const LAS f32x4*)p; }

FI void dma16(unsigned voff, const void* sbase, unsigned dst) {
    asm volatile("s_mov_b32 m0, %2\n\ts_nop 0\n\tglobal_load_lds_dwordx4 %0, %1" :: "v"(voff), "s"(sbase), "s"(__builtin_amdgcn_readfirstlane(dst)) : "memory", "m0");
}
FI void dma16_sc1(unsigned voff, const void* sbase, unsigned dst) {
    asm volatile("s_mov_b32 m0, %2\n\ts_nop 0\n\tglobal_load_lds_dwordx4 %0, %1 sc1" :: "v"(voff), "s"(sbase), "s"(__builtin_amdgcn_readfirstlane(dst)) : "memory", "m0");
}
FI void aload16(u32x4& v, const void* base, unsigned voff) { asm volatile("global_load_dwordx4 %0, %1, %2" : "=v"(v) : "v"(voff), "s"(base) : "memory"); }
FI void aload16_sc1(u32x4& v, const void* base, unsigned voff) { asm volatile("global_load_dwordx4 %0, %1, %2 sc1" : "=v"(v) : "v"(voff), "s"(base) : "memory"); }
FI void aload_flag(u64& v, const char* base, unsigned voff) { asm volatile("global_load_dwordx2 %0, %1, %2 sc1" : "=v"(v) : "v"(voff), "s"(base) : "memory"); }

FI void aprefetch(unsigned& d, const char* p) { asm volatile("global_load_dword %0, %1, off" : "+v"(d) : "v"(p) : "memory"); }
#define SB() __builtin_amdgcn_sched_barrier(0)
#define PIN(x) asm volatile("" : "+v"(x))
FI int opaque(int v) { asm volatile("" : "+v"(v)); return v; }
#define SYNC(N) do { SB(); asm volatile("s_waitcnt vmcnt(" #N ") lgkmcnt(0)\n\ts_barrier" ::: "memory"); SB(); } while (0)
#define SYNCV(N) do { SB(); asm volatile("s_waitcnt vmcnt(" #N ")\n\ts_barrier" ::: "memory"); SB(); } while (0)
#define WAITV(N) do { SB(); asm volatile("s_waitcnt vmcnt(" #N ")" ::: "memory"); SB(); } while (0)

struct Params {
    const u16* x; const u16* wqkv; const u16* wo; const u16* wg; const u16* wu; const u16* wd; u16* out; char* ws;
};

constexpr int wslot(int s) { return (s == 0 ? 4 : s == 1 ? 14 : s == 2 ? 7 : s == 3 ? 0 : s == 4 ? 7 : s == 5 ? 0 : s == 6 ? 10 : s == 7 ? 3 : s == 8 ? 6 : s == 9 ? 9 : s == 10 ? 0 : 3) * GR; }
constexpr int xslot(int c) { return (c == 0 ? 0 : c == 1 ? 2 : c == 2 ? 10 : c == 3 ? 12 : c == 4 ? 3 : c == 5 ? 5 : c == 6 ? 13 : 15) * GR; }
FI constexpr int dloc0(int u) { return (u == 0 ? 12 : u < 4 ? 6 + (u - 1) : u < 7 ? 9 + (u - 4) : u < 9 ? u - 7 : 2 + (u - 9)) * GR; }
FI constexpr int dloc(int u) { return u < 13 ? dloc0(u) : u < 19 ? L_KV + (u - 13) * GR : dloc0(u - 19); }

struct Ctx {
    const Params* p;
    unsigned lds0;
    lp lds;
    int lane, w, tm, g, rb, cg, tb, nb;
    unsigned vq, vf0, vf1, vf2, vx, vlin, vd;
    const char *px, *pq, *pf, *pwd, *pwo;
};

FI void issue_x1(const Ctx& c, int chunk, int k) {
    dma16(k ? c.vx ^ 64u : c.vx, c.px + chunk * 128 + k * 8 * 1024, c.lds0 + xslot(chunk) + 16 * c.w * 128 + k * 8 * 128);
}
FI void issue_x(const Ctx& c, int chunk) { issue_x1(c, chunk, 0); issue_x1(c, chunk, 1); }
template <int S> FI void issue_w1(const Ctx& c, int k) {
    if constexpr (S >= 4) {
        const char* base = c.pf + (size_t)(S - 4) * (64 * 2752);
        dma16(k == 0 ? c.vf0 : k == 1 ? c.vf1 : c.vf2, base, c.lds0 + wslot(S) + c.g * 12288 + c.tm * 3072 + k * 1024);
    } else {
        const char* base = c.pq + (size_t)S * (128 * 3072);
        dma16(c.vq, base + (k == 0 ? 1024 : k == 1 ? 2048 : 0), c.lds0 + wslot(S) + c.w * 1024 + k * 8192);
    }
}
template <int S> FI void issue_w(const Ctx& c) { issue_w1<S>(c, 0); issue_w1<S>(c, 1); issue_w1<S>(c, 2); }
FI void issue_d(const Ctx& c, int u) {
    const char* base;
    if (u < 21) base = c.pwd + (size_t)u * 65536;
    else if (u == 21) base = c.pwd + (size_t)21 * 65536 - (c.w >= 4 ? 32768 : 0);
    else base = c.pwo + (size_t)(u - 22) * 65536;
    dma16(c.vd, base, c.lds0 + dloc(u) + c.w * 1024);
}

FI float blo(unsigned d) { return __uint_as_float(d << 16); }
FI float bhi(unsigned d) { return __uint_as_float(d & 0xffff0000u); }
template <int CTRL> FI float dppadd(float x) { return x + __builtin_bit_cast(float, __builtin_amdgcn_update_dpp(0, __builtin_bit_cast(int, x), CTRL, 0xf, 0xf, true)); }
FI void swap32add(float& u, float v) { auto rr = __builtin_amdgcn_permlane32_swap(__float_as_uint(u), __float_as_uint(v), false, false); u = __uint_as_float(rr[0]) + __uint_as_float(rr[1]); }
FI void swap16add(float& u, float v) { auto rr = __builtin_amdgcn_permlane16_swap(__float_as_uint(u), __float_as_uint(v), false, false); u = __uint_as_float(rr[0]) + __uint_as_float(rr[1]); }

struct G1State {
    bf16x8 xf[32];
    bf16x8 wq[4];
    f32x16 a0, a1, b0, b1;
    f32x16 qa, qb;
    unsigned wbf[4], wbq[4];
    unsigned xb[4];
};
constexpr int fkind(int G, int f) { return G == 0 ? (f < 8 ? (f & 3) : (f & 1)) : (f < 4 ? (f & 1) : f < 6 ? (f & 1) : f < 8 ? 2 + (f & 1) : f < 10 ? (f & 1) : 2 + (f & 1)); }
constexpr int fpos(int G, int f) { return G == 0 ? (f < 8 ? (f >> 2) : 2 + ((f - 8) >> 1)) : (f < 4 ? (f >> 1) : f < 8 ? 2 : 3); }
constexpr int qkind(int G, int f) { return G == 0 ? ((f == 1 || f == 3 || f == 7 || f == 9) ? 1 : 0) : ((f == 3 || f == 5 || f == 9 || f == 11) ? 1 : 0); }
constexpr int qpos(int G, int f) {
    return G == 0 ? (f == 0 ? 0 : f == 1 ? 0 : f == 2 ? 1 : f == 3 ? 1 : f == 4 ? 2 : f == 5 ? 3 : f == 6 ? 4 : f == 7 ? 4 : f == 8 ? 5 : f == 9 ? 5 : f == 10 ? 6 : 7)
                  : (f == 0 ? 0 : f == 1 ? 1 : f == 2 ? 2 : f == 3 ? 2 : f == 4 ? 3 : f == 5 ? 3 : f == 6 ? 4 : f == 7 ? 5 : f == 8 ? 6 : f == 9 ? 6 : f == 10 ? 7 : 7);
}
template <int G, int S> FI bf16x8 rdfrag(const G1State& st, int f) {
    if constexpr (S >= 4) { const int k = fkind(G, f); return trfrag<768>((lcp)(uintptr_t)(st.wbf[fpos(G, f)] + (k & 1) * 12288 + (k >= 2 ? 64 : (G == 0 ? 0 : 128)))); }
    else { const int e = qpos(G, f); return trfrag<256>((lcp)(uintptr_t)(st.wbq[e & 3] + (e >> 2) * 4096 + (qkind(G, f) ? 16384 : G * 8192))); }
}
template <int C> FI void rdx(G1State& st) {
#pragma unroll
    for (int e = 0; e < 4; ++e) { st.xf[4 * C + e] = ldsf((lcp)(uintptr_t)st.xb[e]); if (C < 7) st.xb[e] = (unsigned)opaque((int)(st.xb[e] + (xslot(C + 1) - xslot(C)))); }
}
template <int G, int S, bool FRESH, bool XF, int XP, int NX, class F>
FI void stage(G1State& st, F&& hook) {
    if constexpr (XF) rdx<(XF ? 2 * S : 0)>(st);
    if constexpr (FRESH) {
#pragma unroll
        for (int f = 0; f < 4; ++f) st.wq[f] = rdfrag<G, S>(st, f);
    }
    if constexpr (XF) rdx<(XF ? 2 * S + 1 : 0)>(st);
    SB();
#pragma unroll
    for (int f = 0; f < 12; ++f) {
        const bf16x8 w = st.wq[f & 3];
        if constexpr (S >= 4) {
            const int k = fkind(G, f); const bf16x8 x = st.xf[4 * (S - 4) + fpos(G, f)];
            if (k == 0) st.a0 = mfma(w, x, st.a0); else if (k == 1) st.a1 = mfma(w, x, st.a1); else if (k == 2) st.b0 = mfma(w, x, st.b0); else st.b1 = mfma(w, x, st.b1);
        } else {
            const bf16x8 x = st.xf[8 * S + qpos(G, f)];
            if (qkind(G, f)) st.qb = mfma(w, x, st.qb); else if (G == 0) st.qa = mfma(w, x, st.qa); else st.qa = mfma(x, w, st.qa);
        }
        SB();
        if (f < 8) st.wq[f & 3] = rdfrag<G, S>(st, f + 4);
        else {
            if (f == 8) {
                if constexpr (S < 3) { _Pragma("unroll") for (int j = 0; j < 4; ++j) st.wbq[j] = (unsigned)opaque((int)(st.wbq[j] + (wslot(S + 1) - wslot(S)))); }
                if constexpr (S >= 4 && S < 11) { _Pragma("unroll") for (int j = 0; j < 4; ++j) st.wbf[j] = (unsigned)opaque((int)(st.wbf[j] + (wslot(S + 1) - wslot(S)))); }
            }
            if constexpr (NX >= 0) st.wq[f & 3] = rdfrag<G, (NX >= 0 ? NX : 0)>(st, f - 8);
        }
        if constexpr (XP >= 0) { if (f == 2) rdx<(XP >= 0 ? XP : 0)>(st); if (f == 6) rdx<(XP >= 0 ? XP + 1 : 0)>(st); }
        hook(f);
        SB();
    }
}

FI u64 mix64(u64 z) { z += 0x9E3779B97F4A7C15ull; z = (z ^ (z >> 30)) * 0xBF58476D1CE4E5B9ull; z = (z ^ (z >> 27)) * 0x94D049BB133111EBull; return z ^ (z >> 31); }

constexpr unsigned SPIN_CAP = 1u << 16;
FI void set_timeout(char* ws, int lane) { if (lane == 0) __hip_atomic_store((GAS unsigned*)(ws + WS_TMO), 1u, __ATOMIC_RELAXED, __HIP_MEMORY_SCOPE_AGENT); }
FI void poll_flags(const u64* flags, int n, u64 tag, int lane, char* ws) {
    const GAS u64* pp = (const GAS u64*)flags + (lane < n ? lane : 0);
    for (unsigned spins = 0;; ++spins) {
        const u64 v = __hip_atomic_load(pp, __ATOMIC_RELAXED, __HIP_MEMORY_SCOPE_AGENT);
        if (__all(v == tag)) return;
        if (spins > SPIN_CAP) { set_timeout(ws, lane); return; }
        __builtin_amdgcn_s_sleep(2);
    }
}
FI void store_flag(char* ws, size_t off, u64 tag) { __hip_atomic_store((GAS u64*)(ws + off), tag, __ATOMIC_RELAXED, __HIP_MEMORY_SCOPE_AGENT); }

__global__ __launch_bounds__(512, 2) void fused_kernel(Params P) {
    extern __shared__ __attribute__((aligned(16))) char smem[];
    Ctx c; c.p = &P;
    const int tid = threadIdx.x; c.lane = tid & 63;
    const int w = __builtin_amdgcn_readfirstlane(tid >> 6);
    c.w = w; c.tm = w & 3; c.g = w >> 2;
    c.lds = (lp)smem; c.lds0 = (unsigned)(uintptr_t)c.lds;
    const int xcd = blockIdx.x & 7, idx = blockIdx.x >> 3;
    c.rb = 8 * (xcd >> 2) + (idx >> 2); c.cg = 4 * (xcd & 3) + (idx & 3);
    c.tb = 8 * (xcd >> 1) + (idx >> 2); c.nb = 4 * (xcd & 1) + (idx & 3);
    const int fs = c.cg < 11 ? 3 * c.cg : c.cg < 15 ? 33 + 2 * (c.cg - 11) : 40;
    const bool v0 = c.cg != 15, v2 = c.cg < 11 || c.cg == 15;
    const int lane = c.lane, h = lane >> 5, r32 = lane & 31;
    char* ws = P.ws;
    c.px = (const char*)P.x + (size_t)(128 * c.rb + 16 * w) * 1024;
    c.pf = (const char*)(c.g ? P.wu : P.wg) + (size_t)(16 * c.tm) * 2752 + fs * 64;
    u64 tag = 0; unsigned tagLo = 0, tagHi = 0; float ln_a = 0.f, ln_m = 0.f;
    const int T = 4 * c.rb + c.tm;
    const __amdgpu_buffer_rsrc_t wsr = __builtin_amdgcn_make_buffer_rsrc(ws, 0, 32u << 20, 0x00020000);
    c.pq = (const char*)P.wqkv + ((size_t)(16 * w) * 1536 + 32 * c.cg) * 2;
    c.pwd = (const char*)P.wd + (size_t)(8 * w) * 1024 + c.nb * 128;
    c.pwo = (const char*)P.wo + (size_t)(8 * w) * 1024 + c.nb * 128;
    u32x4 g0a, g0b;
#define GATHER_OFFS(tile) const int gl_ = opaque(lane); const unsigned go0 = (unsigned)(WS_CS + (c.cg * 9 + (tile)) * 2048 + (gl_ >> 3) * 256 + (gl_ & 7) * 32)
#define GATHER_ISSUE(tile) do { GATHER_OFFS(tile); aload16_sc1(g0a, P.ws, go0); aload16_sc1(g0b, P.ws + 16, go0); } while (0)
#define GATHER_OK() (g0a[1] == tagLo && g0a[3] == tagLo && g0b[1] == tagLo && g0b[3] == tagLo)
#define GATHER_RED(tile) do { const int gl_ = opaque(lane); float v0_ = __uint_as_float(g0a[0]), v1_ = __uint_as_float(g0a[2]), v2_ = __uint_as_float(g0b[0]), v3_ = __uint_as_float(g0b[2]); \
        swap32add(v0_, v2_); swap32add(v1_, v3_); swap16add(v0_, v1_); v0_ = dppadd<0x128>(v0_); \
        if ((gl_ & 8) == 0) *(LAS float*)(c.lds + L_CS + (tile) * 128 + (4 * (gl_ & 7) + 2 * (gl_ >> 5) + ((gl_ >> 4) & 1)) * 4) = v0_; } while (0)
#define GATHER_CHECK(tile) do { asm volatile("" : "+v"(g0a), "+v"(g0b)); \
        if (!__all(GATHER_OK())) { GATHER_OFFS(tile); \
            for (unsigned spins = 0;; ++spins) { __builtin_amdgcn_s_sleep(2); \
                g0a = __builtin_amdgcn_raw_buffer_load_b128(wsr, go0, 0, 16); g0b = __builtin_amdgcn_raw_buffer_load_b128(wsr, go0 + 16, 0, 16); __builtin_amdgcn_s_waitcnt(0x0F70); \
                if (__all(GATHER_OK())) break; if (spins > SPIN_CAP) { set_timeout(ws, lane); break; } } } \
        } while (0)
#define F4(v, b) (f32x4){v[b], v[(b) + 1], v[(b) + 2], v[(b) + 3]}

    const int th = w & 1, kq = w >> 1;
    const int T2 = 2 * c.tb + th;
    bf16x8 qf; bool edone_ = false;
    auto gemm1 = [&](auto Gc) {
        constexpr int G = decltype(Gc)::value;
        const int lane = opaque(c.lane), h = lane >> 5, r32 = lane & 31, tid = w * 64 + lane;
        c.vq = (lane >> 2) * 3072 + (lane & 3) * 16;
        c.vx = (lane >> 3) * 1024 + (((lane & 7) ^ (lane >> 4)) * 16); c.vlin = lane * 16;
        G1State st;
        issue_x(c, 0); issue_x(c, 1); issue_w<0>(c);
        u64 ep; aload_flag(ep, ws + WS_EPOCH, 0u);
        u32x4 ax; aload16(ax, (const char*)P.x + (size_t)(128 * c.rb + 8 * c.cg + w) * 1024, c.vlin);
        u32x4 aw0, aw1, aw2, aw3;
        const int ptile = 2 * w + (c.rb & 1); const bool pvalid = ptile <= 8;
        aw0 = aw1 = aw2 = aw3 = (u32x4){0u, 0u, 0u, 0u};
        if (pvalid) {
            const char* tb; unsigned pitch, vo;
            if (ptile < 3) { tb = (const char*)P.wqkv + (ptile == 0 ? 512 : ptile == 1 ? 1024 : 0) * 2 + 64 * c.cg; pitch = 3072; vo = c.vq; }
            else { tb = (const char*)((ptile & 1) ? P.wg : P.wu) + 64 * (fs + ((ptile - 3) >> 1)); pitch = 2752; vo = (lane >> 2) * 2752 + (lane & 3) * 16; }
            tb += (size_t)(64 * (c.rb >> 1)) * pitch;
            aload16(aw0, tb, vo); aload16(aw1, tb + 16 * pitch, vo); aload16(aw2, tb + 32 * pitch, vo); aload16(aw3, tb + 48 * pitch, vo);
        }
        c.vf0 = (lane / 12) * 2752 + (lane % 12) * 16; c.vf1 = ((lane + 64) / 12) * 2752 + ((lane + 64) % 12) * 16; c.vf2 = ((lane + 128) / 12) * 2752 + ((lane + 128) % 12) * 16;
        issue_w<4>(c);
        *(LAS unsigned*)(c.lds + L_ZERO + tid * 4) = 0u;
        if (tid < 4) { *(LAS unsigned*)(c.lds + L_ONES + tid * 4) = 0x3f803f80u; *(LAS unsigned*)(c.lds + L_ONES + 1024 + tid * 4) = 0x3f803f80u; }
        { const unsigned lb = c.lds0 + (8 * h + ((lane & 15) >> 2)) * 64 + 32 * ((lane >> 4) & 1) + 8 * (lane & 3);
#pragma unroll
          for (int j = 0; j < 4; ++j) st.wbq[j] = lb + ((j ^ c.tm) * 1024) + wslot(0); }
        { const unsigned lb = c.lds0 + (8 * h + ((lane & 15) >> 2)) * 192 + 32 * ((lane >> 4) & 1) + 8 * (lane & 3);
#pragma unroll
          for (int j = 0; j < 4; ++j) st.wbf[j] = lb + ((j ^ c.tm) * 3072) + wslot(4); }
        { const int row = 32 * c.tm + r32;
#pragma unroll
          for (int j = 0; j < 4; ++j) st.xb[j] = c.lds0 + xslot(0) + row * 128 + (((2 * (j ^ c.tm) + h) ^ ((row >> 1) & 7)) * 16); }
        st.a0 = f32x16{}; st.a1 = f32x16{}; st.b0 = f32x16{}; st.b1 = f32x16{}; st.qa = f32x16{}; st.qb = f32x16{};

        SYNC(3);
        asm volatile("" : "+v"(ep), "+v"(ax), "+v"(aw0), "+v"(aw1), "+v"(aw2), "+v"(aw3));
        tag = mix64(((u64)(unsigned)__builtin_amdgcn_readfirstlane((int)(unsigned)(ep >> 32)) << 32) | (unsigned)__builtin_amdgcn_readfirstlane((int)(unsigned)ep));
        tagLo = (unsigned)tag; tagHi = (unsigned)(tag >> 32);

        float as_ = 0.f, aq_ = 0.f, av[8];
        auto aux0 = [&](int f) {
            if (f < 4) { const float a = blo(ax[f]), b = bhi(ax[f]); as_ += a + b; aq_ = __builtin_fmaf(a, a, aq_); aq_ = __builtin_fmaf(b, b, aq_); PIN(as_); PIN(aq_); }
            if (f == 4) { as_ = dppadd<0x121>(as_); aq_ = dppadd<0x121>(aq_); as_ = dppadd<0x122>(as_); aq_ = dppadd<0x122>(aq_); PIN(as_); PIN(aq_); }
            if (f == 5) { as_ = dppadd<0x124>(as_); aq_ = dppadd<0x124>(aq_); as_ = dppadd<0x128>(as_); aq_ = dppadd<0x128>(aq_); PIN(as_); PIN(aq_); }
            if (f == 6) { swap16add(as_, as_); swap16add(aq_, aq_); swap32add(as_, as_); swap32add(aq_, aq_); PIN(as_); PIN(aq_); }
            if (f == 8) { const float mean = as_ * (1.f / 512.f), var = aq_ * (1.f / 512.f) - mean * mean, rstd = __builtin_amdgcn_rsqf(fmaxf(var, 0.f) + 1e-5f);
                if (lane == 0) __builtin_amdgcn_raw_buffer_store_b128((u32x4){__float_as_uint(mean), tagLo, __float_as_uint(rstd), tagHi}, wsr, (unsigned)(WS_ST + (128 * c.rb + 8 * c.cg + w) * 16), 0, 16); }
        };
        auto aux1 = [&](int f) {
            if (f < 4) { const int i = f; av[2 * i] = (blo(aw0[i]) + blo(aw1[i])) + (blo(aw2[i]) + blo(aw3[i])); av[2 * i + 1] = (bhi(aw0[i]) + bhi(aw1[i])) + (bhi(aw2[i]) + bhi(aw3[i])); PIN(av[2 * i]); PIN(av[2 * i + 1]); }
            if (f == 8) { swap32add(av[0], av[4]); swap32add(av[1], av[5]); swap32add(av[2], av[6]); swap32add(av[3], av[7]); PIN(av[0]); PIN(av[1]); PIN(av[2]); PIN(av[3]); }
            if (f == 9) { swap16add(av[0], av[2]); swap16add(av[1], av[3]); av[0] = dppadd<0x124>(av[0]); av[1] = dppadd<0x124>(av[1]); av[0] = dppadd<0x128>(av[0]); av[1] = dppadd<0x128>(av[1]); PIN(av[0]); PIN(av[1]); }
            if (f == 10) { const int rho = lane >> 4, col = 8 * (lane & 3) + 4 * (rho >> 1) + 2 * (rho & 1);
                const unsigned off = pvalid ? (unsigned)(WS_CS + (((c.cg * 9 + ptile) * 8 + (c.rb >> 1)) * 32 + col) * 8) : (unsigned)(WS_SCR + (blockIdx.x * 8 + w) * 256 + col * 8);
                if ((lane & 12) == 0) __builtin_amdgcn_raw_buffer_store_b128((u32x4){__float_as_uint(av[0]), tagLo, __float_as_uint(av[1]), tagLo}, wsr, off, 0, 16); }
        };
        u32x4 gs;
        const int gt1 = w < 3 ? w : w < 6 ? w - 3 : w - 6, gt2 = w < 6 ? 3 + w : w - 3;
        stage<G, 0, true, true, -1, -1>(st, [&](int f_) { if (f_ < 2) issue_x1(c, 2, f_); else if (f_ < 4) issue_x1(c, 3, f_ - 2); else if (f_ < 7) issue_w1<1>(c, f_ - 4); aux0(f_); }); SYNCV(8);
        stage<G, 4, true, false, -1, -1>(st, [&](int f_) { if (f_ < 3) issue_w1<5>(c, f_); else if (f_ < 5) issue_x1(c, 4, f_ - 3); else if (f_ < 7) issue_x1(c, 5, f_ - 5); aux1(f_); }); SYNCV(9);
        stage<G, 1, true, true, -1, -1>(st, [&](int f_) { if (f_ >= 1 && f_ < 4) issue_w1<2>(c, f_ - 1); }); SYNCV(4);
        stage<G, 5, true, false, 4, -1>(st, [&](int f_) { if (f_ < 3) issue_w1<6>(c, f_); else if (f_ < 5) issue_x1(c, 6, f_ - 3); else if (f_ < 7) issue_x1(c, 7, f_ - 5); }); SYNCV(7);
        stage<G, 2, true, false, -1, -1>(st, [&](int f_) { if (f_ >= 1 && f_ < 4) issue_w1<3>(c, f_ - 1);
            if (f_ == 8) { aload16_sc1(gs, P.ws, (unsigned)(WS_ST + (128 * c.rb + 32 * c.tm + (opaque(lane) & 31)) * 16)); GATHER_ISSUE(gt1); } }); SYNCV(6);
        stage<G, 6, true, false, 6, -1>(st, [&](int f_) { if (f_ >= 1 && f_ < 4) issue_w1<7>(c, f_ - 1); else if (f_ >= 4 && f_ < 7) issue_w1<8>(c, f_ - 4); }); SYNCV(9);
        stage<G, 3, true, false, -1, -1>(st, [&](int f_) { if (f_ >= 1 && f_ < 4) issue_w1<9>(c, f_ - 1);
            if (f_ == 10) { WAITV(9); PIN(gs);
                if (!__all(gs[1] == tagLo && gs[3] == tagHi)) { const unsigned gso = (unsigned)(WS_ST + (128 * c.rb + 32 * c.tm + (opaque(lane) & 31)) * 16);
                    for (unsigned spins = 0;; ++spins) { __builtin_amdgcn_s_sleep(2); gs = __builtin_amdgcn_raw_buffer_load_b128(wsr, gso, 0, 16); __builtin_amdgcn_s_waitcnt(0x0F70);
                        if (__all(gs[1] == tagLo && gs[3] == tagHi)) break; if (spins > SPIN_CAP) { set_timeout(ws, lane); break; } } }
                ln_m = __uint_as_float(gs[0]); ln_a = __uint_as_float(gs[2]); PIN(ln_m); PIN(ln_a);
                GATHER_CHECK(gt1); }
            if (f_ == 11) GATHER_RED(gt1); });
        { lp xp = c.lds + 13 * GR + w * 2048 + c.vlin; *(LAS f32x4*)(xp) = F4(st.qb, 8 - 8 * G); *(LAS f32x4*)(xp + 1024) = F4(st.qb, 12 - 8 * G);
          if constexpr (G == 1) { if (h == 0) { *(LAS float*)(c.lds + L_CSW + c.tm * 256 + r32 * 4) = ln_m; *(LAS float*)(c.lds + L_CSW + c.tm * 256 + 128 + r32 * 4) = ln_a; } } }
        SYNC(3);
        issue_w<10>(c);
        { const int lane = opaque(c.lane), h = lane >> 5, r32 = lane & 31;
          const unsigned o = (unsigned)(WS_KV + ((size_t)c.cg * 64 + T) * 4096) + lane * 16 + G * 2048;
          f32x16 y;
          if constexpr (G == 0) {
#pragma unroll
              for (int q = 0; q < 4; ++q) { const f32x4 cs = ldsf4((lcp)c.lds + L_CS + (8 * q + 4 * h) * 4);
#pragma unroll
                  for (int i = 0; i < 4; ++i) y[4 * q + i] = ln_a * __builtin_fmaf(-ln_m, cs[i], st.qa[4 * q + i]); }
          } else {
              const float cso = *(const LAS float*)(c.lds + L_CS + 128 + r32 * 4);
#pragma unroll
              for (int q = 0; q < 4; ++q) { const f32x4 mm = ldsf4((lcp)c.lds + L_CSW + c.tm * 256 + (8 * q + 4 * h) * 4), aa = ldsf4((lcp)c.lds + L_CSW + c.tm * 256 + 128 + (8 * q + 4 * h) * 4);
#pragma unroll
                  for (int i = 0; i < 4; ++i) y[4 * q + i] = aa[i] * __builtin_fmaf(-mm[i], cso, st.qa[4 * q + i]); }
          }
          const u32x4 k0 = pack8(y, 0), k1 = pack8(y, 8);
          __builtin_amdgcn_raw_buffer_store_b128(k0, wsr, o, 0, 16); __builtin_amdgcn_raw_buffer_store_b128(k1, wsr, o + 1024, 0, 16);
          if (w == 0 && lane == 0) store_flag(ws, WS_SF + (size_t)blockIdx.x * 8, tag);
          *(LAS u32x4*)(c.lds + L_KVR + c.tm * 4096 + G * 2048 + lane * 16) = k0; *(LAS u32x4*)(c.lds + L_KVR + c.tm * 4096 + G * 2048 + 1024 + lane * 16) = k1;
          SB();
          const f32x4 c0 = ldsf4((lcp)c.lds + L_CS + 2 * 128 + (16 * G + 4 * h) * 4), c1 = ldsf4((lcp)c.lds + L_CS + 2 * 128 + (16 * G + 8 + 4 * h) * 4);
          const lcp xp = (lcp)c.lds + 13 * GR + (w ^ 4) * 2048 + lane * 16;
          const f32x4 p0 = ldsf4(xp), p1 = ldsf4(xp + 1024);
          const float sc = ln_a * (0.25f * 1.4426950408889634f);
#pragma unroll
          for (int i = 0; i < 4; ++i) { y[i] = sc * __builtin_fmaf(-ln_m, c0[i], st.qb[8 * G + i] + p0[i]); y[4 + i] = sc * __builtin_fmaf(-ln_m, c1[i], st.qb[8 * G + 4 + i] + p1[i]); }
          qf = __builtin_bit_cast(bf16x8, pack8(y, 0)); PIN(qf); }
        stage<G, 7, true, false, -1, 8>(st, [&](int) {}); SYNC(5);
        { const int ln = opaque(lane); c.vd = (ln >> 3) * 1024 + (((ln & 7) ^ (((ln >> 4) & 1) << 2)) * 16); }
        stage<G, 8, false, false, -1, 9>(st, [&](int f_) { if (f_ >= 1 && f_ < 4) issue_w1<11>(c, f_ - 1); if (f_ == 4) issue_d(c, 0); }); SYNCV(6);
        stage<G, 9, false, false, -1, 10>(st, [&](int f_) { if (f_ >= 1 && f_ < 4) issue_d(c, f_);
            if (f_ == 5) { WAITV(7); if (lane == 0) store_flag(ws, WS_KVF + ((size_t)(c.cg * 64 + T) * 2 + G) * 8, tag); }
            if (f_ == 8) GATHER_ISSUE(gt2); }); SYNCV(7);
        u64 kf_;
        stage<G, 10, false, false, -1, 11>(st, [&](int f_) { if (f_ >= 1 && f_ < 4) issue_d(c, 3 + f_);
            if (f_ == 6) { WAITV(3); GATHER_CHECK(gt2); GATHER_RED(gt2); }
            if (f_ == 10) { const int tl = 4 * c.rb - 8 + w; aload_flag(kf_, P.ws, (unsigned)(WS_KVF + ((c.cg * 64 + (tl < 0 ? 0 : tl)) * 2 + (opaque(lane) & 1)) * 8)); } }); SYNCV(63);
        stage<G, 11, false, false, -1, -1>(st, [&](int f_) { if (f_ >= 1 && f_ < 3) issue_d(c, 6 + f_);
            if (f_ == 6) { WAITV(2); asm volatile("" : "+v"(kf_)); const int tl = 4 * c.rb - 8 + w;
                if (tl >= 0) {
                    if (!__all(kf_ == tag)) poll_flags((const u64*)(ws + WS_KVF) + (c.cg * 64 + tl) * 2, 2, tag, lane, ws);
                    const char* base = ws + WS_KV + ((size_t)c.cg * 64 + tl) * 4096; const unsigned dst = c.lds0 + L_KV + w * 4096;
                    dma16_sc1(c.vlin, base, dst); dma16_sc1(c.vlin, base + 1024, dst + 1024); dma16_sc1(c.vlin, base + 2048, dst + 2048); dma16_sc1(c.vlin, base + 3072, dst + 3072); } } });
        {
          lp xp = c.lds + 2 * GR + w * 4096 + c.vlin;
          SYNCV(63);
          *(LAS f32x4*)(xp) = F4(st.b0, 8 - 8 * G); *(LAS f32x4*)(xp + 1024) = F4(st.b0, 12 - 8 * G); *(LAS f32x4*)(xp + 2048) = F4(st.b1, 8 - 8 * G); *(LAS f32x4*)(xp + 3072) = F4(st.b1, 12 - 8 * G); }
        SYNC(63);
        {
          const int lane = opaque(c.lane), h = lane >> 5;
          const float a2 = ln_a * ln_a;
          { u32x4 y0, y1;
#pragma unroll
            for (int q = 0; q < 4; ++q) {
                const f32x4 cg_ = ldsf4((lcp)c.lds + L_CS + (3 + 4 * G) * 128 + (8 * q + 4 * h) * 4), cu_ = ldsf4((lcp)c.lds + L_CS + (4 + 4 * G) * 128 + (8 * q + 4 * h) * 4);
                float y[4];
#pragma unroll
                for (int i = 0; i < 4; ++i) { const float g_ = fmaxf(__builtin_fmaf(-ln_m, cg_[i], st.a0[4 * q + i]), 0.f), u_ = __builtin_fmaf(-ln_m, cu_[i], st.a1[4 * q + i]); y[i] = (g_ * a2) * u_; }
                const unsigned lo = pk2(y[0], y[1]), hi = pk2(y[2], y[3]);
                if (q < 2) { y0[2 * q] = lo; y0[2 * q + 1] = hi; } else { y1[2 * q - 4] = lo; y1[2 * q - 3] = hi; }
            }
            if (G == 0 ? v0 : v2) { const unsigned o_ = (unsigned)(WS_C + ((size_t)T * NKS + 2 * (fs + 2 * G)) * 1024) + lane * 16;
                __builtin_amdgcn_raw_buffer_store_b128(y0, wsr, o_, 0, 16); __builtin_amdgcn_raw_buffer_store_b128(y1, wsr, o_ + 1024, 0, 16); } }
          { const lcp xp = (lcp)c.lds + 2 * GR + (w ^ 4) * 4096 + lane * 16;
            u32x4 y0;
#pragma unroll
            for (int q = 0; q < 2; ++q) {
                const f32x4 pg = ldsf4(xp + q * 1024), pu = ldsf4(xp + 2048 + q * 1024);
                const f32x4 cg_ = ldsf4((lcp)c.lds + L_CS + 5 * 128 + (16 * G + 8 * q + 4 * h) * 4), cu_ = ldsf4((lcp)c.lds + L_CS + 6 * 128 + (16 * G + 8 * q + 4 * h) * 4);
                float y[4];
#pragma unroll
                for (int i = 0; i < 4; ++i) { const float g_ = fmaxf(__builtin_fmaf(-ln_m, cg_[i], st.b0[8 * G + 4 * q + i] + pg[i]), 0.f), u_ = __builtin_fmaf(-ln_m, cu_[i], st.b1[8 * G + 4 * q + i] + pu[i]); y[i] = (g_ * a2) * u_; }
                y0[2 * q] = pk2(y[0], y[1]); y0[2 * q + 1] = pk2(y[2], y[3]);
            }
            __builtin_amdgcn_raw_buffer_store_b128(y0, wsr, (unsigned)(WS_C + ((size_t)T * NKS + 2 * (fs + 1) + G) * 1024) + lane * 16, 0, 16); } }
        if (G == 0 ? v0 : v2) { SYNC(3); } else { SYNC(1); }
        if constexpr (G == 1) __builtin_amdgcn_s_setprio(1);
    };
    if (c.g == 0) gemm1(std::integral_constant<int, 0>{}); else gemm1(std::integral_constant<int, 1>{});
    const int lane2 = opaque(lane);
    const unsigned cbase = (unsigned)(WS_C + ((size_t)T2 * NKS + kq) * 1024) + lane2 * 16;
    bf16x8 hf[22];
    u64 hfl; bool hrdy_ = false;
#define LDH(i) do { const bool ok_ = ((i) < 21) || (kq < 2); hf[i] = __builtin_bit_cast(bf16x8, __builtin_amdgcn_raw_buffer_load_b128(wsr, ok_ ? cbase + (i) * 4096 : cbase, 0, 16)); } while (0)
    {
        const int lane = lane2, h = lane >> 5, r32 = lane & 31;
        const int i0 = T >= 8 ? 0 : 8 - T;
        const unsigned kb = c.lds0 + L_KV + c.tm * 4096 + c.g * 1024 + lane * 16;
        const unsigned lanebit = r32 < 16 ? 1u : 0u;
        const unsigned vb = c.lds0 + (r32 < 16 ? (unsigned)(((r32 & 15) + 16 * c.g + 32 * h) * 16) : (unsigned)L_ONES);
        auto koff = [&](int i) -> unsigned { return (unsigned)((i >= i0 ? i : 7) * 4096); };
        auto rdk = [&](int i) -> bf16x8 { return ldsf((lcp)(uintptr_t)(kb + koff(i))); };
        auto vaddr = [&](int i) -> unsigned { const bool valid = i >= i0; const unsigned d = valid ? 0u : (unsigned)(L_ZERO - L_ONES), A = valid ? (unsigned)(L_KV + 2048 + (c.tm + i) * 4096) : (unsigned)L_ZERO;
                                              return lanebit * (A - d) + (vb + d); };
        auto dmask = [&](int r) -> bool { u64 mk = 0;
#pragma unroll
            for (int l = 0; l < 64; ++l) { const int kl = (r & 3) + 8 * (r >> 2) + 4 * (l >> 5); if (kl <= (l & 31)) mk |= 1ull << l; }
            return __builtin_amdgcn_inverse_ballot_w64(mk); };
        const f32x16 zero = f32x16{};
        float m0 = -1e30f, m1 = -1e30f;
#define MAXT(S) do { _Pragma("unroll") for (int r = 0; r < 16; r += 4) { m0 = fmaxf(fmaxf(m0, S[r]), S[r + 1]); m1 = fmaxf(fmaxf(m1, S[r + 2]), S[r + 3]); } PIN(m0); PIN(m1); } while (0)
#define EXPT(S) do { _Pragma("unroll") for (int r = 0; r < 16; ++r) S[r] = __builtin_amdgcn_exp2f(S[r]); } while (0)
#define H_PUBLISH(N) do { WAITV(N); if (lane == 0) store_flag(ws, WS_HF + ((size_t)T * 32 + 2 * c.cg + c.g) * 8, tag); } while (0)
#define H_FLAGS() aload_flag(hfl, P.ws, (unsigned)(WS_HF + (T2 * 32 + (lane & 31)) * 8))
        f32x16 o = f32x16{};
        float m; bool hrdy = false;
        if (T == 0) {
            f32x16 s = mfma(ldsf((lcp)(uintptr_t)(kb + 8 * 4096)), qf, zero);
#pragma unroll
            for (int r = 0; r < 16; ++r) s[r] = dmask(r) ? s[r] : -1e30f;
            MAXT(s);
            m = fmaxf(m0, m1);
            { auto rr = __builtin_amdgcn_permlane32_swap(__float_as_uint(m), __float_as_uint(m), false, false); m = fmaxf(__uint_as_float(rr[0]), __uint_as_float(rr[1])); }
#pragma unroll
            for (int r = 0; r < 16; ++r) s[r] = __builtin_amdgcn_exp2f(s[r] - m);
            const unsigned va = vaddr(8);
            o = mfma(ldsf((lcp)(uintptr_t)va), __builtin_bit_cast(bf16x8, pack8(s, 0)), o);
            o = mfma(ldsf((lcp)(uintptr_t)(va + 1024)), __builtin_bit_cast(bf16x8, pack8(s, 8)), o);
            issue_d(c, 9); issue_d(c, 10); issue_d(c, 11); issue_d(c, 12);
            H_PUBLISH(4);
        } else {
            bf16x8 k0 = rdk(0), k8 = rdk(8), k1 = rdk(1), k2 = rdk(2), k3 = rdk(3);
            f32x16 sA, sB, sC;
            sA = mfma_pin0(k0, qf); sB = mfma_pin0(k8, qf); SB();
            k0 = rdk(4); k8 = rdk(5); sC = mfma_pin0(k1, qf); k1 = rdk(6); SB();
            asm volatile("s_nop 7\n\ts_nop 3"); SB();
#pragma unroll
            for (int r = 0; r < 16; ++r) sA[r] = dmask(r) ? sB[r] : sA[r];
            PIN(sA); SB();
            sB = mfma_pin0(k2, qf); k2 = rdk(7); SB();
            MAXT(sA); SB();
            H_PUBLISH(0);
            unsigned pfw = 0;
            if (w < 3) { const int id = w * 64 + lane; int row = 832 + 8 * id + (idx >> 2); if (row > 1887) row = 1887;
                aprefetch(pfw, (row < 1376 ? (const char*)P.wd + (size_t)row * 1024 : (const char*)P.wo + (size_t)(row - 1376) * 1024) + c.nb * 128); }
            sA = mfma_pin0(k3, qf); issue_d(c, 9); SB(); MAXT(sC); SB();
            sC = mfma_pin0(k0, qf); issue_d(c, 10); SB(); MAXT(sB); SB();
            sB = mfma_pin0(k8, qf); issue_d(c, 11); SB(); MAXT(sA); SB();
            sA = mfma_pin0(k1, qf); issue_d(c, 12); SB(); MAXT(sC); SB();
            sC = mfma_pin0(k2, qf); k0 = rdk(0); k8 = rdk(8); k1 = rdk(1); SB(); MAXT(sB); SB();
            MAXT(sA); SB(); MAXT(sC);
            m = fmaxf(m0, m1);
            { auto rr = __builtin_amdgcn_permlane32_swap(__float_as_uint(m), __float_as_uint(m), false, false); m = fmaxf(__uint_as_float(rr[0]), __uint_as_float(rr[1])); }
            f32x16 cm;
#pragma unroll
            for (int r = 0; r < 16; ++r) cm[r] = -m;
            PIN(cm);
            asm volatile("s_nop 1"); sA = mfma_pin(k0, qf, cm); sB = mfma_pin(k8, qf, cm); sC = mfma_pin(k1, qf, cm); SB();
            bf16x8 va0, va1, vb0, vb1;
            { const unsigned a0 = vaddr(0), a8 = vaddr(8); va0 = ldsf((lcp)(uintptr_t)a0); va1 = ldsf((lcp)(uintptr_t)(a0 + 1024)); vb0 = ldsf((lcp)(uintptr_t)a8); vb1 = ldsf((lcp)(uintptr_t)(a8 + 1024)); }
            k0 = rdk(2);
            unsigned mp[8];
#pragma unroll
            for (int j = 0; j < 8; ++j) mp[j] = (dmask(2 * j) ? 0xffffu : 0u) | (dmask(2 * j + 1) ? 0xffff0000u : 0u);
            SB(); asm volatile("s_nop 7"); SB();
#pragma unroll
            for (int r = 0; r < 16; ++r) sA[r] = dmask(r) ? sB[r] : sA[r];
            EXPT(sA);
            { const u32x4 p0 = pack8(sA, 0), p1 = pack8(sA, 8); u32x4 d0, d1, o0, o1;
#pragma unroll
              for (int j = 0; j < 4; ++j) { d0[j] = p0[j] & mp[j]; d1[j] = p1[j] & mp[4 + j]; o0[j] = p0[j] & ~mp[j]; o1[j] = p1[j] & ~mp[4 + j]; }
              o = mfma(va0, __builtin_bit_cast(bf16x8, o0), o); o = mfma(va1, __builtin_bit_cast(bf16x8, o1), o);
              o = mfma(vb0, __builtin_bit_cast(bf16x8, d0), o); o = mfma(vb1, __builtin_bit_cast(bf16x8, d1), o); }
            { const unsigned a1 = vaddr(1), a2 = vaddr(2); va0 = ldsf((lcp)(uintptr_t)a1); va1 = ldsf((lcp)(uintptr_t)(a1 + 1024)); vb0 = ldsf((lcp)(uintptr_t)a2); vb1 = ldsf((lcp)(uintptr_t)(a2 + 1024)); }
            SB();
#define EXP8(S, b) do { _Pragma("unroll") for (int r = (b); r < (b) + 8; ++r) S[r] = __builtin_amdgcn_exp2f(S[r]); } while (0)
#define PK4(S, b) (u32x4){pk2(S[b], S[(b) + 1]), pk2(S[(b) + 2], S[(b) + 3]), pk2(S[(b) + 4], S[(b) + 5]), pk2(S[(b) + 6], S[(b) + 7])}
#define TILE2(i, S, V0, V1, SN) do { \
                if ((i) + 1 <= 7) { SN = mfma_pin(k0, qf, cm); SB(); if ((i) + 2 <= 7) k0 = rdk((i) + 2); } \
                EXP8(S, 0); { u32x4 p_ = PK4(S, 0); PIN(p_); SB(); o = mfma(V0, __builtin_bit_cast(bf16x8, p_), o); SB(); } \
                EXP8(S, 8); { u32x4 p_ = PK4(S, 8); PIN(p_); SB(); o = mfma(V1, __builtin_bit_cast(bf16x8, p_), o); SB(); } \
                if ((i) + 2 <= 7) { const unsigned a_ = vaddr((i) + 2); V0 = ldsf((lcp)(uintptr_t)a_); V1 = ldsf((lcp)(uintptr_t)(a_ + 1024)); } SB(); } while (0)
            TILE2(1, sC, va0, va1, sB); TILE2(2, sB, vb0, vb1, sC);
            H_FLAGS();
            TILE2(3, sC, va0, va1, sB); TILE2(4, sB, vb0, vb1, sC);
            WAITV(0); asm volatile("" : "+v"(hfl), "+v"(pfw));
            hrdy = __all(hfl == tag);
            if (hrdy) { LDH(0); LDH(1); LDH(2); LDH(3); LDH(4); LDH(5); LDH(6); }
            TILE2(5, sC, va0, va1, sB);
            if (hrdy) { LDH(7); LDH(8); LDH(9); LDH(10); LDH(11); LDH(12); LDH(13); }
            TILE2(6, sB, vb0, vb1, sC);
            if (hrdy) { LDH(14); LDH(15); LDH(16); LDH(17); LDH(18); LDH(19); LDH(20); LDH(21); }
            TILE2(7, sC, va0, va1, sB);
        }
        const float inv = __builtin_amdgcn_rcpf(o[8]);
        f32x16 y;
#pragma unroll
        for (int r = 0; r < 8; ++r) y[r] = o[r] * inv;
        const unsigned oo = (unsigned)(WS_C + ((size_t)T * NKS + 86 + 2 * c.cg + c.g) * 1024) + lane * 16;
        const u32x4 ov = pack8(y, 0);
        __builtin_amdgcn_raw_buffer_store_b128(ov, wsr, oo, 0, 16);
        if (!hrdy) {
            poll_flags((const u64*)(ws + WS_HF) + T2 * 32, 32, tag, lane, ws);
#pragma unroll
            for (int i = 0; i < 22; ++i) LDH(i);
        }
        hrdy_ = hrdy;
        __builtin_amdgcn_s_waitcnt(0x0F71);
        __builtin_amdgcn_s_setprio(0);
    }
    {
        const int lane = opaque(lane2), h = lane >> 5, r32 = lane & 31, q4 = (lane & 15) >> 2, p4 = lane & 3, b16 = (lane >> 4) & 1;
        f32x16 acc0 = f32x16{}, acc1 = f32x16{};
        const unsigned wl = c.lds0 + kq * 2048 + (4 * h + q4) * 128 + 32 * b16 + 8 * p4, swz = ((q4 >> 1) & 1) << 6;
        const unsigned wl0 = wl + (0u ^ swz), wl1 = wl + (64u ^ swz);
        bf16x8 wa[3], wb_[3];
#define RDW(u) do { wa[(u) % 3] = trfrag<1024>((lcp)(uintptr_t)(wl0 + dloc(u))); wb_[(u) % 3] = trfrag<1024>((lcp)(uintptr_t)(wl1 + dloc(u))); } while (0)
#define G2STEP(u, frag, nxt) do { acc0 = mfma(wa[(u) % 3], frag, acc0); acc1 = mfma(wb_[(u) % 3], frag, acc1); SB(); if ((nxt) >= 0) RDW((nxt) < 0 ? 0 : (nxt)); SB(); } while (0)
        SYNC(63);
        const bool ew = blockIdx.x == 0 && w == 7; u32x4 sf0 = {0u, 0u, 0u, 0u}, sf1 = sf0; bool edone = false;
        if (ew) { aload16_sc1(sf0, P.ws + WS_SF, (unsigned)(lane * 32)); aload16_sc1(sf1, P.ws + WS_SF + 16, (unsigned)(lane * 32)); }
        RDW(0); RDW(1); RDW(2);
        G2STEP(0, hf[0], 3);
        issue_d(c, 13); G2STEP(1, hf[1], 4); issue_d(c, 14); G2STEP(2, hf[2], 5); issue_d(c, 15); G2STEP(3, hf[3], 6); issue_d(c, 16); G2STEP(4, hf[4], 7); issue_d(c, 17); G2STEP(5, hf[5], 8); issue_d(c, 18);
        if (hrdy_) { WAITV(6); }
        if (lane == 0) store_flag(ws, WS_OF + ((size_t)T * 32 + 2 * c.cg + c.g) * 8, tag);
        SYNCV(63);
        G2STEP(6, hf[6], 9); issue_d(c, 19); G2STEP(7, hf[7], 10); issue_d(c, 20); G2STEP(8, hf[8], 11); issue_d(c, 21); G2STEP(9, hf[9], 12); issue_d(c, 22);
        G2STEP(10, hf[10], -1); issue_d(c, 23); G2STEP(11, hf[11], -1); issue_d(c, 24); G2STEP(12, hf[12], -1);
        SYNCV(7);
        if (ew) { asm volatile("" : "+v"(sf0), "+v"(sf1));
            edone = __all(sf0[0] == tagLo && sf0[1] == tagHi && sf0[2] == tagLo && sf0[3] == tagHi && sf1[0] == tagLo && sf1[1] == tagHi && sf1[2] == tagLo && sf1[3] == tagHi);
            if (edone && lane == 0) store_flag(ws, WS_EPOCH, tag); }
        edone_ = edone;
        RDW(13); RDW(14); RDW(15);
        u32x4 xres;
        u64 ofl;
        issue_d(c, 25); G2STEP(13, hf[13], 16); issue_d(c, 26); G2STEP(14, hf[14], 17); issue_d(c, 27); G2STEP(15, hf[15], 18); issue_d(c, 28);
        aload_flag(ofl, P.ws, (unsigned)(WS_OF + (T2 * 32 + (lane & 31)) * 8));
        G2STEP(16, hf[16], -1); issue_d(c, 29);
        xres = *(const u32x4*)(P.x + (size_t)(64 * c.tb + 8 * w + (lane >> 3)) * 512 + 64 * c.nb + (lane & 7) * 8);
        G2STEP(17, hf[17], -1); G2STEP(18, hf[18], -1);
        WAITV(2); asm volatile("" : "+v"(ofl));
        if (!__all(ofl == tag)) poll_flags((const u64*)(ws + WS_OF) + T2 * 32, 32, tag, lane, ws);
        bf16x8 of[8];
#pragma unroll
        for (int i = 0; i < 8; ++i) of[i] = __builtin_bit_cast(bf16x8, __builtin_amdgcn_raw_buffer_load_b128(wsr, cbase + (86 + 4 * i) * 1024, 0, 16));
        SYNCV(63);
        RDW(19); RDW(20); RDW(21);
        G2STEP(19, hf[19], 22); G2STEP(20, hf[20], 23);
        if (kq < 2) { acc0 = mfma(wa[21 % 3], hf[21], acc0); acc1 = mfma(wb_[21 % 3], hf[21], acc1); }
        SB(); RDW(24); SB();
        G2STEP(22, of[0], -1); G2STEP(23, of[1], -1); G2STEP(24, of[2], -1);
        SYNCV(63);
        RDW(25); RDW(26); RDW(27);
        G2STEP(25, of[3], 28); G2STEP(26, of[4], 29); G2STEP(27, of[5], -1); G2STEP(28, of[6], -1); G2STEP(29, of[7], -1);
        { lp rbp = c.lds + (kq < 2 ? 4 * GR : 13 * GR - 32768) + (kq * 2 + th) * 8192;
#pragma unroll
          for (int q = 0; q < 4; ++q) { *(LAS f32x4*)(rbp + q * 1024 + ((lane + 2 * q) & 63) * 16) = F4(acc0, 4 * q); *(LAS f32x4*)(rbp + 4096 + q * 1024 + ((lane + 2 * (4 + q)) & 63) * 16) = F4(acc1, 4 * q); } }
        SYNC(63);
        {
          const int t = 8 * w + (lane >> 3), cc = lane & 7;
          const unsigned a0 = (unsigned)((t >> 5) * 8192 + cc * 1024), s0i = (unsigned)(((t & 31) + 2 * cc) & 63) * 16, s1i = (unsigned)(((t & 31) + 32 + 2 * cc) & 63) * 16;
          f32x4 s0 = {0.f, 0.f, 0.f, 0.f}, s1 = s0;
#pragma unroll
          for (int k = 0; k < 4; ++k) { const lcp bp = (lcp)c.lds + (k < 2 ? 4 * GR : 13 * GR - 32768) + k * 16384 + a0; s0 += ldsf4(bp + s0i); s1 += ldsf4(bp + s1i); }
          u32x4 ov;
          ov[0] = pk2(s0[0] + blo(xres[0]), s0[1] + bhi(xres[0])); ov[1] = pk2(s0[2] + blo(xres[1]), s0[3] + bhi(xres[1]));
          ov[2] = pk2(s1[0] + blo(xres[2]), s1[1] + bhi(xres[2])); ov[3] = pk2(s1[2] + blo(xres[3]), s1[3] + bhi(xres[3]));
          *(u32x4*)(P.out + (size_t)(64 * c.tb + t) * 512 + 64 * c.nb + cc * 8) = ov; }
    }
    if (blockIdx.x == 0 && w == 7 && !edone_) {
        const GAS u64* pp = (const GAS u64*)(ws + WS_SF) + lane * 4;
        for (unsigned spins = 0;; ++spins) { bool ok = true;
#pragma unroll
            for (int j = 0; j < 4; ++j) ok &= __hip_atomic_load(pp + j, __ATOMIC_RELAXED, __HIP_MEMORY_SCOPE_AGENT) == tag;
            if (__all(ok)) break; if (spins > SPIN_CAP) { set_timeout(ws, lane); break; } __builtin_amdgcn_s_sleep(2); }
        if (lane == 0) store_flag(ws, WS_EPOCH, tag);
    }
}

extern "C" void kernel_launch(void* const* d_in, const int* in_sizes, int n_in, void* d_out, int out_size, void* d_ws, size_t ws_size, hipStream_t stream) {
    Params p;
    p.x = (const u16*)d_in[0]; p.wqkv = (const u16*)d_in[1]; p.wo = (const u16*)d_in[2];
    p.wg = (const u16*)d_in[4]; p.wu = (const u16*)d_in[5]; p.wd = (const u16*)d_in[6];
    p.out = (u16*)d_out; p.ws = (char*)d_ws;
    (void)hipFuncSetAttribute((const void*)fused_kernel, hipFuncAttributeMaxDynamicSharedMemorySize, LDS_BYTES);
    fused_kernel<<<256, 512, LDS_BYTES, stream>>>(p);
}
```
